```python
import torch

def _softmax(x, axis=-1):
    return torch.softmax(x, dim=axis)
def _gelu(x):
    return torch.nn.functional.gelu(x, approximate="tanh")
def _silu(x):
    return torch.nn.functional.silu(x)
def _sigmoid(x):
    return torch.sigmoid(x)
def _layernorm(x, axis=-1, eps=1e-5):
    m = x.mean(dim=axis, keepdim=True)
    v = x.var(dim=axis, keepdim=True, unbiased=False)
    return (x - m) / torch.sqrt(v + eps)
def _scatter_add(tgt, idx, src, axis=0):
    return tgt.clone().index_add_(axis, idx, src)
def _segment_sum(src, seg, n):
    out = torch.zeros((n,) + src.shape[1:], dtype=src.dtype)
    return out.index_add_(0, seg, src)
def _pad(x, pw):
    flat = tuple(v for pair in reversed(pw) for v in pair)
    return torch.nn.functional.pad(x, flat)

kernel_name = 'standalone__gqa_block_cached__near_ridge'

def setup_inputs():

    _w = (lambda a: (lambda t: t.to(torch.bfloat16) if t.is_floating_point() else t)(torch.as_tensor(a)))
    B, Sq, D = 16, 224, 512
    Hq, Hkv, Sk, Dh = 32, 32, 512, 128
    assert Hq % Hkv == 0, (Hq, Hkv)
    return {
        "x": _w((lambda *s: torch.randn(*s, dtype=torch.bfloat16) * 0.8)(B, Sq, D)),
        "k_cache": _w((lambda *s: torch.randn(*s, dtype=torch.bfloat16) * 0.8)(B, Hkv, Sk, Dh)),
        "v_cache": _w((lambda *s: torch.randn(*s, dtype=torch.bfloat16) * 0.05)(B, Hkv, Sk, Dh)),
        "wq": _w((lambda *s: torch.randn(*s, dtype=torch.bfloat16) * 0.8)(D, Hq * Dh) * (D ** -0.5)),
        "wo": _w((lambda *s: torch.randn(*s, dtype=torch.bfloat16) * 0.05)(Hq * Dh, D) * ((Hq * Dh) ** -0.5)),
    }

def reference(x, k_cache, v_cache, wq, wo):
    B, Sq, D = x.shape
    Hq, Dh = 32, 128
    Hkv = k_cache.shape[1]
    rep = Hq // Hkv
    q = (lambda x, *d: x.permute(*d))((x @ wq).reshape(B, Sq, Hq, Dh), 0, 2, 1, 3)
    k = (lambda x, n, axis: torch.repeat_interleave(x, n, dim=axis))(k_cache, rep, axis=1)
    v = (lambda x, n, axis: torch.repeat_interleave(x, n, dim=axis))(v_cache, rep, axis=1)
    scores = torch.einsum("bhqd,bhkd->bhqk", q, k) * (Dh ** -0.5)
    p = _softmax(scores, axis=-1)
    o = torch.einsum("bhqk,bhkd->bhqd", p, v)
    return (lambda x, *d: x.permute(*d))(o, 0, 2, 1, 3).reshape(B, Sq, Hq * Dh) @ wo
```

```cpp
#include <hip/hip_runtime.h>
#include <hip/hip_bf16.h>
typedef unsigned short u16;
namespace naive {
constexpr int B=16,SQ=224,DM=512,H=32,SK=512,DH=128;
__device__ __forceinline__ float bf2f(u16 v){ return __uint_as_float(((unsigned)v)<<16); }
__device__ __forceinline__ u16 f2bf(float f){ unsigned u=__float_as_uint(f); u += 0x7fffu + ((u>>16)&1u); return (u16)(u>>16); }
__global__ void gemm(const u16* A,const u16* W,u16* C,int M,int N,int K){
  int n = blockIdx.x*blockDim.x+threadIdx.x; int m = blockIdx.y;
  if(n>=N) return;
  float acc=0.f;
  for(int k=0;k<K;++k) acc += bf2f(A[(size_t)m*K+k])*bf2f(W[(size_t)k*N+n]);
  C[(size_t)m*N+n]=f2bf(acc);
}
__global__ void attn(const u16* Q,const u16* Kc,const u16* Vc,u16* O){
  __shared__ float q[DH]; __shared__ float s[SK]; __shared__ float red[128];
  int row=blockIdx.x, h=blockIdx.y, b=blockIdx.z, t=threadIdx.x;
  q[t]=bf2f(Q[((size_t)(b*SQ+row))*(H*DH)+h*DH+t]);
  __syncthreads();
  const u16* Kb=Kc+((size_t)(b*H+h))*SK*DH; const u16* Vb=Vc+((size_t)(b*H+h))*SK*DH;
  float mx=-1e30f;
  for(int k=t;k<SK;k+=128){ float a=0; for(int d=0;d<DH;++d) a+=q[d]*bf2f(Kb[(size_t)k*DH+d]); a*=0.08838834764831845f; s[k]=a; mx=fmaxf(mx,a);}
  red[t]=mx; __syncthreads();
  for(int o=64;o>0;o>>=1){ if(t<o) red[t]=fmaxf(red[t],red[t+o]); __syncthreads(); }
  mx=red[0]; __syncthreads();
  float sum=0; for(int k=t;k<SK;k+=128){ float e=__expf(s[k]-mx); s[k]=e; sum+=e; }
  red[t]=sum; __syncthreads();
  for(int o=64;o>0;o>>=1){ if(t<o) red[t]+=red[t+o]; __syncthreads(); }
  sum=red[0];
  float a=0; for(int k=0;k<SK;++k) a+=s[k]*bf2f(Vb[(size_t)k*DH+t]);
  O[((size_t)(b*SQ+row))*(H*DH)+h*DH+t]=f2bf(a/sum);
}
inline void run(void* const* d_in, void* d_out, void* d_ws, hipStream_t stream){
  const u16* x=(const u16*)d_in[0]; const u16* kc=(const u16*)d_in[1]; const u16* vc=(const u16*)d_in[2];
  const u16* wq=(const u16*)d_in[3]; const u16* wo=(const u16*)d_in[4];
  u16* q=(u16*)d_ws; u16* o=q+(size_t)B*SQ*H*DH;
  gemm<<<dim3(H*DH/256,B*SQ),256,0,stream>>>(x,wq,q,B*SQ,H*DH,DM);
  attn<<<dim3(SQ,H,B),128,0,stream>>>(q,kc,vc,o);
  gemm<<<dim3(DM/256,B*SQ),256,0,stream>>>(o,wo,(u16*)d_out,B*SQ,DM,H*DH);
}
}
extern "C" void kernel_launch(void* const* d_in, const int* in_sizes, int n_in, void* d_out, int out_size, void* d_ws, size_t ws_size, hipStream_t stream){
  naive::run(d_in,d_out,d_ws,stream);
}
```

```cpp
#include <hip/hip_runtime.h>

typedef unsigned short u16;
typedef unsigned u32x4 __attribute__((ext_vector_type(4)));
static __device__ __forceinline__ float bf_lo(unsigned v){ return __uint_as_float(v<<16); }
static __device__ __forceinline__ float bf_hi(unsigned v){ return __uint_as_float(v&0xffff0000u); }
static __device__ __forceinline__ unsigned bf_rn(float f){ unsigned u=__float_as_uint(f); return (f!=f)?0x7fc0u:((u+0x7fffu+((u>>16)&1u))>>16); }
static __device__ __forceinline__ float bf_rt(float f){ return __uint_as_float(bf_rn(f)<<16); }
static __device__ __forceinline__ void wsync(){ asm volatile("s_waitcnt lgkmcnt(0)":::"memory"); __builtin_amdgcn_wave_barrier(); }
static __device__ __forceinline__ void exact_tile(const char* x,const char* kc,const char* vc,const char* wq,const char* wo,char* out,float* lds,unsigned b,unsigned row0,unsigned col0,unsigned lane){
  float* xs=lds; float* ps=lds+4096; float* qs=lds+8192;
  for(unsigned rb=0;rb<2;++rb){
    const unsigned* xg=(const unsigned*)(x+(size_t)(row0+rb*8)*1024);
    wsync();
    for(unsigned i=lane;i<2048;i+=64){ unsigned v=xg[i]; xs[2*i]=bf_lo(v); xs[2*i+1]=bf_hi(v); }
    float acc[8][2];
    #pragma unroll
    for(int r=0;r<8;++r){ acc[r][0]=0.f; acc[r][1]=0.f; }
    for(unsigned h=0;h<32;++h){
      float a[8][2];
      #pragma unroll
      for(int r=0;r<8;++r){ a[r][0]=0.f; a[r][1]=0.f; }
      wsync();
      const unsigned* wg=(const unsigned*)(wq+h*256)+lane;
      for(unsigned k=0;k<512;++k){ unsigned v=wg[k*2048]; float w0=bf_lo(v),w1=bf_hi(v);
        #pragma unroll
        for(int r=0;r<8;++r){ float xv=xs[r*512+k]; a[r][0]+=xv*w0; a[r][1]+=xv*w1; } }
      #pragma unroll
      for(int r=0;r<8;++r){ qs[r*128+2*lane]=bf_rt(a[r][0]); qs[r*128+2*lane+1]=bf_rt(a[r][1]); }
      wsync();
      float mx[8];
      #pragma unroll
      for(int r=0;r<8;++r) mx[r]=-__builtin_inff();
      const char* kg=kc+(size_t)(b*32+h)*131072;
      for(unsigned jj=0;jj<8;++jj){
        float s[8];
        #pragma unroll
        for(int r=0;r<8;++r) s[r]=0.f;
        const u32x4* kr=(const u32x4*)(kg+(jj*64+lane)*256);
        for(unsigned d8=0;d8<16;++d8){ u32x4 v=kr[d8];
          #pragma unroll
          for(int r=0;r<8;++r){ const float* qq=qs+r*128+d8*8;
            s[r]+=qq[0]*bf_lo(v.x)+qq[1]*bf_hi(v.x)+qq[2]*bf_lo(v.y)+qq[3]*bf_hi(v.y)+qq[4]*bf_lo(v.z)+qq[5]*bf_hi(v.z)+qq[6]*bf_lo(v.w)+qq[7]*bf_hi(v.w); } }
        #pragma unroll
        for(int r=0;r<8;++r){ float t=s[r]*0.08838834764831845f; ps[r*512+jj*64+lane]=t; mx[r]=(t!=t)?t:fmaxf(mx[r],t); }
      }
      float sm[8];
      #pragma unroll
      for(int r=0;r<8;++r){
        for(int o=32;o>0;o>>=1) mx[r]=fmaxf(mx[r],__shfl_xor(mx[r],o));
        sm[r]=0.f; }
      for(unsigned jj=0;jj<8;++jj){
        #pragma unroll
        for(int r=0;r<8;++r){ float e=exp2f((ps[r*512+jj*64+lane]-mx[r])*1.4426950408889634f); ps[r*512+jj*64+lane]=e; sm[r]+=e; } }
      #pragma unroll
      for(int r=0;r<8;++r){ for(int o=32;o>0;o>>=1) sm[r]+=__shfl_xor(sm[r],o); a[r][0]=0.f; a[r][1]=0.f; }
      wsync();
      const unsigned* vg=(const unsigned*)(vc+(size_t)(b*32+h)*131072)+lane;
      for(unsigned j=0;j<512;++j){ unsigned v=vg[j*64]; float w0=bf_lo(v),w1=bf_hi(v);
        #pragma unroll
        for(int r=0;r<8;++r){ float pv=ps[r*512+j]; a[r][0]+=pv*w0; a[r][1]+=pv*w1; } }
      #pragma unroll
      for(int r=0;r<8;++r){ qs[r*128+2*lane]=bf_rt(a[r][0]/sm[r]); qs[r*128+2*lane+1]=bf_rt(a[r][1]/sm[r]); }
      wsync();
      const unsigned* og=(const unsigned*)(wo+(size_t)h*131072+col0*2)+lane;
      for(unsigned d=0;d<128;++d){ unsigned v=og[d*256]; float w0=bf_lo(v),w1=bf_hi(v);
        #pragma unroll
        for(int r=0;r<8;++r){ float ov=qs[r*128+d]; acc[r][0]+=ov*w0; acc[r][1]+=ov*w1; } }
    }
    #pragma unroll
    for(int r=0;r<8;++r) ((unsigned*)(out+(size_t)(row0+rb*8+r)*1024+col0*2))[lane]=bf_rn(acc[r][0])|(bf_rn(acc[r][1])<<16);
  }
}

__global__ __launch_bounds__(256,1) void gqa_fused(const char* __restrict__ x, const char* __restrict__ kc, const char* __restrict__ vc, const char* __restrict__ wq, const char* __restrict__ wo, char* __restrict__ out, char* __restrict__ ws) {
  __shared__ __attribute__((aligned(1024))) char smem[163840];
  unsigned tid=threadIdx.x, bid=blockIdx.x, w;
  const __attribute__((address_space(4))) char* ka=(const __attribute__((address_space(4))) char*)__builtin_amdgcn_kernarg_segment_ptr();
  asm volatile(
"s_load_dwordx2 s[12:13], %[ka], 24\n"
"s_load_dwordx2 s[8:9], %[ka], 0\n"
"s_load_dwordx4 s[40:43], %[ka], 8\n"
"s_load_dwordx2 s[24:25], %[ka], 32\n"
"s_load_dwordx2 s[28:29], %[ka], 40\n"
"s_load_dwordx2 s[20:21], %[ka], 48\n"
"v_lshrrev_b32 v16, 6, %[tid]\n"
"v_lshlrev_b32 v1, 4, %[tid]\n"
"v_lshlrev_b32 v17, 1, %[tid]\n"
"v_readfirstlane_b32 s82, v16\n"
"v_and_b32 v1, 1008, v1\n"
"v_and_b32 v17, 96, v17\n"
"v_and_b32 v16, 48, v1\n"
"v_bfe_u32 v13, %[tid], 2, 3\n"
"v_xor_b32 v2, v16, v17\n"
"v_and_or_b32 v3, v17, 64, v16\n"
"v_lshl_or_b32 v2, v13, 10, v2\n"
"v_lshl_or_b32 v3, v13, 13, v3\n"
"s_bfe_u32 s84, %[bid], 131072\n"
"s_lshl_b32 s84, s84, 2\n"
"s_bfe_u32 s86, %[bid], 131075\n"
"s_or_b32 s84, s84, s86\n"
"s_bfe_u32 s83, %[bid], 65538\n"
"s_lshl_b32 s83, s83, 3\n"
"s_bfe_u32 s87, %[bid], 196613\n"
"s_or_b32 s83, s83, s87\n"
"s_lshr_b32 s85, s82, 1\n"
"s_and_b32 s47, s82, 1\n"
"s_mov_b32 s65, 0\n"
"s_lshl_b32 s45, s82, 12\n"
"s_or_b32 s45, s45, 65536\n"
"s_lshl1_add_u32 s88, s84, s85\n"
"s_lshl_b32 s87, s88, 8\n"
"s_lshl_b32 s89, s47, 18\n"
"s_or_b32 s78, s87, s89\n"
"s_mov_b32 s14, 3924224\n"
"s_mov_b32 s15, 131072\n"
"s_lshl_b32 s46, s82, 13\n"
"s_lshl_b32 s89, s83, 23\n"
"s_or_b32 s89, s89, 6282240\n"
"s_mul_i32 s44, s82, 7168\n"
"s_lshl_b32 s66, s44, 3\n"
"s_add_u32 s44, s44, 32768\n"
"s_mul_i32 s79, s83, 229376\n"
"s_mov_b32 s10, 229376\n"
"s_mov_b32 s11, 131072\n"
"s_mul_i32 s87, s85, 229376\n"
"s_add_u32 s87, s87, 106496\n"
"s_sub_u32 s87, s87, s66\n"
"s_mov_b32 s36, 8192\n"
"s_mov_b32 s37, -49024\n"
"s_and_b32 s86, s84, 2\n"
"s_cselect_b32 s66, s66, s87\n"
"s_cselect_b32 s36, s36, -8192\n"
"s_cselect_b32 s37, s37, 49280\n"
"s_and_b32 s86, %[bid], 7\n"
"s_lshl_b32 s86, s86, 2\n"
"s_lshr_b32 s86, 828662833, s86\n"
"s_and_b32 s86, s86, 7\n"
"s_lshl_b32 s37, s86, 7\n"
"s_waitcnt lgkmcnt(0)\n"
"s_add_u32 s12, s12, s78\n"
"s_addc_u32 s13, s13, 0\n"
"s_xor_b32 m0, s45, 0\n"
"s_xor_b32 s75, s65, 0\n"
"buffer_load_dwordx4 v3, s[12:15], s75 offen lds\n"
"s_xor_b32 m0, s45, 1024\n"
"s_xor_b32 s74, s65, 65536\n"
"buffer_load_dwordx4 v3, s[12:15], s74 offen lds\n"
"s_xor_b32 m0, s45, 2048\n"
"s_xor_b32 s75, s65, 131072\n"
"buffer_load_dwordx4 v3, s[12:15], s75 offen lds\n"
"s_xor_b32 m0, s45, 3072\n"
"s_xor_b32 s74, s65, 196608\n"
"buffer_load_dwordx4 v3, s[12:15], s74 offen lds\n"
"s_add_u32 s8, s8, s79\n"
"s_addc_u32 s9, s9, 0\n"
"s_add_u32 m0, s44, 0\n"
"s_nop 0\n"
"buffer_load_dwordx4 v2, s[8:11], s66 offen lds\n"
"s_add_u32 m0, s44, 1024\n"
"s_add_u32 s66, s66, s36\n"
"buffer_load_dwordx4 v2, s[8:11], s66 offen lds\n"
"s_add_u32 m0, s44, 2048\n"
"s_add_u32 s66, s66, s36\n"
"buffer_load_dwordx4 v2, s[8:11], s66 offen lds\n"
"s_add_u32 m0, s44, 3072\n"
"s_add_u32 s66, s66, s36\n"
"buffer_load_dwordx4 v2, s[8:11], s66 offen lds\n"
"s_add_u32 m0, s44, 4096\n"
"s_add_u32 s66, s66, s36\n"
"buffer_load_dwordx4 v2, s[8:11], s66 offen lds\n"
"s_add_u32 m0, s44, 5120\n"
"s_add_u32 s66, s66, s36\n"
"buffer_load_dwordx4 v2, s[8:11], s66 offen lds\n"
"s_add_u32 m0, s44, 6144\n"
"s_add_u32 s66, s66, s36\n"
"buffer_load_dwordx4 v2, s[8:11], s66 offen lds\n"
"s_add_u32 s20, s20, 2097151\n"
"s_addc_u32 s21, s21, 0\n"
"s_and_b32 s20, s20, 4292870144\n"
"s_load_dwordx2 s[68:69], s[20:21], s89\n"
"s_xor_b32 m0, s45, 16384\n"
"s_xor_b32 s75, s65, 128\n"
"buffer_load_dwordx4 v3, s[12:15], s75 offen lds\n"
"s_xor_b32 m0, s45, 17408\n"
"s_xor_b32 s74, s65, 65664\n"
"buffer_load_dwordx4 v3, s[12:15], s74 offen lds\n"
"s_xor_b32 m0, s45, 18432\n"
"s_xor_b32 s75, s65, 131200\n"
"buffer_load_dwordx4 v3, s[12:15], s75 offen lds\n"
"s_xor_b32 m0, s45, 19456\n"
"s_xor_b32 s74, s65, 196736\n"
"buffer_load_dwordx4 v3, s[12:15], s74 offen lds\n"
"s_lshr_b32 s86, %[bid], 3\n"
"s_lshl2_add_u32 s86, s86, s82\n"
"s_getpc_b64 s[16:17]\n"
"31:\n"
"s_mov_b32 s18, 30f-31b\n"
"s_mov_b32 s19, 131072\n"
"s_lshl_b32 s67, s86, 7\n"
"v_lshlrev_b32 v21, 10, v1\n"
"buffer_load_dword v31, v21, s[16:19], s67 offen\n"
"s_lshl_b32 s49, s86, 13\n"
"v_lshrrev_b32 v22, 8, v1\n"
"v_and_b32 v24, 240, v1\n"
"v_lshrrev_b32 v28, 1, v1\n"
"v_and_b32 v28, 32, v28\n"
"v_lshlrev_b32 v13, 4, v22\n"
"v_xor_b32 v28, v28, v13\n"
"s_mul_i32 s86, s47, 14336\n"
"s_add_u32 s86, s86, 32768\n"
"v_and_b32 v6, 112, v1\n"
"v_lshl_or_b32 v6, v6, 2, v28\n"
"v_and_b32 v8, 128, v1\n"
"v_lshl_or_b32 v6, v8, 3, v6\n"
"v_add_u32 v6, s86, v6\n"
"s_lshl_b32 s86, s85, 13\n"
"s_or_b32 s86, s86, 65536\n"
"v_lshl_or_b32 v7, v22, 10, v24\n"
"v_or_b32 v7, s86, v7\n"
"s_add_u32 m0, s44, 71680\n"
"s_xor_b32 s66, s66, 128\n"
"buffer_load_dwordx4 v2, s[8:11], s66 offen lds\n"
"s_add_u32 m0, s44, 70656\n"
"s_sub_u32 s66, s66, s36\n"
"buffer_load_dwordx4 v2, s[8:11], s66 offen lds\n"
"s_add_u32 m0, s44, 69632\n"
"s_sub_u32 s66, s66, s36\n"
"buffer_load_dwordx4 v2, s[8:11], s66 offen lds\n"
"s_add_u32 m0, s44, 68608\n"
"s_sub_u32 s66, s66, s36\n"
"buffer_load_dwordx4 v2, s[8:11], s66 offen lds\n"
"s_waitcnt vmcnt(9) lgkmcnt(0)\n"
"s_barrier\n"
"ds_read_b64_tr_b16 a[112:113], v7 offset:0\n"
"ds_read_b128 a[0:3], v6 offset:0\n"
"ds_read_b64_tr_b16 a[114:115], v7 offset:256\n"
"ds_read_b128 a[28:31], v6 offset:512\n"
"ds_read_b64_tr_b16 a[116:117], v7 offset:4096\n"
"ds_read_b128 a[4:7], v6 offset:2048\n"
"s_sub_u32 s66, s66, s36\n"
"ds_read_b64_tr_b16 a[118:119], v7 offset:4352\n"
"s_add_u32 m0, s44, 67584\n"
"ds_read_b128 a[32:35], v6 offset:2560\n"
"buffer_load_dwordx4 v2, s[8:11], s66 offen lds\n"
"s_sub_u32 s66, s66, s36\n"
"s_add_u32 m0, s44, 66560\n"
"ds_read_b128 a[8:11], v6 offset:4096\n"
"buffer_load_dwordx4 v2, s[8:11], s66 offen lds\n"
"s_sub_u32 s66, s66, s36\n"
"s_add_u32 m0, s44, 65536\n"
"ds_read_b128 a[36:39], v6 offset:4608\n"
"buffer_load_dwordx4 v2, s[8:11], s66 offen lds\n"
"ds_read_b64_tr_b16 a[120:121], v7 offset:8\n"
"ds_read_b128 a[12:15], v6 offset:6144\n"
"ds_read_b64_tr_b16 a[122:123], v7 offset:264\n"
"ds_read_b128 a[40:43], v6 offset:6656\n"
"ds_read_b64_tr_b16 a[124:125], v7 offset:4104\n"
"ds_read_b128 a[16:19], v6 offset:8192\n"
"s_xor_b32 s75, s65, 524288\n"
"ds_read_b64_tr_b16 a[126:127], v7 offset:4360\n"
"s_xor_b32 m0, s45, 196608\n"
"ds_read_b128 a[44:47], v6 offset:8704\n"
"buffer_load_dwordx4 v3, s[12:15], s75 offen lds\n"
"s_xor_b32 s74, s65, 589824\n"
"s_xor_b32 m0, s45, 197632\n"
"ds_read_b128 a[20:23], v6 offset:10240\n"
"buffer_load_dwordx4 v3, s[12:15], s74 offen lds\n"
"s_xor_b32 s75, s65, 655360\n"
"s_xor_b32 m0, s45, 198656\n"
"ds_read_b128 a[48:51], v6 offset:10752\n"
"buffer_load_dwordx4 v3, s[12:15], s75 offen lds\n"
"ds_read_b64_tr_b16 a[128:129], v7 offset:512\n"
"ds_read_b128 a[24:27], v6 offset:12288\n"
"ds_read_b64_tr_b16 a[130:131], v7 offset:768\n"
"s_xor_b32 s74, s65, 720896\n"
"ds_read_b128 a[52:55], v6 offset:12800\n"
"s_xor_b32 m0, s45, 199680\n"
"ds_read_b64_tr_b16 a[132:133], v7 offset:4608\n"
"buffer_load_dwordx4 v3, s[12:15], s74 offen lds\n"
"s_xor_b32 s75, s65, 524416\n"
"s_xor_b32 m0, s45, 212992\n"
"ds_read_b64_tr_b16 a[134:135], v7 offset:4864\n"
"buffer_load_dwordx4 v3, s[12:15], s75 offen lds\n"
"s_xor_b32 s74, s65, 589952\n"
"s_xor_b32 m0, s45, 214016\n"
"s_nop 0\n"
"buffer_load_dwordx4 v3, s[12:15], s74 offen lds\n"
"s_waitcnt lgkmcnt(15)\n"
"v_mfma_f32_16x16x32_bf16 v[32:35], a[112:115], a[0:3], 0\n"
"s_waitcnt lgkmcnt(15)\n"
"s_xor_b32 s75, s65, 655488\n"
"v_mfma_f32_16x16x32_bf16 v[32:35], a[116:119], a[28:31], v[32:35]\n"
"s_waitcnt lgkmcnt(15)\n"
"s_xor_b32 m0, s45, 215040\n"
"ds_read_b64_tr_b16 a[136:137], v7 offset:520\n"
"v_mfma_f32_16x16x32_bf16 v[40:43], a[116:119], a[32:35], 0\n"
"buffer_load_dwordx4 v3, s[12:15], s75 offen lds\n"
"v_lshrrev_b32 v23, 9, v1\n"
"v_mfma_f32_16x16x32_bf16 v[40:43], a[112:115], a[4:7], v[40:43]\n"
"s_waitcnt lgkmcnt(15)\n"
"s_cmp_lg_u32 s47, 0\n"
"s_cselect_b32 s16, s42, s40\n"
"s_cselect_b32 s17, s43, s41\n"
"ds_read_b64_tr_b16 a[138:139], v7 offset:776\n"
"v_mfma_f32_16x16x32_bf16 v[48:51], a[112:115], a[8:11], 0\n"
"s_waitcnt lgkmcnt(15)\n"
"s_xor_b32 s74, s65, 721024\n"
"v_mul_u32_u24 v23, 8064, v23\n"
"v_mfma_f32_16x16x32_bf16 v[48:51], a[116:119], a[36:39], v[48:51]\n"
"s_waitcnt lgkmcnt(14)\n"
"s_xor_b32 m0, s45, 216064\n"
"ds_read_b64_tr_b16 a[140:141], v7 offset:4616\n"
"v_mfma_f32_16x16x32_bf16 v[56:59], a[116:119], a[40:43], 0\n"
"buffer_load_dwordx4 v3, s[12:15], s74 offen lds\n"
"v_lshrrev_b32 v27, 2, v1\n"
"v_mfma_f32_16x16x32_bf16 v[56:59], a[112:115], a[12:15], v[56:59]\n"
"s_waitcnt lgkmcnt(13)\n"
"s_lshl_b32 s77, s83, 5\n"
"ds_read_b64_tr_b16 a[142:143], v7 offset:4872\n"
"v_mfma_f32_16x16x32_bf16 v[64:67], a[112:115], a[16:19], 0\n"
"s_waitcnt lgkmcnt(12)\n"
"v_add_u32 v23, v23, v27\n"
"v_mfma_f32_16x16x32_bf16 v[64:67], a[116:119], a[44:47], v[64:67]\n"
"s_waitcnt lgkmcnt(10)\n"
"s_add_u32 s77, s77, s88\n"
"v_mfma_f32_16x16x32_bf16 v[72:75], a[116:119], a[48:51], 0\n"
"v_mov_b32 v27, 1\n"
"v_mfma_f32_16x16x32_bf16 v[72:75], a[112:115], a[20:23], v[72:75]\n"
"s_waitcnt lgkmcnt(8)\n"
"s_lshl_b32 s77, s77, 17\n"
"v_mfma_f32_16x16x32_bf16 v[80:83], a[112:115], a[24:27], 0\n"
"s_waitcnt lgkmcnt(6)\n"
"v_min_u32 v19, 16, v1\n"
"v_mfma_f32_16x16x32_bf16 v[80:83], a[116:119], a[52:55], v[80:83]\n"
"s_mov_b32 s72, s46\n"
"v_mfma_f32_16x16x32_bf16 v[84:87], a[124:127], a[52:55], 0\n"
"s_waitcnt vmcnt(16) lgkmcnt(0)\n"
"s_barrier\n"
"s_add_u32 s37, s37, 128\n"
"s_cmp_eq_u32 s37, 1024\n"
"s_cselect_b32 s37, 256, s37\n"
"s_lshl_b32 s65, s37, 12\n"
"s_andn2_b32 s66, s66, 896\n"
"s_or_b32 s66, s66, s37\n"
"v_lshlrev_b32 v19, 26, v19\n"
"v_mfma_f32_16x16x32_bf16 v[84:87], a[120:123], a[24:27], v[84:87]\n"
"s_add_u32 m0, s44, 0\n"
"ds_read_b64_tr_b16 a[112:113], v7 offset:16384\n"
"v_mfma_f32_16x16x32_bf16 v[76:79], a[120:123], a[20:23], 0\n"
"buffer_load_dwordx4 v2, s[8:11], s66 offen lds\n"
"v_mfma_f32_16x16x32_bf16 v[76:79], a[124:127], a[48:51], v[76:79]\n"
"s_mov_b32 s73, 32768\n"
"ds_read_b64_tr_b16 a[114:115], v7 offset:16640\n"
"v_mfma_f32_16x16x32_bf16 v[68:71], a[124:127], a[44:47], 0\n"
"s_add_u32 s16, s16, s77\n"
"s_addc_u32 s17, s17, 0\n"
"v_mfma_f32_16x16x32_bf16 v[68:71], a[120:123], a[16:19], v[68:71]\n"
"s_mov_b32 s18, 131072\n"
"ds_read_b64_tr_b16 a[116:117], v7 offset:20480\n"
"v_mfma_f32_16x16x32_bf16 v[60:63], a[120:123], a[12:15], 0\n"
"s_add_u32 s66, s66, s36\n"
"v_mfma_f32_16x16x32_bf16 v[60:63], a[124:127], a[40:43], v[60:63]\n"
"s_add_u32 m0, s44, 1024\n"
"ds_read_b64_tr_b16 a[118:119], v7 offset:20736\n"
"v_mfma_f32_16x16x32_bf16 v[52:55], a[124:127], a[36:39], 0\n"
"buffer_load_dwordx4 v2, s[8:11], s66 offen lds\n"
"v_mfma_f32_16x16x32_bf16 v[52:55], a[120:123], a[8:11], v[52:55]\n"
"s_lshr_b32 s40, %[bid], 7\n"
"v_mfma_f32_16x16x32_bf16 v[44:47], a[120:123], a[4:7], 0\n"
"s_mul_i32 s40, s40, 2\n"
"v_mfma_f32_16x16x32_bf16 v[44:47], a[124:127], a[32:35], v[44:47]\n"
"s_mul_i32 s41, s85, 1\n"
"v_mfma_f32_16x16x32_bf16 v[36:39], a[124:127], a[28:31], 0\n"
"s_add_u32 s66, s66, s36\n"
"v_mfma_f32_16x16x32_bf16 v[36:39], a[120:123], a[0:3], v[36:39]\n"
"s_add_u32 m0, s44, 2048\n"
"v_mfma_f32_16x16x32_bf16 v[88:91], a[128:131], a[0:3], 0\n"
"buffer_load_dwordx4 v2, s[8:11], s66 offen lds\n"
"v_mfma_f32_16x16x32_bf16 v[88:91], a[132:135], a[28:31], v[88:91]\n"
"s_add_u32 s40, s40, s41\n"
"ds_read_b64_tr_b16 a[120:121], v7 offset:16392\n"
"v_mfma_f32_16x16x32_bf16 v[96:99], a[132:135], a[32:35], 0\n"
"s_and_b32 s40, s40, 3\n"
"v_mfma_f32_16x16x32_bf16 v[96:99], a[128:131], a[4:7], v[96:99]\n"
"s_lshl_b32 s40, s40, 12\n"
"ds_read_b64_tr_b16 a[122:123], v7 offset:16648\n"
"v_mfma_f32_16x16x32_bf16 v[104:107], a[128:131], a[8:11], 0\n"
"s_xor_b32 s41, s40, 32768\n"
"v_mfma_f32_16x16x32_bf16 v[104:107], a[132:135], a[36:39], v[104:107]\n"
"s_bfe_u32 s86, %[bid], 131072\n"
"ds_read_b64_tr_b16 a[124:125], v7 offset:20488\n"
"v_mfma_f32_16x16x32_bf16 v[112:115], a[132:135], a[40:43], 0\n"
"s_add_u32 s66, s66, s36\n"
"v_mfma_f32_16x16x32_bf16 v[112:115], a[128:131], a[12:15], v[112:115]\n"
"s_add_u32 m0, s44, 3072\n"
"ds_read_b64_tr_b16 a[126:127], v7 offset:20744\n"
"v_mfma_f32_16x16x32_bf16 v[120:123], a[128:131], a[16:19], 0\n"
"buffer_load_dwordx4 v2, s[8:11], s66 offen lds\n"
"v_mfma_f32_16x16x32_bf16 v[120:123], a[132:135], a[44:47], v[120:123]\n"
"s_lshl_b32 s86, s86, 20\n"
"v_mfma_f32_16x16x32_bf16 v[128:131], a[132:135], a[48:51], 0\n"
"s_or_b32 s49, s49, s86\n"
"v_mfma_f32_16x16x32_bf16 v[128:131], a[128:131], a[20:23], v[128:131]\n"
"v_and_b32 v26, 192, v1\n"
"v_mfma_f32_16x16x32_bf16 v[136:139], a[128:131], a[24:27], 0\n"
"s_xor_b32 s75, s65, 0\n"
"v_mfma_f32_16x16x32_bf16 v[136:139], a[132:135], a[52:55], v[136:139]\n"
"s_xor_b32 m0, s45, 0\n"
"v_mfma_f32_16x16x32_bf16 v[140:143], a[140:143], a[52:55], 0\n"
"buffer_load_dwordx4 v3, s[12:15], s75 offen lds\n"
"v_mfma_f32_16x16x32_bf16 v[140:143], a[136:139], a[24:27], v[140:143]\n"
"ds_read_b64_tr_b16 a[128:129], v7 offset:16896\n"
"v_lshlrev_b32 v4, 2, v26\n"
"v_mfma_f32_16x16x32_bf16 v[132:135], a[136:139], a[20:23], 0\n"
"v_lshl_or_b32 v4, v22, 6, v4\n"
"v_mfma_f32_16x16x32_bf16 v[132:135], a[140:143], a[48:51], v[132:135]\n"
"ds_read_b64_tr_b16 a[130:131], v7 offset:17152\n"
"v_or_b32 v4, v4, v16\n"
"v_mfma_f32_16x16x32_bf16 v[124:127], a[140:143], a[44:47], 0\n"
"v_xor_b32 v5, 32, v4\n"
"v_mfma_f32_16x16x32_bf16 v[124:127], a[136:139], a[16:19], v[124:127]\n"
"ds_read_b64_tr_b16 a[132:133], v7 offset:20992\n"
"v_lshl_or_b32 v13, v24, 6, v13\n"
"v_mfma_f32_16x16x32_bf16 v[116:119], a[136:139], a[12:15], 0\n"
"s_xor_b32 s74, s65, 65536\n"
"v_mfma_f32_16x16x32_bf16 v[116:119], a[140:143], a[40:43], v[116:119]\n"
"s_xor_b32 m0, s45, 1024\n"
"ds_read_b64_tr_b16 a[134:135], v7 offset:21248\n"
"v_mfma_f32_16x16x32_bf16 v[108:111], a[140:143], a[36:39], 0\n"
"buffer_load_dwordx4 v3, s[12:15], s74 offen lds\n"
"v_mfma_f32_16x16x32_bf16 v[108:111], a[136:139], a[8:11], v[108:111]\n"
"s_lshl_b32 s86, s85, 14\n"
"v_mfma_f32_16x16x32_bf16 v[100:103], a[136:139], a[4:7], 0\n"
"v_lshl_or_b32 v8, v16, 2, v28\n"
"v_mfma_f32_16x16x32_bf16 v[100:103], a[140:143], a[32:35], v[100:103]\n"
"v_lshl_or_b32 v8, v26, 4, v8\n"
"v_mfma_f32_16x16x32_bf16 v[92:95], a[140:143], a[28:31], 0\n"
"s_xor_b32 s75, s65, 131072\n"
"v_mfma_f32_16x16x32_bf16 v[92:95], a[136:139], a[0:3], v[92:95]\n"
"s_waitcnt lgkmcnt(10)\n"
"s_xor_b32 m0, s45, 2048\n"
"v_mfma_f32_16x16x32_bf16 v[144:147], a[112:115], a[0:3], 0\n"
"s_waitcnt lgkmcnt(8)\n"
"buffer_load_dwordx4 v3, s[12:15], s75 offen lds\n"
"v_mfma_f32_16x16x32_bf16 v[144:147], a[116:119], a[28:31], v[144:147]\n"
"ds_read_b64_tr_b16 a[136:137], v7 offset:16904\n"
"v_or_b32 v8, s86, v8\n"
"v_mfma_f32_16x16x32_bf16 v[152:155], a[116:119], a[32:35], 0\n"
"v_and_b32 v17, 32, v17\n"
"v_mfma_f32_16x16x32_bf16 v[152:155], a[112:115], a[4:7], v[152:155]\n"
"ds_read_b64_tr_b16 a[138:139], v7 offset:17160\n"
"v_lshrrev_b32 v30, 1, v16\n"
"v_mfma_f32_16x16x32_bf16 v[160:163], a[112:115], a[8:11], 0\n"
"v_xor_b32 v30, v30, v17\n"
"v_mfma_f32_16x16x32_bf16 v[160:163], a[116:119], a[36:39], v[160:163]\n"
"ds_read_b64_tr_b16 a[140:141], v7 offset:21000\n"
"v_or_b32 v30, v30, v26\n"
"v_mfma_f32_16x16x32_bf16 v[168:171], a[116:119], a[40:43], 0\n"
"s_xor_b32 s74, s65, 196608\n"
"v_mfma_f32_16x16x32_bf16 v[168:171], a[112:115], a[12:15], v[168:171]\n"
"s_xor_b32 m0, s45, 3072\n"
"ds_read_b64_tr_b16 a[142:143], v7 offset:21256\n"
"v_mfma_f32_16x16x32_bf16 v[176:179], a[112:115], a[16:19], 0\n"
"buffer_load_dwordx4 v3, s[12:15], s74 offen lds\n"
"v_mfma_f32_16x16x32_bf16 v[176:179], a[116:119], a[44:47], v[176:179]\n"
"s_or_b32 s86, s86, 8192\n"
"v_mfma_f32_16x16x32_bf16 v[184:187], a[116:119], a[48:51], 0\n"
"v_lshl_or_b32 v9, v22, 10, v30\n"
"v_mfma_f32_16x16x32_bf16 v[184:187], a[112:115], a[20:23], v[184:187]\n"
"v_or_b32 v9, s86, v9\n"
"v_mfma_f32_16x16x32_bf16 v[192:195], a[112:115], a[24:27], 0\n"
"v_xor_b32 v6, 65536, v6\n"
"v_mfma_f32_16x16x32_bf16 v[192:195], a[116:119], a[52:55], v[192:195]\n"
"s_waitcnt lgkmcnt(8)\n"
"v_xor_b32 v7, 196608, v7\n"
"v_mfma_f32_16x16x32_bf16 v[196:199], a[124:127], a[52:55], 0\n"
"s_waitcnt vmcnt(12) lgkmcnt(0)\n"
"s_barrier\n"
"v_mfma_f32_16x16x32_bf16 v[196:199], a[120:123], a[24:27], v[196:199]\n"
"ds_read_b64_tr_b16 a[112:113], v7 offset:0\n"
"v_xor_b32 v10, 32, v9\n"
"v_mfma_f32_16x16x32_bf16 v[188:191], a[120:123], a[20:23], 0\n"
"ds_read_b128 a[56:59], v6 offset:0\n"
"v_lshl_or_b32 v11, v22, 8, v30\n"
"v_mfma_f32_16x16x32_bf16 v[188:191], a[124:127], a[48:51], v[188:191]\n"
"ds_read_b64_tr_b16 a[114:115], v7 offset:256\n"
"v_or_b32 v11, s46, v11\n"
"v_mfma_f32_16x16x32_bf16 v[180:183], a[124:127], a[44:47], 0\n"
"ds_read_b128 a[84:87], v6 offset:512\n"
"v_xor_b32 v12, 32, v11\n"
"v_mfma_f32_16x16x32_bf16 v[180:183], a[120:123], a[16:19], v[180:183]\n"
"ds_read_b64_tr_b16 a[116:117], v7 offset:4096\n"
"v_xor_b32 v14, v16, v17\n"
"v_mfma_f32_16x16x32_bf16 v[172:175], a[120:123], a[12:15], 0\n"
"ds_read_b128 a[60:63], v6 offset:2048\n"
"v_lshrrev_b32 v15, 6, v1\n"
"v_mfma_f32_16x16x32_bf16 v[172:175], a[124:127], a[40:43], v[172:175]\n"
"s_add_u32 s66, s66, s36\n"
"ds_read_b64_tr_b16 a[118:119], v7 offset:4352\n"
"v_mfma_f32_16x16x32_bf16 v[164:167], a[124:127], a[36:39], 0\n"
"s_add_u32 m0, s44, 4096\n"
"ds_read_b128 a[88:91], v6 offset:2560\n"
"v_mfma_f32_16x16x32_bf16 v[164:167], a[120:123], a[8:11], v[164:167]\n"
"buffer_load_dwordx4 v2, s[8:11], s66 offen lds\n"
"v_mfma_f32_16x16x32_bf16 v[156:159], a[120:123], a[4:7], 0\n"
"s_add_u32 s66, s66, s36\n"
"s_add_u32 m0, s44, 5120\n"
"ds_read_b128 a[64:67], v6 offset:4096\n"
"v_mfma_f32_16x16x32_bf16 v[156:159], a[124:127], a[32:35], v[156:159]\n"
"buffer_load_dwordx4 v2, s[8:11], s66 offen lds\n"
"v_mfma_f32_16x16x32_bf16 v[148:151], a[124:127], a[28:31], 0\n"
"ds_read_b128 a[92:95], v6 offset:4608\n"
"v_lshl_or_b32 v14, v15, 10, v14\n"
"v_mfma_f32_16x16x32_bf16 v[148:151], a[120:123], a[0:3], v[148:151]\n"
"v_add_u32 v15, 15360, v14\n"
"v_mfma_f32_16x16x32_bf16 v[200:203], a[128:131], a[0:3], 0\n"
"s_mov_b32 s22, 134217728\n"
"v_mfma_f32_16x16x32_bf16 v[200:203], a[132:135], a[28:31], v[200:203]\n"
"s_mov_b32 s23, 131072\n"
"ds_read_b64_tr_b16 a[120:121], v7 offset:8\n"
"v_mfma_f32_16x16x32_bf16 v[208:211], a[132:135], a[32:35], 0\n"
"s_mov_b32 s26, 4194304\n"
"ds_read_b128 a[68:71], v6 offset:6144\n"
"v_mfma_f32_16x16x32_bf16 v[208:211], a[128:131], a[4:7], v[208:211]\n"
"s_mov_b32 s27, 131072\n"
"ds_read_b64_tr_b16 a[122:123], v7 offset:264\n"
"v_mfma_f32_16x16x32_bf16 v[216:219], a[128:131], a[8:11], 0\n"
"s_mov_b32 s30, 3670016\n"
"ds_read_b128 a[96:99], v6 offset:6656\n"
"v_mfma_f32_16x16x32_bf16 v[216:219], a[132:135], a[36:39], v[216:219]\n"
"s_mov_b32 s31, 131072\n"
"ds_read_b64_tr_b16 a[124:125], v7 offset:4104\n"
"v_mfma_f32_16x16x32_bf16 v[224:227], a[132:135], a[40:43], 0\n"
"s_and_b32 s70, s84, 1\n"
"ds_read_b128 a[72:75], v6 offset:8192\n"
"v_mfma_f32_16x16x32_bf16 v[224:227], a[128:131], a[12:15], v[224:227]\n"
"s_add_u32 s66, s66, s36\n"
"ds_read_b64_tr_b16 a[126:127], v7 offset:4360\n"
"v_mfma_f32_16x16x32_bf16 v[232:235], a[128:131], a[16:19], 0\n"
"s_add_u32 m0, s44, 6144\n"
"ds_read_b128 a[100:103], v6 offset:8704\n"
"v_mfma_f32_16x16x32_bf16 v[232:235], a[132:135], a[44:47], v[232:235]\n"
"buffer_load_dwordx4 v2, s[8:11], s66 offen lds\n"
"v_mfma_f32_16x16x32_bf16 v[240:243], a[132:135], a[48:51], 0\n"
"s_xor_b32 s75, s65, 128\n"
"s_xor_b32 m0, s45, 16384\n"
"ds_read_b128 a[76:79], v6 offset:10240\n"
"v_mfma_f32_16x16x32_bf16 v[240:243], a[128:131], a[20:23], v[240:243]\n"
"buffer_load_dwordx4 v3, s[12:15], s75 offen lds\n"
"v_mfma_f32_16x16x32_bf16 v[248:251], a[128:131], a[24:27], 0\n"
"s_lshr_b32 s71, s84, 1\n"
"ds_read_b128 a[104:107], v6 offset:10752\n"
"v_mfma_f32_16x16x32_bf16 v[248:251], a[132:135], a[52:55], v[248:251]\n"
"s_and_b32 s76, s71, 1\n"
"v_mfma_f32_16x16x32_bf16 v[252:255], a[140:143], a[52:55], 0\n"
"s_lshr_b32 s86, s84, 2\n"
"v_mfma_f32_16x16x32_bf16 v[252:255], a[136:139], a[24:27], v[252:255]\n"
"s_lshl_b32 s86, s86, 8\n"
"ds_read_b64_tr_b16 a[128:129], v7 offset:512\n"
"v_mfma_f32_16x16x32_bf16 v[244:247], a[136:139], a[20:23], 0\n"
"s_lshl_b32 s87, s82, 6\n"
"ds_read_b128 a[80:83], v6 offset:12288\n"
"v_mfma_f32_16x16x32_bf16 v[244:247], a[140:143], a[48:51], v[244:247]\n"
"s_or_b32 s86, s86, s87\n"
"ds_read_b64_tr_b16 a[130:131], v7 offset:768\n"
"v_mfma_f32_16x16x32_bf16 v[236:239], a[140:143], a[44:47], 0\n"
"s_xor_b32 s74, s65, 65664\n"
"ds_read_b128 a[108:111], v6 offset:12800\n"
"v_mfma_f32_16x16x32_bf16 v[236:239], a[136:139], a[16:19], v[236:239]\n"
"s_xor_b32 m0, s45, 17408\n"
"ds_read_b64_tr_b16 a[132:133], v7 offset:4608\n"
"v_mfma_f32_16x16x32_bf16 v[228:231], a[136:139], a[12:15], 0\n"
"buffer_load_dwordx4 v3, s[12:15], s74 offen lds\n"
"v_mfma_f32_16x16x32_bf16 v[228:231], a[140:143], a[40:43], v[228:231]\n"
"s_lshl_b32 s48, s84, 18\n"
"ds_read_b64_tr_b16 a[134:135], v7 offset:4864\n"
"v_mfma_f32_16x16x32_bf16 v[220:223], a[140:143], a[36:39], 0\n"
"s_or_b32 s48, s48, s86\n"
"v_mfma_f32_16x16x32_bf16 v[220:223], a[136:139], a[8:11], v[220:223]\n"
"s_xor_b32 s48, s48, 768\n"
"v_mfma_f32_16x16x32_bf16 v[212:215], a[136:139], a[4:7], 0\n"
"s_xor_b32 s75, s65, 131200\n"
"v_mfma_f32_16x16x32_bf16 v[212:215], a[140:143], a[32:35], v[212:215]\n"
"s_xor_b32 m0, s45, 18432\n"
"v_mfma_f32_16x16x32_bf16 v[204:207], a[140:143], a[28:31], 0\n"
"buffer_load_dwordx4 v3, s[12:15], s75 offen lds\n"
"v_mfma_f32_16x16x32_bf16 v[204:207], a[136:139], a[0:3], v[204:207]\n"
"s_waitcnt lgkmcnt(15)\n"
"s_lshl1_add_u32 s87, s83, s70\n"
"v_mfma_f32_16x16x32_bf16 v[32:35], a[112:115], a[56:59], v[32:35]\n"
"s_waitcnt lgkmcnt(15)\n"
"s_mul_i32 s56, s87, 114688\n"
"v_mfma_f32_16x16x32_bf16 v[32:35], a[116:119], a[84:87], v[32:35]\n"
"s_waitcnt lgkmcnt(15)\n"
"s_add_u32 s56, s56, s86\n"
"ds_read_b64_tr_b16 a[136:137], v7 offset:520\n"
"v_mfma_f32_16x16x32_bf16 v[40:43], a[116:119], a[88:91], v[40:43]\n"
"s_add_u32 s56, s56, 49152\n"
"v_mfma_f32_16x16x32_bf16 v[40:43], a[112:115], a[60:63], v[40:43]\n"
"s_waitcnt lgkmcnt(15)\n"
"s_and_b32 s86, s83, 8\n"
"ds_read_b64_tr_b16 a[138:139], v7 offset:776\n"
"v_mfma_f32_16x16x32_bf16 v[48:51], a[112:115], a[64:67], v[48:51]\n"
"s_waitcnt lgkmcnt(15)\n"
"s_xor_b32 s74, s65, 196736\n"
"v_mfma_f32_16x16x32_bf16 v[48:51], a[116:119], a[92:95], v[48:51]\n"
"s_waitcnt lgkmcnt(14)\n"
"s_xor_b32 m0, s45, 19456\n"
"ds_read_b64_tr_b16 a[140:141], v7 offset:4616\n"
"v_mfma_f32_16x16x32_bf16 v[56:59], a[116:119], a[96:99], v[56:59]\n"
"buffer_load_dwordx4 v3, s[12:15], s74 offen lds\n"
"v_mfma_f32_16x16x32_bf16 v[56:59], a[112:115], a[68:71], v[56:59]\n"
"s_waitcnt lgkmcnt(13)\n"
"s_lshl_b32 s86, s86, 10\n"
"ds_read_b64_tr_b16 a[142:143], v7 offset:4872\n"
"v_mfma_f32_16x16x32_bf16 v[64:67], a[112:115], a[72:75], v[64:67]\n"
"s_waitcnt lgkmcnt(12)\n"
"s_xor_b32 s86, s86, s89\n"
"v_mfma_f32_16x16x32_bf16 v[64:67], a[116:119], a[100:103], v[64:67]\n"
"s_waitcnt lgkmcnt(10)\n"
"s_xor_b32 s87, s86, 2087936\n"
"v_mfma_f32_16x16x32_bf16 v[72:75], a[116:119], a[104:107], v[72:75]\n"
"s_lshl_b32 s50, s88, 16\n"
"v_mfma_f32_16x16x32_bf16 v[72:75], a[112:115], a[76:79], v[72:75]\n"
"s_waitcnt lgkmcnt(8)\n"
"s_or_b32 s50, s50, s87\n"
"v_mfma_f32_16x16x32_bf16 v[80:83], a[112:115], a[80:83], v[80:83]\n"
"s_waitcnt lgkmcnt(6)\n"
"s_lshl_b32 s86, s47, 14\n"
"v_mfma_f32_16x16x32_bf16 v[80:83], a[116:119], a[108:111], v[80:83]\n"
"s_xor_b32 s51, s50, s86\n"
"v_mfma_f32_16x16x32_bf16 v[84:87], a[124:127], a[108:111], v[84:87]\n"
"s_waitcnt vmcnt(15) lgkmcnt(0)\n"
"s_barrier\n"
"s_add_u32 s37, s37, 128\n"
"s_cmp_eq_u32 s37, 1024\n"
"s_cselect_b32 s37, 256, s37\n"
"s_lshl_b32 s65, s37, 12\n"
"s_andn2_b32 s66, s66, 896\n"
"s_or_b32 s66, s66, s37\n"
"v_mfma_f32_16x16x32_bf16 v[84:87], a[120:123], a[80:83], v[84:87]\n"
"s_add_u32 m0, s44, 71680\n"
"ds_read_b64_tr_b16 a[112:113], v7 offset:16384\n"
"v_mfma_f32_16x16x32_bf16 v[76:79], a[120:123], a[76:79], v[76:79]\n"
"buffer_load_dwordx4 v2, s[8:11], s66 offen lds\n"
"v_mfma_f32_16x16x32_bf16 v[76:79], a[124:127], a[104:107], v[76:79]\n"
"s_xor_b32 s51, s51, 131072\n"
"ds_read_b64_tr_b16 a[114:115], v7 offset:16640\n"
"v_mfma_f32_16x16x32_bf16 v[68:71], a[124:127], a[100:103], v[68:71]\n"
"s_lshl3_add_u32 s86, s84, s82\n"
"v_mfma_f32_16x16x32_bf16 v[68:71], a[120:123], a[72:75], v[68:71]\n"
"s_lshl_b32 s86, s86, 14\n"
"ds_read_b64_tr_b16 a[116:117], v7 offset:20480\n"
"v_mfma_f32_16x16x32_bf16 v[60:63], a[120:123], a[68:71], v[60:63]\n"
"s_sub_u32 s66, s66, s36\n"
"v_mfma_f32_16x16x32_bf16 v[60:63], a[124:127], a[96:99], v[60:63]\n"
"s_add_u32 m0, s44, 70656\n"
"ds_read_b64_tr_b16 a[118:119], v7 offset:20736\n"
"v_mfma_f32_16x16x32_bf16 v[52:55], a[124:127], a[92:95], v[52:55]\n"
"buffer_load_dwordx4 v2, s[8:11], s66 offen lds\n"
"v_mfma_f32_16x16x32_bf16 v[52:55], a[120:123], a[64:67], v[52:55]\n"
"s_or_b32 s60, s87, s86\n"
"v_mfma_f32_16x16x32_bf16 v[44:47], a[120:123], a[60:63], v[44:47]\n"
"s_or_b32 s60, s60, 7168\n"
"v_mfma_f32_16x16x32_bf16 v[44:47], a[124:127], a[88:91], v[44:47]\n"
"s_andn2_b32 s59, s60, 262144\n"
"v_mfma_f32_16x16x32_bf16 v[36:39], a[124:127], a[84:87], v[36:39]\n"
"s_sub_u32 s66, s66, s36\n"
"v_mfma_f32_16x16x32_bf16 v[36:39], a[120:123], a[56:59], v[36:39]\n"
"s_add_u32 m0, s44, 69632\n"
"v_mfma_f32_16x16x32_bf16 v[88:91], a[128:131], a[56:59], v[88:91]\n"
"buffer_load_dwordx4 v2, s[8:11], s66 offen lds\n"
"v_mfma_f32_16x16x32_bf16 v[88:91], a[132:135], a[84:87], v[88:91]\n"
"s_xor_b32 s64, s47, s70\n"
"ds_read_b64_tr_b16 a[120:121], v7 offset:16392\n"
"v_mfma_f32_16x16x32_bf16 v[96:99], a[132:135], a[88:91], v[96:99]\n"
"s_lshl_b32 s86, s64, 14\n"
"v_mfma_f32_16x16x32_bf16 v[96:99], a[128:131], a[60:63], v[96:99]\n"
"s_xor_b32 s61, s60, s86\n"
"ds_read_b64_tr_b16 a[122:123], v7 offset:16648\n"
"v_mfma_f32_16x16x32_bf16 v[104:107], a[128:131], a[64:67], v[104:107]\n"
"s_xor_b32 s61, s61, 131072\n"
"v_mfma_f32_16x16x32_bf16 v[104:107], a[132:135], a[92:95], v[104:107]\n"
"s_lshl4_add_u32 s86, s70, s71\n"
"ds_read_b64_tr_b16 a[124:125], v7 offset:20488\n"
"v_mfma_f32_16x16x32_bf16 v[112:115], a[132:135], a[96:99], v[112:115]\n"
"s_sub_u32 s66, s66, s36\n"
"v_mfma_f32_16x16x32_bf16 v[112:115], a[128:131], a[68:71], v[112:115]\n"
"s_add_u32 m0, s44, 68608\n"
"ds_read_b64_tr_b16 a[126:127], v7 offset:20744\n"
"v_mfma_f32_16x16x32_bf16 v[120:123], a[128:131], a[72:75], v[120:123]\n"
"buffer_load_dwordx4 v2, s[8:11], s66 offen lds\n"
"v_mfma_f32_16x16x32_bf16 v[120:123], a[132:135], a[100:103], v[120:123]\n"
"s_lshl3_add_u32 s86, s86, s82\n"
"v_mfma_f32_16x16x32_bf16 v[128:131], a[132:135], a[104:107], v[128:131]\n"
"s_lshl2_add_u32 s86, s86, s76\n"
"v_mfma_f32_16x16x32_bf16 v[128:131], a[128:131], a[76:79], v[128:131]\n"
"s_lshl_b32 s86, s86, 12\n"
"v_mfma_f32_16x16x32_bf16 v[136:139], a[128:131], a[80:83], v[136:139]\n"
"s_xor_b32 s75, s65, 0\n"
"v_mfma_f32_16x16x32_bf16 v[136:139], a[132:135], a[108:111], v[136:139]\n"
"s_xor_b32 m0, s45, 196608\n"
"v_mfma_f32_16x16x32_bf16 v[140:143], a[140:143], a[108:111], v[140:143]\n"
"buffer_load_dwordx4 v3, s[12:15], s75 offen lds\n"
"v_mfma_f32_16x16x32_bf16 v[140:143], a[136:139], a[80:83], v[140:143]\n"
"s_xor_b32 s52, s87, s86\n"
"ds_read_b64_tr_b16 a[128:129], v7 offset:16896\n"
"v_mfma_f32_16x16x32_bf16 v[132:135], a[136:139], a[76:79], v[132:135]\n"
"s_xor_b32 s52, s52, 4194304\n"
"v_mfma_f32_16x16x32_bf16 v[132:135], a[140:143], a[104:107], v[132:135]\n"
"s_lshl_b32 s86, s70, 13\n"
"ds_read_b64_tr_b16 a[130:131], v7 offset:17152\n"
"v_mfma_f32_16x16x32_bf16 v[124:127], a[140:143], a[100:103], v[124:127]\n"
"s_xor_b32 s52, s52, s86\n"
"v_mfma_f32_16x16x32_bf16 v[124:127], a[136:139], a[72:75], v[124:127]\n"
"s_andn2_b32 s53, s52, 131072\n"
"ds_read_b64_tr_b16 a[132:133], v7 offset:20992\n"
"v_mfma_f32_16x16x32_bf16 v[116:119], a[136:139], a[68:71], v[116:119]\n"
"s_xor_b32 s74, s65, 65536\n"
"v_mfma_f32_16x16x32_bf16 v[116:119], a[140:143], a[96:99], v[116:119]\n"
"s_xor_b32 m0, s45, 197632\n"
"ds_read_b64_tr_b16 a[134:135], v7 offset:21248\n"
"v_mfma_f32_16x16x32_bf16 v[108:111], a[140:143], a[92:95], v[108:111]\n"
"buffer_load_dwordx4 v3, s[12:15], s74 offen lds\n"
"v_mfma_f32_16x16x32_bf16 v[108:111], a[136:139], a[64:67], v[108:111]\n"
"s_lshl_b32 s86, s85, 11\n"
"v_mfma_f32_16x16x32_bf16 v[100:103], a[136:139], a[60:63], v[100:103]\n"
"s_or_b32 s63, s86, 65536\n"
"v_mfma_f32_16x16x32_bf16 v[100:103], a[140:143], a[88:91], v[100:103]\n"
"s_cmp_lg_u32 s76, 0\n"
"s_cselect_b32 s54, s53, 2147483648\n"
"s_cselect_b32 s55, 2147483648, s52\n"
"s_cselect_b32 s57, 2147483648, s56\n"
"s_mov_b32 s86, 16384\n"
"s_cselect_b32 s58, s86, -16384\n"
"s_cselect_b32 s86, 0, 8192\n"
"v_mfma_f32_16x16x32_bf16 v[92:95], a[140:143], a[84:87], v[92:95]\n"
"s_xor_b32 s75, s65, 131072\n"
"v_mfma_f32_16x16x32_bf16 v[92:95], a[136:139], a[56:59], v[92:95]\n"
"s_waitcnt lgkmcnt(10)\n"
"s_xor_b32 m0, s45, 198656\n"
"v_mfma_f32_16x16x32_bf16 v[144:147], a[112:115], a[56:59], v[144:147]\n"
"s_waitcnt lgkmcnt(8)\n"
"buffer_load_dwordx4 v3, s[12:15], s75 offen lds\n"
"v_mfma_f32_16x16x32_bf16 v[144:147], a[116:119], a[84:87], v[144:147]\n"
"ds_read_b64_tr_b16 a[136:137], v7 offset:16904\n"
"v_xor_b32 v13, s86, v13\n"
"v_mfma_f32_16x16x32_bf16 v[152:155], a[116:119], a[88:91], v[152:155]\n"
"s_or_b32 s86, s82, s84\n"
"s_cselect_b32 s62, 2147483648, s89\n"
"v_mfma_f32_16x16x32_bf16 v[152:155], a[112:115], a[60:63], v[152:155]\n"
"ds_read_b64_tr_b16 a[138:139], v7 offset:17160\n"
"v_mov_b32 v18, 0\n"
"v_mfma_f32_16x16x32_bf16 v[160:163], a[112:115], a[64:67], v[160:163]\n"
"v_mov_b32 v20, 0\n"
"v_mfma_f32_16x16x32_bf16 v[160:163], a[116:119], a[92:95], v[160:163]\n"
"ds_read_b64_tr_b16 a[140:141], v7 offset:21000\n"
"v_mov_b32 v22, 0\n"
"v_mfma_f32_16x16x32_bf16 v[168:171], a[116:119], a[96:99], v[168:171]\n"
"s_xor_b32 s74, s65, 196608\n"
"v_mfma_f32_16x16x32_bf16 v[168:171], a[112:115], a[68:71], v[168:171]\n"
"s_xor_b32 m0, s45, 199680\n"
"ds_read_b64_tr_b16 a[142:143], v7 offset:21256\n"
"v_mfma_f32_16x16x32_bf16 v[176:179], a[112:115], a[72:75], v[176:179]\n"
"buffer_load_dwordx4 v3, s[12:15], s74 offen lds\n"
"v_mfma_f32_16x16x32_bf16 v[176:179], a[116:119], a[100:103], v[176:179]\n"
"v_mov_b32 v24, 0\n"
"v_mfma_f32_16x16x32_bf16 v[184:187], a[116:119], a[104:107], v[184:187]\n"
"v_mov_b32 v26, 0\n"
"v_mfma_f32_16x16x32_bf16 v[184:187], a[112:115], a[76:79], v[184:187]\n"
"v_mov_b32 v28, 0\n"
"v_mfma_f32_16x16x32_bf16 v[192:195], a[112:115], a[80:83], v[192:195]\n"
"v_xor_b32 v6, 65536, v6\n"
"v_mfma_f32_16x16x32_bf16 v[192:195], a[116:119], a[108:111], v[192:195]\n"
"s_waitcnt lgkmcnt(8)\n"
"v_xor_b32 v7, 196608, v7\n"
"v_mfma_f32_16x16x32_bf16 v[196:199], a[124:127], a[108:111], v[196:199]\n"
"s_waitcnt vmcnt(12) lgkmcnt(0)\n"
"s_barrier\n"
"v_mfma_f32_16x16x32_bf16 v[196:199], a[120:123], a[80:83], v[196:199]\n"
"ds_read_b64_tr_b16 a[112:113], v7 offset:0\n"
"v_mov_b32 v30, 0\n"
"v_mfma_f32_16x16x32_bf16 v[188:191], a[120:123], a[76:79], v[188:191]\n"
"s_lshl1_add_u32 s86, s85, s70\n"
"ds_read_b128 a[0:3], v6 offset:0\n"
"v_mfma_f32_16x16x32_bf16 v[188:191], a[124:127], a[104:107], v[188:191]\n"
"s_lshl3_add_u32 s86, s86, s71\n"
"ds_read_b64_tr_b16 a[114:115], v7 offset:256\n"
"v_mfma_f32_16x16x32_bf16 v[180:183], a[124:127], a[100:103], v[180:183]\n"
"s_lshl1_add_u32 s86, s86, s47\n"
"ds_read_b128 a[28:31], v6 offset:512\n"
"v_mfma_f32_16x16x32_bf16 v[180:183], a[120:123], a[72:75], v[180:183]\n"
"s_lshl4_add_u32 s43, s86, s89\n"
"ds_read_b64_tr_b16 a[116:117], v7 offset:4096\n"
"v_mfma_f32_16x16x32_bf16 v[172:175], a[120:123], a[68:71], v[172:175]\n"
"s_or_b32 s43, s43, 8192\n"
"ds_read_b128 a[4:7], v6 offset:2048\n"
"v_mfma_f32_16x16x32_bf16 v[172:175], a[124:127], a[96:99], v[172:175]\n"
"s_sub_u32 s66, s66, s36\n"
"ds_read_b64_tr_b16 a[118:119], v7 offset:4352\n"
"v_mfma_f32_16x16x32_bf16 v[164:167], a[124:127], a[92:95], v[164:167]\n"
"s_add_u32 m0, s44, 67584\n"
"ds_read_b128 a[32:35], v6 offset:2560\n"
"v_mfma_f32_16x16x32_bf16 v[164:167], a[120:123], a[64:67], v[164:167]\n"
"buffer_load_dwordx4 v2, s[8:11], s66 offen lds\n"
"v_mfma_f32_16x16x32_bf16 v[156:159], a[120:123], a[60:63], v[156:159]\n"
"s_sub_u32 s66, s66, s36\n"
"s_add_u32 m0, s44, 66560\n"
"ds_read_b128 a[8:11], v6 offset:4096\n"
"v_mfma_f32_16x16x32_bf16 v[156:159], a[124:127], a[88:91], v[156:159]\n"
"buffer_load_dwordx4 v2, s[8:11], s66 offen lds\n"
"v_mfma_f32_16x16x32_bf16 v[148:151], a[124:127], a[84:87], v[148:151]\n"
"s_xor_b32 s86, s52, 1114112\n"
"ds_read_b128 a[36:39], v6 offset:4608\n"
"v_mfma_f32_16x16x32_bf16 v[148:151], a[120:123], a[56:59], v[148:151]\n"
"s_or_b32 s87, s60, 65536\n"
"v_mfma_f32_16x16x32_bf16 v[200:203], a[128:131], a[56:59], v[200:203]\n"
"s_cmp_lg_u32 s85, 0\n"
"s_cselect_b32 s42, s86, s87\n"
"v_mfma_f32_16x16x32_bf16 v[200:203], a[132:135], a[84:87], v[200:203]\n"
"s_lshl_b32 s86, s76, 13\n"
"ds_read_b64_tr_b16 a[120:121], v7 offset:8\n"
"v_mfma_f32_16x16x32_bf16 v[208:211], a[132:135], a[88:91], v[208:211]\n"
"s_xor_b32 s42, s42, s86\n"
"ds_read_b128 a[12:15], v6 offset:6144\n"
"v_mfma_f32_16x16x32_bf16 v[208:211], a[128:131], a[60:63], v[208:211]\n"
"s_mov_b32 s78, 0x3e0293ee\n"
"ds_read_b64_tr_b16 a[122:123], v7 offset:264\n"
"v_mfma_f32_16x16x32_bf16 v[216:219], a[128:131], a[64:67], v[216:219]\n"
"s_mov_b32 s79, 0x3e0293ee\n"
"ds_read_b128 a[40:43], v6 offset:6656\n"
"v_mfma_f32_16x16x32_bf16 v[216:219], a[132:135], a[92:95], v[216:219]\n"
"s_mov_b32 s76, 4\n"
"ds_read_b64_tr_b16 a[124:125], v7 offset:4104\n"
"v_mfma_f32_16x16x32_bf16 v[224:227], a[132:135], a[96:99], v[224:227]\n"
"s_mov_b32 s77, 262144\n"
"ds_read_b128 a[16:19], v6 offset:8192\n"
"v_mfma_f32_16x16x32_bf16 v[224:227], a[128:131], a[68:71], v[224:227]\n"
"s_sub_u32 s66, s66, s36\n"
"ds_read_b64_tr_b16 a[126:127], v7 offset:4360\n"
"v_mfma_f32_16x16x32_bf16 v[232:235], a[128:131], a[72:75], v[232:235]\n"
"s_add_u32 m0, s44, 65536\n"
"ds_read_b128 a[44:47], v6 offset:8704\n"
"v_mfma_f32_16x16x32_bf16 v[232:235], a[132:135], a[100:103], v[232:235]\n"
"buffer_load_dwordx4 v2, s[8:11], s66 offen lds\n"
"v_mfma_f32_16x16x32_bf16 v[240:243], a[132:135], a[104:107], v[240:243]\n"
"s_xor_b32 s75, s65, 128\n"
"s_xor_b32 m0, s45, 212992\n"
"ds_read_b128 a[20:23], v6 offset:10240\n"
"v_mfma_f32_16x16x32_bf16 v[240:243], a[128:131], a[76:79], v[240:243]\n"
"buffer_load_dwordx4 v3, s[12:15], s75 offen lds\n"
"v_mfma_f32_16x16x32_bf16 v[248:251], a[128:131], a[80:83], v[248:251]\n"
"s_andn2_b32 s67, s42, 8192\n"
"ds_read_b128 a[48:51], v6 offset:10752\n"
"v_mfma_f32_16x16x32_bf16 v[248:251], a[132:135], a[108:111], v[248:251]\n"
"buffer_store_dword v27, v23, s[20:23], s67 offen sc1\n"
"v_mfma_f32_16x16x32_bf16 v[252:255], a[140:143], a[108:111], v[252:255]\n"
"s_mov_b32 s70, 0\n"
"v_mfma_f32_16x16x32_bf16 v[252:255], a[136:139], a[80:83], v[252:255]\n"
"s_add_u32 s68, s68, 1\n"
"ds_read_b64_tr_b16 a[128:129], v7 offset:512\n"
"v_mfma_f32_16x16x32_bf16 v[244:247], a[136:139], a[76:79], v[244:247]\n"
"ds_read_b128 a[24:27], v6 offset:12288\n"
"v_mfma_f32_16x16x32_bf16 v[244:247], a[140:143], a[104:107], v[244:247]\n"
"ds_read_b64_tr_b16 a[130:131], v7 offset:768\n"
"v_mfma_f32_16x16x32_bf16 v[236:239], a[140:143], a[100:103], v[236:239]\n"
"s_xor_b32 s74, s65, 65664\n"
"ds_read_b128 a[52:55], v6 offset:12800\n"
"v_mfma_f32_16x16x32_bf16 v[236:239], a[136:139], a[72:75], v[236:239]\n"
"s_xor_b32 m0, s45, 214016\n"
"ds_read_b64_tr_b16 a[132:133], v7 offset:4608\n"
"v_mfma_f32_16x16x32_bf16 v[228:231], a[136:139], a[68:71], v[228:231]\n"
"buffer_load_dwordx4 v3, s[12:15], s74 offen lds\n"
"v_mfma_f32_16x16x32_bf16 v[228:231], a[140:143], a[96:99], v[228:231]\n"
"ds_read_b64_tr_b16 a[134:135], v7 offset:4864\n"
"v_mfma_f32_16x16x32_bf16 v[220:223], a[140:143], a[92:95], v[220:223]\n"
"v_mfma_f32_16x16x32_bf16 v[220:223], a[136:139], a[64:67], v[220:223]\n"
"v_mfma_f32_16x16x32_bf16 v[212:215], a[136:139], a[60:63], v[212:215]\n"
"s_xor_b32 s75, s65, 131200\n"
"v_mfma_f32_16x16x32_bf16 v[212:215], a[140:143], a[88:91], v[212:215]\n"
"s_xor_b32 m0, s45, 215040\n"
"v_mfma_f32_16x16x32_bf16 v[204:207], a[140:143], a[84:87], v[204:207]\n"
"buffer_load_dwordx4 v3, s[12:15], s75 offen lds\n"
"v_mfma_f32_16x16x32_bf16 v[204:207], a[136:139], a[56:59], v[204:207]\n"
"s_waitcnt lgkmcnt(15)\n"
"v_mfma_f32_16x16x32_bf16 v[32:35], a[112:115], a[0:3], v[32:35]\n"
"s_waitcnt lgkmcnt(15)\n"
"v_mfma_f32_16x16x32_bf16 v[32:35], a[116:119], a[28:31], v[32:35]\n"
"s_waitcnt lgkmcnt(15)\n"
"ds_read_b64_tr_b16 a[136:137], v7 offset:520\n"
"v_mfma_f32_16x16x32_bf16 v[40:43], a[116:119], a[32:35], v[40:43]\n"
"v_mfma_f32_16x16x32_bf16 v[40:43], a[112:115], a[4:7], v[40:43]\n"
"s_waitcnt lgkmcnt(15)\n"
"ds_read_b64_tr_b16 a[138:139], v7 offset:776\n"
"v_mfma_f32_16x16x32_bf16 v[48:51], a[112:115], a[8:11], v[48:51]\n"
"s_waitcnt lgkmcnt(15)\n"
"s_xor_b32 s74, s65, 196736\n"
"v_mfma_f32_16x16x32_bf16 v[48:51], a[116:119], a[36:39], v[48:51]\n"
"s_waitcnt lgkmcnt(14)\n"
"s_xor_b32 m0, s45, 216064\n"
"ds_read_b64_tr_b16 a[140:141], v7 offset:4616\n"
"v_mfma_f32_16x16x32_bf16 v[56:59], a[116:119], a[40:43], v[56:59]\n"
"buffer_load_dwordx4 v3, s[12:15], s74 offen lds\n"
"v_mfma_f32_16x16x32_bf16 v[56:59], a[112:115], a[12:15], v[56:59]\n"
"s_waitcnt lgkmcnt(13)\n"
"ds_read_b64_tr_b16 a[142:143], v7 offset:4872\n"
"v_mfma_f32_16x16x32_bf16 v[64:67], a[112:115], a[16:19], v[64:67]\n"
"s_waitcnt lgkmcnt(12)\n"
"v_mfma_f32_16x16x32_bf16 v[64:67], a[116:119], a[44:47], v[64:67]\n"
"s_waitcnt lgkmcnt(10)\n"
"v_mfma_f32_16x16x32_bf16 v[72:75], a[116:119], a[48:51], v[72:75]\n"
"v_mfma_f32_16x16x32_bf16 v[72:75], a[112:115], a[20:23], v[72:75]\n"
"s_waitcnt lgkmcnt(8)\n"
"v_mfma_f32_16x16x32_bf16 v[80:83], a[112:115], a[24:27], v[80:83]\n"
"s_waitcnt lgkmcnt(6)\n"
"v_mfma_f32_16x16x32_bf16 v[80:83], a[116:119], a[52:55], v[80:83]\n"
"v_mfma_f32_16x16x32_bf16 v[84:87], a[124:127], a[52:55], v[84:87]\n"
"s_waitcnt vmcnt(16) lgkmcnt(0)\n"
"s_barrier\n"
"s_add_u32 s37, s37, 128\n"
"s_cmp_eq_u32 s37, 1024\n"
"s_cselect_b32 s37, 256, s37\n"
"s_lshl_b32 s65, s37, 12\n"
"s_andn2_b32 s66, s66, 896\n"
"s_or_b32 s66, s66, s37\n"
"v_mfma_f32_16x16x32_bf16 v[84:87], a[120:123], a[24:27], v[84:87]\n"
"s_add_u32 m0, s44, 0\n"
"ds_read_b64_tr_b16 a[112:113], v7 offset:16384\n"
"v_mfma_f32_16x16x32_bf16 v[76:79], a[120:123], a[20:23], v[76:79]\n"
"buffer_load_dwordx4 v2, s[8:11], s66 offen lds\n"
"v_mfma_f32_16x16x32_bf16 v[76:79], a[124:127], a[48:51], v[76:79]\n"
"ds_read_b64_tr_b16 a[114:115], v7 offset:16640\n"
"v_mfma_f32_16x16x32_bf16 v[68:71], a[124:127], a[44:47], v[68:71]\n"
"v_mfma_f32_16x16x32_bf16 v[68:71], a[120:123], a[16:19], v[68:71]\n"
"ds_read_b64_tr_b16 a[116:117], v7 offset:20480\n"
"v_mfma_f32_16x16x32_bf16 v[60:63], a[120:123], a[12:15], v[60:63]\n"
"s_add_u32 s66, s66, s36\n"
"v_mfma_f32_16x16x32_bf16 v[60:63], a[124:127], a[40:43], v[60:63]\n"
"s_add_u32 m0, s44, 1024\n"
"ds_read_b64_tr_b16 a[118:119], v7 offset:20736\n"
"v_mfma_f32_16x16x32_bf16 v[52:55], a[124:127], a[36:39], v[52:55]\n"
"buffer_load_dwordx4 v2, s[8:11], s66 offen lds\n"
"v_mfma_f32_16x16x32_bf16 v[52:55], a[120:123], a[8:11], v[52:55]\n"
"v_mfma_f32_16x16x32_bf16 v[44:47], a[120:123], a[4:7], v[44:47]\n"
"v_mfma_f32_16x16x32_bf16 v[44:47], a[124:127], a[32:35], v[44:47]\n"
"v_mfma_f32_16x16x32_bf16 v[36:39], a[124:127], a[28:31], v[36:39]\n"
"s_add_u32 s66, s66, s36\n"
"v_mfma_f32_16x16x32_bf16 v[36:39], a[120:123], a[0:3], v[36:39]\n"
"s_add_u32 m0, s44, 2048\n"
"v_mfma_f32_16x16x32_bf16 v[88:91], a[128:131], a[0:3], v[88:91]\n"
"buffer_load_dwordx4 v2, s[8:11], s66 offen lds\n"
"v_mfma_f32_16x16x32_bf16 v[88:91], a[132:135], a[28:31], v[88:91]\n"
"ds_read_b64_tr_b16 a[120:121], v7 offset:16392\n"
"v_mfma_f32_16x16x32_bf16 v[96:99], a[132:135], a[32:35], v[96:99]\n"
"v_mfma_f32_16x16x32_bf16 v[96:99], a[128:131], a[4:7], v[96:99]\n"
"ds_read_b64_tr_b16 a[122:123], v7 offset:16648\n"
"v_mfma_f32_16x16x32_bf16 v[104:107], a[128:131], a[8:11], v[104:107]\n"
"v_mfma_f32_16x16x32_bf16 v[104:107], a[132:135], a[36:39], v[104:107]\n"
"ds_read_b64_tr_b16 a[124:125], v7 offset:20488\n"
"v_mfma_f32_16x16x32_bf16 v[112:115], a[132:135], a[40:43], v[112:115]\n"
"s_add_u32 s66, s66, s36\n"
"v_mfma_f32_16x16x32_bf16 v[112:115], a[128:131], a[12:15], v[112:115]\n"
"s_add_u32 m0, s44, 3072\n"
"ds_read_b64_tr_b16 a[126:127], v7 offset:20744\n"
"v_mfma_f32_16x16x32_bf16 v[120:123], a[128:131], a[16:19], v[120:123]\n"
"buffer_load_dwordx4 v2, s[8:11], s66 offen lds\n"
"v_mfma_f32_16x16x32_bf16 v[120:123], a[132:135], a[44:47], v[120:123]\n"
"v_mfma_f32_16x16x32_bf16 v[128:131], a[132:135], a[48:51], v[128:131]\n"
"v_mfma_f32_16x16x32_bf16 v[128:131], a[128:131], a[20:23], v[128:131]\n"
"v_mfma_f32_16x16x32_bf16 v[136:139], a[128:131], a[24:27], v[136:139]\n"
"s_xor_b32 s74, s65, 0\n"
"v_mfma_f32_16x16x32_bf16 v[136:139], a[132:135], a[52:55], v[136:139]\n"
"s_xor_b32 m0, s45, 0\n"
"v_mfma_f32_16x16x32_bf16 v[140:143], a[140:143], a[52:55], v[140:143]\n"
"buffer_load_dwordx4 v3, s[12:15], s74 offen lds\n"
"v_mfma_f32_16x16x32_bf16 v[140:143], a[136:139], a[24:27], v[140:143]\n"
"ds_read_b64_tr_b16 a[128:129], v7 offset:16896\n"
"v_mfma_f32_16x16x32_bf16 v[132:135], a[136:139], a[20:23], v[132:135]\n"
"v_mfma_f32_16x16x32_bf16 v[132:135], a[140:143], a[48:51], v[132:135]\n"
"ds_read_b64_tr_b16 a[130:131], v7 offset:17152\n"
"v_mfma_f32_16x16x32_bf16 v[124:127], a[140:143], a[44:47], v[124:127]\n"
"v_mfma_f32_16x16x32_bf16 v[124:127], a[136:139], a[16:19], v[124:127]\n"
"ds_read_b64_tr_b16 a[132:133], v7 offset:20992\n"
"v_mfma_f32_16x16x32_bf16 v[116:119], a[136:139], a[12:15], v[116:119]\n"
"s_xor_b32 s75, s65, 65536\n"
"v_mfma_f32_16x16x32_bf16 v[116:119], a[140:143], a[40:43], v[116:119]\n"
"s_xor_b32 m0, s45, 1024\n"
"ds_read_b64_tr_b16 a[134:135], v7 offset:21248\n"
"v_mfma_f32_16x16x32_bf16 v[108:111], a[140:143], a[36:39], v[108:111]\n"
"buffer_load_dwordx4 v3, s[12:15], s75 offen lds\n"
"v_mfma_f32_16x16x32_bf16 v[108:111], a[136:139], a[8:11], v[108:111]\n"
"v_mfma_f32_16x16x32_bf16 v[100:103], a[136:139], a[4:7], v[100:103]\n"
"v_mfma_f32_16x16x32_bf16 v[100:103], a[140:143], a[32:35], v[100:103]\n"
"v_mfma_f32_16x16x32_bf16 v[92:95], a[140:143], a[28:31], v[92:95]\n"
"s_xor_b32 s74, s65, 131072\n"
"v_mfma_f32_16x16x32_bf16 v[92:95], a[136:139], a[0:3], v[92:95]\n"
"s_waitcnt lgkmcnt(10)\n"
"s_xor_b32 m0, s45, 2048\n"
"v_mfma_f32_16x16x32_bf16 v[144:147], a[112:115], a[0:3], v[144:147]\n"
"s_waitcnt lgkmcnt(8)\n"
"buffer_load_dwordx4 v3, s[12:15], s74 offen lds\n"
"v_mfma_f32_16x16x32_bf16 v[144:147], a[116:119], a[28:31], v[144:147]\n"
"ds_read_b64_tr_b16 a[136:137], v7 offset:16904\n"
"v_mfma_f32_16x16x32_bf16 v[152:155], a[116:119], a[32:35], v[152:155]\n"
"v_mfma_f32_16x16x32_bf16 v[152:155], a[112:115], a[4:7], v[152:155]\n"
"ds_read_b64_tr_b16 a[138:139], v7 offset:17160\n"
"v_mfma_f32_16x16x32_bf16 v[160:163], a[112:115], a[8:11], v[160:163]\n"
"v_mfma_f32_16x16x32_bf16 v[160:163], a[116:119], a[36:39], v[160:163]\n"
"ds_read_b64_tr_b16 a[140:141], v7 offset:21000\n"
"v_mfma_f32_16x16x32_bf16 v[168:171], a[116:119], a[40:43], v[168:171]\n"
"s_xor_b32 s75, s65, 196608\n"
"v_mfma_f32_16x16x32_bf16 v[168:171], a[112:115], a[12:15], v[168:171]\n"
"s_xor_b32 m0, s45, 3072\n"
"ds_read_b64_tr_b16 a[142:143], v7 offset:21256\n"
"v_mfma_f32_16x16x32_bf16 v[176:179], a[112:115], a[16:19], v[176:179]\n"
"buffer_load_dwordx4 v3, s[12:15], s75 offen lds\n"
"v_mfma_f32_16x16x32_bf16 v[176:179], a[116:119], a[44:47], v[176:179]\n"
"v_mfma_f32_16x16x32_bf16 v[184:187], a[116:119], a[48:51], v[184:187]\n"
"v_mfma_f32_16x16x32_bf16 v[184:187], a[112:115], a[20:23], v[184:187]\n"
"v_mfma_f32_16x16x32_bf16 v[192:195], a[112:115], a[24:27], v[192:195]\n"
"v_xor_b32 v6, 65536, v6\n"
"v_mfma_f32_16x16x32_bf16 v[192:195], a[116:119], a[52:55], v[192:195]\n"
"s_waitcnt lgkmcnt(8)\n"
"v_xor_b32 v7, 196608, v7\n"
"v_mfma_f32_16x16x32_bf16 v[196:199], a[124:127], a[52:55], v[196:199]\n"
"s_waitcnt vmcnt(13) lgkmcnt(0)\n"
"s_barrier\n"
"v_mfma_f32_16x16x32_bf16 v[196:199], a[120:123], a[24:27], v[196:199]\n"
"ds_read_b64_tr_b16 a[112:113], v7 offset:0\n"
"v_mfma_f32_16x16x32_bf16 v[188:191], a[120:123], a[20:23], v[188:191]\n"
"ds_read_b128 a[56:59], v6 offset:0\n"
"v_mfma_f32_16x16x32_bf16 v[188:191], a[124:127], a[48:51], v[188:191]\n"
"ds_read_b64_tr_b16 a[114:115], v7 offset:256\n"
"v_mfma_f32_16x16x32_bf16 v[180:183], a[124:127], a[44:47], v[180:183]\n"
"ds_read_b128 a[84:87], v6 offset:512\n"
"v_mfma_f32_16x16x32_bf16 v[180:183], a[120:123], a[16:19], v[180:183]\n"
"ds_read_b64_tr_b16 a[116:117], v7 offset:4096\n"
"v_mfma_f32_16x16x32_bf16 v[172:175], a[120:123], a[12:15], v[172:175]\n"
"ds_read_b128 a[60:63], v6 offset:2048\n"
"v_mfma_f32_16x16x32_bf16 v[172:175], a[124:127], a[40:43], v[172:175]\n"
"s_add_u32 s66, s66, s36\n"
"ds_read_b64_tr_b16 a[118:119], v7 offset:4352\n"
"v_mfma_f32_16x16x32_bf16 v[164:167], a[124:127], a[36:39], v[164:167]\n"
"s_add_u32 m0, s44, 4096\n"
"ds_read_b128 a[88:91], v6 offset:2560\n"
"v_mfma_f32_16x16x32_bf16 v[164:167], a[120:123], a[8:11], v[164:167]\n"
"buffer_load_dwordx4 v2, s[8:11], s66 offen lds\n"
"v_mfma_f32_16x16x32_bf16 v[156:159], a[120:123], a[4:7], v[156:159]\n"
"s_add_u32 s66, s66, s36\n"
"s_add_u32 m0, s44, 5120\n"
"ds_read_b128 a[64:67], v6 offset:4096\n"
"v_mfma_f32_16x16x32_bf16 v[156:159], a[124:127], a[32:35], v[156:159]\n"
"buffer_load_dwordx4 v2, s[8:11], s66 offen lds\n"
"v_mfma_f32_16x16x32_bf16 v[148:151], a[124:127], a[28:31], v[148:151]\n"
"s_add_u32 s66, s66, s36\n"
"s_add_u32 m0, s44, 6144\n"
"ds_read_b128 a[92:95], v6 offset:4608\n"
"v_mfma_f32_16x16x32_bf16 v[148:151], a[120:123], a[0:3], v[148:151]\n"
"buffer_load_dwordx4 v2, s[8:11], s66 offen lds\n"
"v_mfma_f32_16x16x32_bf16 v[200:203], a[128:131], a[0:3], v[200:203]\n"
"v_mfma_f32_16x16x32_bf16 v[200:203], a[132:135], a[28:31], v[200:203]\n"
"ds_read_b64_tr_b16 a[120:121], v7 offset:8\n"
"v_mfma_f32_16x16x32_bf16 v[208:211], a[132:135], a[32:35], v[208:211]\n"
"ds_read_b128 a[68:71], v6 offset:6144\n"
"v_mfma_f32_16x16x32_bf16 v[208:211], a[128:131], a[4:7], v[208:211]\n"
"ds_read_b64_tr_b16 a[122:123], v7 offset:264\n"
"v_mfma_f32_16x16x32_bf16 v[216:219], a[128:131], a[8:11], v[216:219]\n"
"ds_read_b128 a[96:99], v6 offset:6656\n"
"v_mfma_f32_16x16x32_bf16 v[216:219], a[132:135], a[36:39], v[216:219]\n"
"ds_read_b64_tr_b16 a[124:125], v7 offset:4104\n"
"v_mfma_f32_16x16x32_bf16 v[224:227], a[132:135], a[40:43], v[224:227]\n"
"ds_read_b128 a[72:75], v6 offset:8192\n"
"v_mfma_f32_16x16x32_bf16 v[224:227], a[128:131], a[12:15], v[224:227]\n"
"s_xor_b32 s75, s40, 0\n"
"ds_read_b64_tr_b16 a[126:127], v7 offset:4360\n"
"v_mfma_f32_16x16x32_bf16 v[232:235], a[128:131], a[16:19], v[232:235]\n"
"s_add_u32 m0, s72, 0\n"
"ds_read_b128 a[100:103], v6 offset:8704\n"
"v_mfma_f32_16x16x32_bf16 v[232:235], a[132:135], a[44:47], v[232:235]\n"
"buffer_load_dwordx4 v4, s[16:19], s75 offen nt lds\n"
"v_mfma_f32_16x16x32_bf16 v[240:243], a[132:135], a[48:51], v[240:243]\n"
"ds_read_b128 a[76:79], v6 offset:10240\n"
"v_mfma_f32_16x16x32_bf16 v[240:243], a[128:131], a[20:23], v[240:243]\n"
"buffer_load_dwordx4 v5, s[16:19], s75 offen offset:1024 nt lds\n"
"v_mfma_f32_16x16x32_bf16 v[248:251], a[128:131], a[24:27], v[248:251]\n"
"ds_read_b128 a[104:107], v6 offset:10752\n"
"v_mfma_f32_16x16x32_bf16 v[248:251], a[132:135], a[52:55], v[248:251]\n"
"v_mfma_f32_16x16x32_bf16 v[252:255], a[140:143], a[52:55], v[252:255]\n"
"v_mfma_f32_16x16x32_bf16 v[252:255], a[136:139], a[24:27], v[252:255]\n"
"ds_read_b64_tr_b16 a[128:129], v7 offset:512\n"
"v_mfma_f32_16x16x32_bf16 v[244:247], a[136:139], a[20:23], v[244:247]\n"
"ds_read_b128 a[80:83], v6 offset:12288\n"
"v_mfma_f32_16x16x32_bf16 v[244:247], a[140:143], a[48:51], v[244:247]\n"
"ds_read_b64_tr_b16 a[130:131], v7 offset:768\n"
"v_mfma_f32_16x16x32_bf16 v[236:239], a[140:143], a[44:47], v[236:239]\n"
"s_xor_b32 s74, s65, 128\n"
"ds_read_b128 a[108:111], v6 offset:12800\n"
"v_mfma_f32_16x16x32_bf16 v[236:239], a[136:139], a[16:19], v[236:239]\n"
"s_xor_b32 m0, s45, 16384\n"
"ds_read_b64_tr_b16 a[132:133], v7 offset:4608\n"
"v_mfma_f32_16x16x32_bf16 v[228:231], a[136:139], a[12:15], v[228:231]\n"
"buffer_load_dwordx4 v3, s[12:15], s74 offen lds\n"
"v_mfma_f32_16x16x32_bf16 v[228:231], a[140:143], a[40:43], v[228:231]\n"
"s_xor_b32 s75, s65, 65664\n"
"ds_read_b64_tr_b16 a[134:135], v7 offset:4864\n"
"v_mfma_f32_16x16x32_bf16 v[220:223], a[140:143], a[36:39], v[220:223]\n"
"s_xor_b32 m0, s45, 17408\n"
"v_mfma_f32_16x16x32_bf16 v[220:223], a[136:139], a[8:11], v[220:223]\n"
"buffer_load_dwordx4 v3, s[12:15], s75 offen lds\n"
"v_mfma_f32_16x16x32_bf16 v[212:215], a[136:139], a[4:7], v[212:215]\n"
"v_mfma_f32_16x16x32_bf16 v[212:215], a[140:143], a[32:35], v[212:215]\n"
"v_mfma_f32_16x16x32_bf16 v[204:207], a[140:143], a[28:31], v[204:207]\n"
"v_mfma_f32_16x16x32_bf16 v[204:207], a[136:139], a[0:3], v[204:207]\n"
"s_waitcnt lgkmcnt(15)\n"
"v_mfma_f32_16x16x32_bf16 v[32:35], a[112:115], a[56:59], v[32:35]\n"
"s_waitcnt lgkmcnt(15)\n"
"s_xor_b32 s74, s65, 131200\n"
"v_mfma_f32_16x16x32_bf16 v[32:35], a[116:119], a[84:87], v[32:35]\n"
"s_waitcnt lgkmcnt(15)\n"
"s_xor_b32 m0, s45, 18432\n"
"ds_read_b64_tr_b16 a[136:137], v7 offset:520\n"
"v_mfma_f32_16x16x32_bf16 v[40:43], a[116:119], a[88:91], v[40:43]\n"
"buffer_load_dwordx4 v3, s[12:15], s74 offen lds\n"
"v_mfma_f32_16x16x32_bf16 v[40:43], a[112:115], a[60:63], v[40:43]\n"
"s_waitcnt lgkmcnt(15)\n"
"ds_read_b64_tr_b16 a[138:139], v7 offset:776\n"
"v_mfma_f32_16x16x32_bf16 v[48:51], a[112:115], a[64:67], v[48:51]\n"
"s_waitcnt lgkmcnt(15)\n"
"v_mfma_f32_16x16x32_bf16 v[48:51], a[116:119], a[92:95], v[48:51]\n"
"s_waitcnt lgkmcnt(14)\n"
"ds_read_b64_tr_b16 a[140:141], v7 offset:4616\n"
"v_mfma_f32_16x16x32_bf16 v[56:59], a[116:119], a[96:99], v[56:59]\n"
"s_xor_b32 s75, s65, 196736\n"
"v_mfma_f32_16x16x32_bf16 v[56:59], a[112:115], a[68:71], v[56:59]\n"
"s_waitcnt lgkmcnt(13)\n"
"s_xor_b32 m0, s45, 19456\n"
"ds_read_b64_tr_b16 a[142:143], v7 offset:4872\n"
"v_mfma_f32_16x16x32_bf16 v[64:67], a[112:115], a[72:75], v[64:67]\n"
"s_waitcnt lgkmcnt(12)\n"
"buffer_load_dwordx4 v3, s[12:15], s75 offen lds\n"
"v_mfma_f32_16x16x32_bf16 v[64:67], a[116:119], a[100:103], v[64:67]\n"
"s_waitcnt lgkmcnt(10)\n"
"v_mfma_f32_16x16x32_bf16 v[72:75], a[116:119], a[104:107], v[72:75]\n"
"v_mfma_f32_16x16x32_bf16 v[72:75], a[112:115], a[76:79], v[72:75]\n"
"s_waitcnt lgkmcnt(8)\n"
"v_mfma_f32_16x16x32_bf16 v[80:83], a[112:115], a[80:83], v[80:83]\n"
"s_waitcnt lgkmcnt(6)\n"
"v_mfma_f32_16x16x32_bf16 v[80:83], a[116:119], a[108:111], v[80:83]\n"
"v_mfma_f32_16x16x32_bf16 v[84:87], a[124:127], a[108:111], v[84:87]\n"
"s_waitcnt vmcnt(17) lgkmcnt(0)\n"
"s_barrier\n"
"s_add_u32 s37, s37, 128\n"
"s_cmp_eq_u32 s37, 1024\n"
"s_cselect_b32 s37, 256, s37\n"
"s_lshl_b32 s65, s37, 12\n"
"s_andn2_b32 s66, s66, 896\n"
"s_or_b32 s66, s66, s37\n"
"v_mfma_f32_16x16x32_bf16 v[84:87], a[120:123], a[80:83], v[84:87]\n"
"s_add_u32 m0, s44, 71680\n"
"ds_read_b64_tr_b16 a[112:113], v7 offset:16384\n"
"v_mfma_f32_16x16x32_bf16 v[76:79], a[120:123], a[76:79], v[76:79]\n"
"buffer_load_dwordx4 v2, s[8:11], s66 offen lds\n"
"v_mfma_f32_16x16x32_bf16 v[76:79], a[124:127], a[104:107], v[76:79]\n"
"ds_read_b64_tr_b16 a[114:115], v7 offset:16640\n"
"v_mfma_f32_16x16x32_bf16 v[68:71], a[124:127], a[100:103], v[68:71]\n"
"v_mfma_f32_16x16x32_bf16 v[68:71], a[120:123], a[72:75], v[68:71]\n"
"ds_read_b64_tr_b16 a[116:117], v7 offset:20480\n"
"v_mfma_f32_16x16x32_bf16 v[60:63], a[120:123], a[68:71], v[60:63]\n"
"s_sub_u32 s66, s66, s36\n"
"v_mfma_f32_16x16x32_bf16 v[60:63], a[124:127], a[96:99], v[60:63]\n"
"s_add_u32 m0, s44, 70656\n"
"ds_read_b64_tr_b16 a[118:119], v7 offset:20736\n"
"v_mfma_f32_16x16x32_bf16 v[52:55], a[124:127], a[92:95], v[52:55]\n"
"buffer_load_dwordx4 v2, s[8:11], s66 offen lds\n"
"v_mfma_f32_16x16x32_bf16 v[52:55], a[120:123], a[64:67], v[52:55]\n"
"v_mfma_f32_16x16x32_bf16 v[44:47], a[120:123], a[60:63], v[44:47]\n"
"v_mfma_f32_16x16x32_bf16 v[44:47], a[124:127], a[88:91], v[44:47]\n"
"v_mfma_f32_16x16x32_bf16 v[36:39], a[124:127], a[84:87], v[36:39]\n"
"s_sub_u32 s66, s66, s36\n"
"v_mfma_f32_16x16x32_bf16 v[36:39], a[120:123], a[56:59], v[36:39]\n"
"s_add_u32 m0, s44, 69632\n"
"v_mfma_f32_16x16x32_bf16 v[88:91], a[128:131], a[56:59], v[88:91]\n"
"buffer_load_dwordx4 v2, s[8:11], s66 offen lds\n"
"v_mfma_f32_16x16x32_bf16 v[88:91], a[132:135], a[84:87], v[88:91]\n"
"ds_read_b64_tr_b16 a[120:121], v7 offset:16392\n"
"v_mfma_f32_16x16x32_bf16 v[96:99], a[132:135], a[88:91], v[96:99]\n"
"v_mfma_f32_16x16x32_bf16 v[96:99], a[128:131], a[60:63], v[96:99]\n"
"ds_read_b64_tr_b16 a[122:123], v7 offset:16648\n"
"v_mfma_f32_16x16x32_bf16 v[104:107], a[128:131], a[64:67], v[104:107]\n"
"v_mfma_f32_16x16x32_bf16 v[104:107], a[132:135], a[92:95], v[104:107]\n"
"ds_read_b64_tr_b16 a[124:125], v7 offset:20488\n"
"v_mfma_f32_16x16x32_bf16 v[112:115], a[132:135], a[96:99], v[112:115]\n"
"s_sub_u32 s66, s66, s36\n"
"v_mfma_f32_16x16x32_bf16 v[112:115], a[128:131], a[68:71], v[112:115]\n"
"s_add_u32 m0, s44, 68608\n"
"ds_read_b64_tr_b16 a[126:127], v7 offset:20744\n"
"v_mfma_f32_16x16x32_bf16 v[120:123], a[128:131], a[72:75], v[120:123]\n"
"buffer_load_dwordx4 v2, s[8:11], s66 offen lds\n"
"v_mfma_f32_16x16x32_bf16 v[120:123], a[132:135], a[100:103], v[120:123]\n"
"v_mfma_f32_16x16x32_bf16 v[128:131], a[132:135], a[104:107], v[128:131]\n"
"v_mfma_f32_16x16x32_bf16 v[128:131], a[128:131], a[76:79], v[128:131]\n"
"v_mfma_f32_16x16x32_bf16 v[136:139], a[128:131], a[80:83], v[136:139]\n"
"s_xor_b32 s75, s65, 0\n"
"v_mfma_f32_16x16x32_bf16 v[136:139], a[132:135], a[108:111], v[136:139]\n"
"s_xor_b32 m0, s45, 196608\n"
"v_mfma_f32_16x16x32_bf16 v[140:143], a[140:143], a[108:111], v[140:143]\n"
"buffer_load_dwordx4 v3, s[12:15], s75 offen lds\n"
"v_mfma_f32_16x16x32_bf16 v[140:143], a[136:139], a[80:83], v[140:143]\n"
"ds_read_b64_tr_b16 a[128:129], v7 offset:16896\n"
"v_mfma_f32_16x16x32_bf16 v[132:135], a[136:139], a[76:79], v[132:135]\n"
"v_mfma_f32_16x16x32_bf16 v[132:135], a[140:143], a[104:107], v[132:135]\n"
"ds_read_b64_tr_b16 a[130:131], v7 offset:17152\n"
"v_mfma_f32_16x16x32_bf16 v[124:127], a[140:143], a[100:103], v[124:127]\n"
"v_mfma_f32_16x16x32_bf16 v[124:127], a[136:139], a[72:75], v[124:127]\n"
"ds_read_b64_tr_b16 a[132:133], v7 offset:20992\n"
"v_mfma_f32_16x16x32_bf16 v[116:119], a[136:139], a[68:71], v[116:119]\n"
"s_xor_b32 s74, s65, 65536\n"
"v_mfma_f32_16x16x32_bf16 v[116:119], a[140:143], a[96:99], v[116:119]\n"
"s_xor_b32 m0, s45, 197632\n"
"ds_read_b64_tr_b16 a[134:135], v7 offset:21248\n"
"v_mfma_f32_16x16x32_bf16 v[108:111], a[140:143], a[92:95], v[108:111]\n"
"buffer_load_dwordx4 v3, s[12:15], s74 offen lds\n"
"v_mfma_f32_16x16x32_bf16 v[108:111], a[136:139], a[64:67], v[108:111]\n"
"v_mfma_f32_16x16x32_bf16 v[100:103], a[136:139], a[60:63], v[100:103]\n"
"v_mfma_f32_16x16x32_bf16 v[100:103], a[140:143], a[88:91], v[100:103]\n"
"v_mfma_f32_16x16x32_bf16 v[92:95], a[140:143], a[84:87], v[92:95]\n"
"s_xor_b32 s75, s65, 131072\n"
"v_mfma_f32_16x16x32_bf16 v[92:95], a[136:139], a[56:59], v[92:95]\n"
"s_waitcnt lgkmcnt(10)\n"
"s_xor_b32 m0, s45, 198656\n"
"v_mfma_f32_16x16x32_bf16 v[144:147], a[112:115], a[56:59], v[144:147]\n"
"s_waitcnt lgkmcnt(8)\n"
"buffer_load_dwordx4 v3, s[12:15], s75 offen lds\n"
"v_mfma_f32_16x16x32_bf16 v[144:147], a[116:119], a[84:87], v[144:147]\n"
"ds_read_b64_tr_b16 a[136:137], v7 offset:16904\n"
"v_mfma_f32_16x16x32_bf16 v[152:155], a[116:119], a[88:91], v[152:155]\n"
"v_mfma_f32_16x16x32_bf16 v[152:155], a[112:115], a[60:63], v[152:155]\n"
"ds_read_b64_tr_b16 a[138:139], v7 offset:17160\n"
"v_mfma_f32_16x16x32_bf16 v[160:163], a[112:115], a[64:67], v[160:163]\n"
"v_mfma_f32_16x16x32_bf16 v[160:163], a[116:119], a[92:95], v[160:163]\n"
"ds_read_b64_tr_b16 a[140:141], v7 offset:21000\n"
"v_mfma_f32_16x16x32_bf16 v[168:171], a[116:119], a[96:99], v[168:171]\n"
"s_xor_b32 s74, s65, 196608\n"
"v_mfma_f32_16x16x32_bf16 v[168:171], a[112:115], a[68:71], v[168:171]\n"
"s_xor_b32 m0, s45, 199680\n"
"ds_read_b64_tr_b16 a[142:143], v7 offset:21256\n"
"v_mfma_f32_16x16x32_bf16 v[176:179], a[112:115], a[72:75], v[176:179]\n"
"buffer_load_dwordx4 v3, s[12:15], s74 offen lds\n"
"v_mfma_f32_16x16x32_bf16 v[176:179], a[116:119], a[100:103], v[176:179]\n"
"v_mfma_f32_16x16x32_bf16 v[184:187], a[116:119], a[104:107], v[184:187]\n"
"v_mfma_f32_16x16x32_bf16 v[184:187], a[112:115], a[76:79], v[184:187]\n"
"v_mfma_f32_16x16x32_bf16 v[192:195], a[112:115], a[80:83], v[192:195]\n"
"v_xor_b32 v6, 65536, v6\n"
"v_mfma_f32_16x16x32_bf16 v[192:195], a[116:119], a[108:111], v[192:195]\n"
"s_waitcnt lgkmcnt(8)\n"
"v_xor_b32 v7, 196608, v7\n"
"v_mfma_f32_16x16x32_bf16 v[196:199], a[124:127], a[108:111], v[196:199]\n"
"s_waitcnt vmcnt(14) lgkmcnt(0)\n"
"s_barrier\n"
"v_mfma_f32_16x16x32_bf16 v[196:199], a[120:123], a[80:83], v[196:199]\n"
"ds_read_b64_tr_b16 a[112:113], v7 offset:0\n"
"v_mfma_f32_16x16x32_bf16 v[188:191], a[120:123], a[76:79], v[188:191]\n"
"ds_read_b128 a[0:3], v6 offset:0\n"
"v_mfma_f32_16x16x32_bf16 v[188:191], a[124:127], a[104:107], v[188:191]\n"
"ds_read_b64_tr_b16 a[114:115], v7 offset:256\n"
"v_mfma_f32_16x16x32_bf16 v[180:183], a[124:127], a[100:103], v[180:183]\n"
"ds_read_b128 a[28:31], v6 offset:512\n"
"v_mfma_f32_16x16x32_bf16 v[180:183], a[120:123], a[72:75], v[180:183]\n"
"ds_read_b64_tr_b16 a[116:117], v7 offset:4096\n"
"v_mfma_f32_16x16x32_bf16 v[172:175], a[120:123], a[68:71], v[172:175]\n"
"ds_read_b128 a[4:7], v6 offset:2048\n"
"v_mfma_f32_16x16x32_bf16 v[172:175], a[124:127], a[96:99], v[172:175]\n"
"s_sub_u32 s66, s66, s36\n"
"ds_read_b64_tr_b16 a[118:119], v7 offset:4352\n"
"v_mfma_f32_16x16x32_bf16 v[164:167], a[124:127], a[92:95], v[164:167]\n"
"s_add_u32 m0, s44, 67584\n"
"ds_read_b128 a[32:35], v6 offset:2560\n"
"v_mfma_f32_16x16x32_bf16 v[164:167], a[120:123], a[64:67], v[164:167]\n"
"buffer_load_dwordx4 v2, s[8:11], s66 offen lds\n"
"v_mfma_f32_16x16x32_bf16 v[156:159], a[120:123], a[60:63], v[156:159]\n"
"s_sub_u32 s66, s66, s36\n"
"s_add_u32 m0, s44, 66560\n"
"ds_read_b128 a[8:11], v6 offset:4096\n"
"v_mfma_f32_16x16x32_bf16 v[156:159], a[124:127], a[88:91], v[156:159]\n"
"buffer_load_dwordx4 v2, s[8:11], s66 offen lds\n"
"v_mfma_f32_16x16x32_bf16 v[148:151], a[124:127], a[84:87], v[148:151]\n"
"s_sub_u32 s66, s66, s36\n"
"s_add_u32 m0, s44, 65536\n"
"ds_read_b128 a[36:39], v6 offset:4608\n"
"v_mfma_f32_16x16x32_bf16 v[148:151], a[120:123], a[56:59], v[148:151]\n"
"buffer_load_dwordx4 v2, s[8:11], s66 offen lds\n"
"v_mfma_f32_16x16x32_bf16 v[200:203], a[128:131], a[56:59], v[200:203]\n"
"v_mfma_f32_16x16x32_bf16 v[200:203], a[132:135], a[84:87], v[200:203]\n"
"ds_read_b64_tr_b16 a[120:121], v7 offset:8\n"
"v_mfma_f32_16x16x32_bf16 v[208:211], a[132:135], a[88:91], v[208:211]\n"
"ds_read_b128 a[12:15], v6 offset:6144\n"
"v_mfma_f32_16x16x32_bf16 v[208:211], a[128:131], a[60:63], v[208:211]\n"
"ds_read_b64_tr_b16 a[122:123], v7 offset:264\n"
"v_mfma_f32_16x16x32_bf16 v[216:219], a[128:131], a[64:67], v[216:219]\n"
"ds_read_b128 a[40:43], v6 offset:6656\n"
"v_mfma_f32_16x16x32_bf16 v[216:219], a[132:135], a[92:95], v[216:219]\n"
"ds_read_b64_tr_b16 a[124:125], v7 offset:4104\n"
"v_mfma_f32_16x16x32_bf16 v[224:227], a[132:135], a[96:99], v[224:227]\n"
"ds_read_b128 a[16:19], v6 offset:8192\n"
"v_mfma_f32_16x16x32_bf16 v[224:227], a[128:131], a[68:71], v[224:227]\n"
"s_xor_b32 s74, s40, 0\n"
"ds_read_b64_tr_b16 a[126:127], v7 offset:4360\n"
"v_mfma_f32_16x16x32_bf16 v[232:235], a[128:131], a[72:75], v[232:235]\n"
"s_add_u32 m0, s72, 0\n"
"ds_read_b128 a[44:47], v6 offset:8704\n"
"v_mfma_f32_16x16x32_bf16 v[232:235], a[132:135], a[100:103], v[232:235]\n"
"buffer_load_dwordx4 v4, s[16:19], s74 offen offset:2048 nt lds\n"
"v_mfma_f32_16x16x32_bf16 v[240:243], a[132:135], a[104:107], v[240:243]\n"
"ds_read_b128 a[20:23], v6 offset:10240\n"
"v_mfma_f32_16x16x32_bf16 v[240:243], a[128:131], a[76:79], v[240:243]\n"
"buffer_load_dwordx4 v5, s[16:19], s74 offen offset:3072 nt lds\n"
"v_mfma_f32_16x16x32_bf16 v[248:251], a[128:131], a[80:83], v[248:251]\n"
"ds_read_b128 a[48:51], v6 offset:10752\n"
"v_mfma_f32_16x16x32_bf16 v[248:251], a[132:135], a[108:111], v[248:251]\n"
"v_mfma_f32_16x16x32_bf16 v[252:255], a[140:143], a[108:111], v[252:255]\n"
"v_mfma_f32_16x16x32_bf16 v[252:255], a[136:139], a[80:83], v[252:255]\n"
"ds_read_b64_tr_b16 a[128:129], v7 offset:512\n"
"v_mfma_f32_16x16x32_bf16 v[244:247], a[136:139], a[76:79], v[244:247]\n"
"ds_read_b128 a[24:27], v6 offset:12288\n"
"v_mfma_f32_16x16x32_bf16 v[244:247], a[140:143], a[104:107], v[244:247]\n"
"ds_read_b64_tr_b16 a[130:131], v7 offset:768\n"
"v_mfma_f32_16x16x32_bf16 v[236:239], a[140:143], a[100:103], v[236:239]\n"
"s_xor_b32 s75, s65, 128\n"
"ds_read_b128 a[52:55], v6 offset:12800\n"
"v_mfma_f32_16x16x32_bf16 v[236:239], a[136:139], a[72:75], v[236:239]\n"
"s_xor_b32 m0, s45, 212992\n"
"ds_read_b64_tr_b16 a[132:133], v7 offset:4608\n"
"v_mfma_f32_16x16x32_bf16 v[228:231], a[136:139], a[68:71], v[228:231]\n"
"buffer_load_dwordx4 v3, s[12:15], s75 offen lds\n"
"v_mfma_f32_16x16x32_bf16 v[228:231], a[140:143], a[96:99], v[228:231]\n"
"s_xor_b32 s74, s65, 65664\n"
"ds_read_b64_tr_b16 a[134:135], v7 offset:4864\n"
"v_mfma_f32_16x16x32_bf16 v[220:223], a[140:143], a[92:95], v[220:223]\n"
"s_xor_b32 m0, s45, 214016\n"
"v_mfma_f32_16x16x32_bf16 v[220:223], a[136:139], a[64:67], v[220:223]\n"
"buffer_load_dwordx4 v3, s[12:15], s74 offen lds\n"
"v_mfma_f32_16x16x32_bf16 v[212:215], a[136:139], a[60:63], v[212:215]\n"
"v_mfma_f32_16x16x32_bf16 v[212:215], a[140:143], a[88:91], v[212:215]\n"
"v_mfma_f32_16x16x32_bf16 v[204:207], a[140:143], a[84:87], v[204:207]\n"
"v_mfma_f32_16x16x32_bf16 v[204:207], a[136:139], a[56:59], v[204:207]\n"
"s_waitcnt lgkmcnt(15)\n"
"v_mfma_f32_16x16x32_bf16 v[32:35], a[112:115], a[0:3], v[32:35]\n"
"s_waitcnt lgkmcnt(15)\n"
"s_xor_b32 s75, s65, 131200\n"
"v_mfma_f32_16x16x32_bf16 v[32:35], a[116:119], a[28:31], v[32:35]\n"
"s_waitcnt lgkmcnt(15)\n"
"s_xor_b32 m0, s45, 215040\n"
"ds_read_b64_tr_b16 a[136:137], v7 offset:520\n"
"v_mfma_f32_16x16x32_bf16 v[40:43], a[116:119], a[32:35], v[40:43]\n"
"buffer_load_dwordx4 v3, s[12:15], s75 offen lds\n"
"v_mfma_f32_16x16x32_bf16 v[40:43], a[112:115], a[4:7], v[40:43]\n"
"s_waitcnt lgkmcnt(15)\n"
"ds_read_b64_tr_b16 a[138:139], v7 offset:776\n"
"v_mfma_f32_16x16x32_bf16 v[48:51], a[112:115], a[8:11], v[48:51]\n"
"s_waitcnt lgkmcnt(15)\n"
"v_mfma_f32_16x16x32_bf16 v[48:51], a[116:119], a[36:39], v[48:51]\n"
"s_waitcnt lgkmcnt(14)\n"
"ds_read_b64_tr_b16 a[140:141], v7 offset:4616\n"
"v_mfma_f32_16x16x32_bf16 v[56:59], a[116:119], a[40:43], v[56:59]\n"
"s_xor_b32 s74, s65, 196736\n"
"v_mfma_f32_16x16x32_bf16 v[56:59], a[112:115], a[12:15], v[56:59]\n"
"s_waitcnt lgkmcnt(13)\n"
"s_xor_b32 m0, s45, 216064\n"
"ds_read_b64_tr_b16 a[142:143], v7 offset:4872\n"
"v_mfma_f32_16x16x32_bf16 v[64:67], a[112:115], a[16:19], v[64:67]\n"
"s_waitcnt lgkmcnt(12)\n"
"buffer_load_dwordx4 v3, s[12:15], s74 offen lds\n"
"v_mfma_f32_16x16x32_bf16 v[64:67], a[116:119], a[44:47], v[64:67]\n"
"s_waitcnt lgkmcnt(10)\n"
"v_mfma_f32_16x16x32_bf16 v[72:75], a[116:119], a[48:51], v[72:75]\n"
"v_mfma_f32_16x16x32_bf16 v[72:75], a[112:115], a[20:23], v[72:75]\n"
"s_waitcnt lgkmcnt(8)\n"
"v_mfma_f32_16x16x32_bf16 v[80:83], a[112:115], a[24:27], v[80:83]\n"
"s_waitcnt lgkmcnt(6)\n"
"v_mfma_f32_16x16x32_bf16 v[80:83], a[116:119], a[52:55], v[80:83]\n"
"v_mfma_f32_16x16x32_bf16 v[84:87], a[124:127], a[52:55], v[84:87]\n"
"s_waitcnt vmcnt(17) lgkmcnt(0)\n"
"s_barrier\n"
"s_add_u32 s37, s37, 128\n"
"s_cmp_eq_u32 s37, 1024\n"
"s_cselect_b32 s37, 256, s37\n"
"s_lshl_b32 s65, s37, 12\n"
"s_andn2_b32 s66, s66, 896\n"
"s_or_b32 s66, s66, s37\n"
"v_mfma_f32_16x16x32_bf16 v[84:87], a[120:123], a[24:27], v[84:87]\n"
"s_add_u32 m0, s44, 0\n"
"ds_read_b64_tr_b16 a[112:113], v7 offset:16384\n"
"v_mfma_f32_16x16x32_bf16 v[76:79], a[120:123], a[20:23], v[76:79]\n"
"buffer_load_dwordx4 v2, s[8:11], s66 offen lds\n"
"v_mfma_f32_16x16x32_bf16 v[76:79], a[124:127], a[48:51], v[76:79]\n"
"ds_read_b64_tr_b16 a[114:115], v7 offset:16640\n"
"v_mfma_f32_16x16x32_bf16 v[68:71], a[124:127], a[44:47], v[68:71]\n"
"v_mfma_f32_16x16x32_bf16 v[68:71], a[120:123], a[16:19], v[68:71]\n"
"ds_read_b64_tr_b16 a[116:117], v7 offset:20480\n"
"v_mfma_f32_16x16x32_bf16 v[60:63], a[120:123], a[12:15], v[60:63]\n"
"s_add_u32 s66, s66, s36\n"
"v_mfma_f32_16x16x32_bf16 v[60:63], a[124:127], a[40:43], v[60:63]\n"
"s_add_u32 m0, s44, 1024\n"
"ds_read_b64_tr_b16 a[118:119], v7 offset:20736\n"
"v_mfma_f32_16x16x32_bf16 v[52:55], a[124:127], a[36:39], v[52:55]\n"
"buffer_load_dwordx4 v2, s[8:11], s66 offen lds\n"
"v_mfma_f32_16x16x32_bf16 v[52:55], a[120:123], a[8:11], v[52:55]\n"
"v_mfma_f32_16x16x32_bf16 v[44:47], a[120:123], a[4:7], v[44:47]\n"
"v_mfma_f32_16x16x32_bf16 v[44:47], a[124:127], a[32:35], v[44:47]\n"
"v_mfma_f32_16x16x32_bf16 v[36:39], a[124:127], a[28:31], v[36:39]\n"
"s_add_u32 s66, s66, s36\n"
"v_mfma_f32_16x16x32_bf16 v[36:39], a[120:123], a[0:3], v[36:39]\n"
"s_add_u32 m0, s44, 2048\n"
"v_mfma_f32_16x16x32_bf16 v[88:91], a[128:131], a[0:3], v[88:91]\n"
"buffer_load_dwordx4 v2, s[8:11], s66 offen lds\n"
"v_mfma_f32_16x16x32_bf16 v[88:91], a[132:135], a[28:31], v[88:91]\n"
"ds_read_b64_tr_b16 a[120:121], v7 offset:16392\n"
"v_mfma_f32_16x16x32_bf16 v[96:99], a[132:135], a[32:35], v[96:99]\n"
"v_mfma_f32_16x16x32_bf16 v[96:99], a[128:131], a[4:7], v[96:99]\n"
"ds_read_b64_tr_b16 a[122:123], v7 offset:16648\n"
"v_mfma_f32_16x16x32_bf16 v[104:107], a[128:131], a[8:11], v[104:107]\n"
"v_mfma_f32_16x16x32_bf16 v[104:107], a[132:135], a[36:39], v[104:107]\n"
"ds_read_b64_tr_b16 a[124:125], v7 offset:20488\n"
"v_mfma_f32_16x16x32_bf16 v[112:115], a[132:135], a[40:43], v[112:115]\n"
"s_add_u32 s66, s66, s36\n"
"v_mfma_f32_16x16x32_bf16 v[112:115], a[128:131], a[12:15], v[112:115]\n"
"s_add_u32 m0, s44, 3072\n"
"ds_read_b64_tr_b16 a[126:127], v7 offset:20744\n"
"v_mfma_f32_16x16x32_bf16 v[120:123], a[128:131], a[16:19], v[120:123]\n"
"buffer_load_dwordx4 v2, s[8:11], s66 offen lds\n"
"v_mfma_f32_16x16x32_bf16 v[120:123], a[132:135], a[44:47], v[120:123]\n"
"v_mfma_f32_16x16x32_bf16 v[128:131], a[132:135], a[48:51], v[128:131]\n"
"v_mfma_f32_16x16x32_bf16 v[128:131], a[128:131], a[20:23], v[128:131]\n"
"v_mfma_f32_16x16x32_bf16 v[136:139], a[128:131], a[24:27], v[136:139]\n"
"s_xor_b32 s74, s65, 0\n"
"v_mfma_f32_16x16x32_bf16 v[136:139], a[132:135], a[52:55], v[136:139]\n"
"s_xor_b32 m0, s45, 0\n"
"v_mfma_f32_16x16x32_bf16 v[140:143], a[140:143], a[52:55], v[140:143]\n"
"buffer_load_dwordx4 v3, s[12:15], s74 offen lds\n"
"v_mfma_f32_16x16x32_bf16 v[140:143], a[136:139], a[24:27], v[140:143]\n"
"ds_read_b64_tr_b16 a[128:129], v7 offset:16896\n"
"v_mfma_f32_16x16x32_bf16 v[132:135], a[136:139], a[20:23], v[132:135]\n"
"v_mfma_f32_16x16x32_bf16 v[132:135], a[140:143], a[48:51], v[132:135]\n"
"ds_read_b64_tr_b16 a[130:131], v7 offset:17152\n"
"v_mfma_f32_16x16x32_bf16 v[124:127], a[140:143], a[44:47], v[124:127]\n"
"v_mfma_f32_16x16x32_bf16 v[124:127], a[136:139], a[16:19], v[124:127]\n"
"ds_read_b64_tr_b16 a[132:133], v7 offset:20992\n"
"v_mfma_f32_16x16x32_bf16 v[116:119], a[136:139], a[12:15], v[116:119]\n"
"s_xor_b32 s75, s65, 65536\n"
"v_mfma_f32_16x16x32_bf16 v[116:119], a[140:143], a[40:43], v[116:119]\n"
"s_xor_b32 m0, s45, 1024\n"
"ds_read_b64_tr_b16 a[134:135], v7 offset:21248\n"
"v_mfma_f32_16x16x32_bf16 v[108:111], a[140:143], a[36:39], v[108:111]\n"
"buffer_load_dwordx4 v3, s[12:15], s75 offen lds\n"
"v_mfma_f32_16x16x32_bf16 v[108:111], a[136:139], a[8:11], v[108:111]\n"
"v_mfma_f32_16x16x32_bf16 v[100:103], a[136:139], a[4:7], v[100:103]\n"
"v_mfma_f32_16x16x32_bf16 v[100:103], a[140:143], a[32:35], v[100:103]\n"
"v_mfma_f32_16x16x32_bf16 v[92:95], a[140:143], a[28:31], v[92:95]\n"
"s_xor_b32 s74, s65, 131072\n"
"v_mfma_f32_16x16x32_bf16 v[92:95], a[136:139], a[0:3], v[92:95]\n"
"s_waitcnt lgkmcnt(10)\n"
"s_xor_b32 m0, s45, 2048\n"
"v_mfma_f32_16x16x32_bf16 v[144:147], a[112:115], a[0:3], v[144:147]\n"
"s_waitcnt lgkmcnt(8)\n"
"buffer_load_dwordx4 v3, s[12:15], s74 offen lds\n"
"v_mfma_f32_16x16x32_bf16 v[144:147], a[116:119], a[28:31], v[144:147]\n"
"ds_read_b64_tr_b16 a[136:137], v7 offset:16904\n"
"v_mfma_f32_16x16x32_bf16 v[152:155], a[116:119], a[32:35], v[152:155]\n"
"v_mfma_f32_16x16x32_bf16 v[152:155], a[112:115], a[4:7], v[152:155]\n"
"ds_read_b64_tr_b16 a[138:139], v7 offset:17160\n"
"v_mfma_f32_16x16x32_bf16 v[160:163], a[112:115], a[8:11], v[160:163]\n"
"v_mfma_f32_16x16x32_bf16 v[160:163], a[116:119], a[36:39], v[160:163]\n"
"ds_read_b64_tr_b16 a[140:141], v7 offset:21000\n"
"v_mfma_f32_16x16x32_bf16 v[168:171], a[116:119], a[40:43], v[168:171]\n"
"s_xor_b32 s75, s65, 196608\n"
"v_mfma_f32_16x16x32_bf16 v[168:171], a[112:115], a[12:15], v[168:171]\n"
"s_xor_b32 m0, s45, 3072\n"
"ds_read_b64_tr_b16 a[142:143], v7 offset:21256\n"
"v_mfma_f32_16x16x32_bf16 v[176:179], a[112:115], a[16:19], v[176:179]\n"
"buffer_load_dwordx4 v3, s[12:15], s75 offen lds\n"
"v_mfma_f32_16x16x32_bf16 v[176:179], a[116:119], a[44:47], v[176:179]\n"
"v_mfma_f32_16x16x32_bf16 v[184:187], a[116:119], a[48:51], v[184:187]\n"
"v_mfma_f32_16x16x32_bf16 v[184:187], a[112:115], a[20:23], v[184:187]\n"
"v_mfma_f32_16x16x32_bf16 v[192:195], a[112:115], a[24:27], v[192:195]\n"
"v_xor_b32 v6, 65536, v6\n"
"v_mfma_f32_16x16x32_bf16 v[192:195], a[116:119], a[52:55], v[192:195]\n"
"s_waitcnt lgkmcnt(8)\n"
"v_xor_b32 v7, 196608, v7\n"
"v_mfma_f32_16x16x32_bf16 v[196:199], a[124:127], a[52:55], v[196:199]\n"
"s_waitcnt vmcnt(14) lgkmcnt(0)\n"
"s_barrier\n"
"v_mfma_f32_16x16x32_bf16 v[196:199], a[120:123], a[24:27], v[196:199]\n"
"s_mov_b32 s67, 0\n"
"ds_read_b64_tr_b16 a[112:113], v7 offset:0\n"
"v_mfma_f32_16x16x32_bf16 v[188:191], a[120:123], a[20:23], v[188:191]\n"
"s_load_dword s67, s[20:21], s42 glc\n"
"ds_read_b128 a[56:59], v6 offset:0\n"
"v_mfma_f32_16x16x32_bf16 v[188:191], a[124:127], a[48:51], v[188:191]\n"
"ds_read_b64_tr_b16 a[114:115], v7 offset:256\n"
"v_mfma_f32_16x16x32_bf16 v[180:183], a[124:127], a[44:47], v[180:183]\n"
"ds_read_b128 a[84:87], v6 offset:512\n"
"v_mfma_f32_16x16x32_bf16 v[180:183], a[120:123], a[16:19], v[180:183]\n"
"s_xor_b32 s42, s42, 8192\n"
"ds_read_b64_tr_b16 a[116:117], v7 offset:4096\n"
"v_mfma_f32_16x16x32_bf16 v[172:175], a[120:123], a[12:15], v[172:175]\n"
"ds_read_b128 a[60:63], v6 offset:2048\n"
"v_mfma_f32_16x16x32_bf16 v[172:175], a[124:127], a[40:43], v[172:175]\n"
"s_add_u32 s66, s66, s36\n"
"ds_read_b64_tr_b16 a[118:119], v7 offset:4352\n"
"v_mfma_f32_16x16x32_bf16 v[164:167], a[124:127], a[36:39], v[164:167]\n"
"s_add_u32 m0, s44, 4096\n"
"ds_read_b128 a[88:91], v6 offset:2560\n"
"v_mfma_f32_16x16x32_bf16 v[164:167], a[120:123], a[8:11], v[164:167]\n"
"buffer_load_dwordx4 v2, s[8:11], s66 offen lds\n"
"v_mfma_f32_16x16x32_bf16 v[156:159], a[120:123], a[4:7], v[156:159]\n"
"s_add_u32 s66, s66, s36\n"
"s_add_u32 m0, s44, 5120\n"
"ds_read_b128 a[64:67], v6 offset:4096\n"
"v_mfma_f32_16x16x32_bf16 v[156:159], a[124:127], a[32:35], v[156:159]\n"
"buffer_load_dwordx4 v2, s[8:11], s66 offen lds\n"
"v_mfma_f32_16x16x32_bf16 v[148:151], a[124:127], a[28:31], v[148:151]\n"
"s_add_u32 s66, s66, s36\n"
"s_add_u32 m0, s44, 6144\n"
"ds_read_b128 a[92:95], v6 offset:4608\n"
"v_mfma_f32_16x16x32_bf16 v[148:151], a[120:123], a[0:3], v[148:151]\n"
"buffer_load_dwordx4 v2, s[8:11], s66 offen lds\n"
"v_mfma_f32_16x16x32_bf16 v[200:203], a[128:131], a[0:3], v[200:203]\n"
"v_mfma_f32_16x16x32_bf16 v[200:203], a[132:135], a[28:31], v[200:203]\n"
"ds_read_b64_tr_b16 a[120:121], v7 offset:8\n"
"v_mfma_f32_16x16x32_bf16 v[208:211], a[132:135], a[32:35], v[208:211]\n"
"ds_read_b128 a[68:71], v6 offset:6144\n"
"v_mfma_f32_16x16x32_bf16 v[208:211], a[128:131], a[4:7], v[208:211]\n"
"s_add_u32 s70, s70, s67\n"
"ds_read_b64_tr_b16 a[122:123], v7 offset:264\n"
"v_mfma_f32_16x16x32_bf16 v[216:219], a[128:131], a[8:11], v[216:219]\n"
"ds_read_b128 a[96:99], v6 offset:6656\n"
"v_mfma_f32_16x16x32_bf16 v[216:219], a[132:135], a[36:39], v[216:219]\n"
"ds_read_b64_tr_b16 a[124:125], v7 offset:4104\n"
"v_mfma_f32_16x16x32_bf16 v[224:227], a[132:135], a[40:43], v[224:227]\n"
"ds_read_b128 a[72:75], v6 offset:8192\n"
"v_mfma_f32_16x16x32_bf16 v[224:227], a[128:131], a[12:15], v[224:227]\n"
"s_xor_b32 s75, s40, 4096\n"
"ds_read_b64_tr_b16 a[126:127], v7 offset:4360\n"
"v_mfma_f32_16x16x32_bf16 v[232:235], a[128:131], a[16:19], v[232:235]\n"
"s_add_u32 m0, s72, 4096\n"
"ds_read_b128 a[100:103], v6 offset:8704\n"
"v_mfma_f32_16x16x32_bf16 v[232:235], a[132:135], a[44:47], v[232:235]\n"
"s_add_u32 s70, s70, s67\n"
"buffer_load_dwordx4 v4, s[16:19], s75 offen nt lds\n"
"v_mfma_f32_16x16x32_bf16 v[240:243], a[132:135], a[48:51], v[240:243]\n"
"ds_read_b128 a[76:79], v6 offset:10240\n"
"v_mfma_f32_16x16x32_bf16 v[240:243], a[128:131], a[20:23], v[240:243]\n"
"s_add_u32 s70, s70, s67\n"
"buffer_load_dwordx4 v5, s[16:19], s75 offen offset:1024 nt lds\n"
"v_mfma_f32_16x16x32_bf16 v[248:251], a[128:131], a[24:27], v[248:251]\n"
"ds_read_b128 a[104:107], v6 offset:10752\n"
"v_mfma_f32_16x16x32_bf16 v[248:251], a[132:135], a[52:55], v[248:251]\n"
"v_mfma_f32_16x16x32_bf16 v[252:255], a[140:143], a[52:55], v[252:255]\n"
"v_mfma_f32_16x16x32_bf16 v[252:255], a[136:139], a[24:27], v[252:255]\n"
"s_add_u32 s70, s70, s67\n"
"ds_read_b64_tr_b16 a[128:129], v7 offset:512\n"
"v_mfma_f32_16x16x32_bf16 v[244:247], a[136:139], a[20:23], v[244:247]\n"
"ds_read_b128 a[80:83], v6 offset:12288\n"
"v_mfma_f32_16x16x32_bf16 v[244:247], a[140:143], a[48:51], v[244:247]\n"
"ds_read_b64_tr_b16 a[130:131], v7 offset:768\n"
"v_mfma_f32_16x16x32_bf16 v[236:239], a[140:143], a[44:47], v[236:239]\n"
"s_xor_b32 s74, s65, 128\n"
"ds_read_b128 a[108:111], v6 offset:12800\n"
"v_mfma_f32_16x16x32_bf16 v[236:239], a[136:139], a[16:19], v[236:239]\n"
"s_xor_b32 m0, s45, 16384\n"
"ds_read_b64_tr_b16 a[132:133], v7 offset:4608\n"
"v_mfma_f32_16x16x32_bf16 v[228:231], a[136:139], a[12:15], v[228:231]\n"
"s_add_u32 s70, s70, s67\n"
"buffer_load_dwordx4 v3, s[12:15], s74 offen lds\n"
"v_mfma_f32_16x16x32_bf16 v[228:231], a[140:143], a[40:43], v[228:231]\n"
"s_xor_b32 s75, s65, 65664\n"
"ds_read_b64_tr_b16 a[134:135], v7 offset:4864\n"
"v_mfma_f32_16x16x32_bf16 v[220:223], a[140:143], a[36:39], v[220:223]\n"
"s_xor_b32 m0, s45, 17408\n"
"v_mfma_f32_16x16x32_bf16 v[220:223], a[136:139], a[8:11], v[220:223]\n"
"s_add_u32 s70, s70, s67\n"
"buffer_load_dwordx4 v3, s[12:15], s75 offen lds\n"
"v_mfma_f32_16x16x32_bf16 v[212:215], a[136:139], a[4:7], v[212:215]\n"
"v_mfma_f32_16x16x32_bf16 v[212:215], a[140:143], a[32:35], v[212:215]\n"
"v_mfma_f32_16x16x32_bf16 v[204:207], a[140:143], a[28:31], v[204:207]\n"
"v_mfma_f32_16x16x32_bf16 v[204:207], a[136:139], a[0:3], v[204:207]\n"
"s_waitcnt lgkmcnt(15)\n"
"s_add_u32 s70, s70, s67\n"
"v_mfma_f32_16x16x32_bf16 v[32:35], a[112:115], a[56:59], v[32:35]\n"
"s_waitcnt lgkmcnt(15)\n"
"s_xor_b32 s74, s65, 131200\n"
"v_mfma_f32_16x16x32_bf16 v[32:35], a[116:119], a[84:87], v[32:35]\n"
"s_waitcnt lgkmcnt(15)\n"
"s_xor_b32 m0, s45, 18432\n"
"ds_read_b64_tr_b16 a[136:137], v7 offset:520\n"
"v_mfma_f32_16x16x32_bf16 v[40:43], a[116:119], a[88:91], v[40:43]\n"
"buffer_load_dwordx4 v3, s[12:15], s74 offen lds\n"
"v_mfma_f32_16x16x32_bf16 v[40:43], a[112:115], a[60:63], v[40:43]\n"
"s_waitcnt lgkmcnt(15)\n"
"s_add_u32 s70, s70, s67\n"
"ds_read_b64_tr_b16 a[138:139], v7 offset:776\n"
"v_mfma_f32_16x16x32_bf16 v[48:51], a[112:115], a[64:67], v[48:51]\n"
"s_waitcnt lgkmcnt(15)\n"
"v_mfma_f32_16x16x32_bf16 v[48:51], a[116:119], a[92:95], v[48:51]\n"
"s_waitcnt lgkmcnt(14)\n"
"ds_read_b64_tr_b16 a[140:141], v7 offset:4616\n"
"v_mfma_f32_16x16x32_bf16 v[56:59], a[116:119], a[96:99], v[56:59]\n"
"s_xor_b32 s75, s65, 196736\n"
"v_mfma_f32_16x16x32_bf16 v[56:59], a[112:115], a[68:71], v[56:59]\n"
"s_waitcnt lgkmcnt(13)\n"
"s_xor_b32 m0, s45, 19456\n"
"ds_read_b64_tr_b16 a[142:143], v7 offset:4872\n"
"v_mfma_f32_16x16x32_bf16 v[64:67], a[112:115], a[72:75], v[64:67]\n"
"s_waitcnt lgkmcnt(12)\n"
"s_add_u32 s70, s70, s67\n"
"buffer_load_dwordx4 v3, s[12:15], s75 offen lds\n"
"v_mfma_f32_16x16x32_bf16 v[64:67], a[116:119], a[100:103], v[64:67]\n"
"s_waitcnt lgkmcnt(10)\n"
"v_mfma_f32_16x16x32_bf16 v[72:75], a[116:119], a[104:107], v[72:75]\n"
"v_mfma_f32_16x16x32_bf16 v[72:75], a[112:115], a[76:79], v[72:75]\n"
"s_waitcnt lgkmcnt(8)\n"
"v_mfma_f32_16x16x32_bf16 v[80:83], a[112:115], a[80:83], v[80:83]\n"
"s_waitcnt lgkmcnt(6)\n"
"v_mfma_f32_16x16x32_bf16 v[80:83], a[116:119], a[108:111], v[80:83]\n"
"v_mfma_f32_16x16x32_bf16 v[84:87], a[124:127], a[108:111], v[84:87]\n"
"s_waitcnt vmcnt(17) lgkmcnt(0)\n"
"s_barrier\n"
"s_add_u32 s37, s37, 128\n"
"s_cmp_eq_u32 s37, 1024\n"
"s_cselect_b32 s37, 256, s37\n"
"s_lshl_b32 s65, s37, 12\n"
"s_andn2_b32 s66, s66, 896\n"
"s_or_b32 s66, s66, s37\n"
"v_mfma_f32_16x16x32_bf16 v[84:87], a[120:123], a[80:83], v[84:87]\n"
"s_add_u32 m0, s44, 71680\n"
"ds_read_b64_tr_b16 a[112:113], v7 offset:16384\n"
"v_mfma_f32_16x16x32_bf16 v[76:79], a[120:123], a[76:79], v[76:79]\n"
"buffer_load_dwordx4 v2, s[8:11], s66 offen lds\n"
"v_mfma_f32_16x16x32_bf16 v[76:79], a[124:127], a[104:107], v[76:79]\n"
"ds_read_b64_tr_b16 a[114:115], v7 offset:16640\n"
"v_mfma_f32_16x16x32_bf16 v[68:71], a[124:127], a[100:103], v[68:71]\n"
"v_mfma_f32_16x16x32_bf16 v[68:71], a[120:123], a[72:75], v[68:71]\n"
"ds_read_b64_tr_b16 a[116:117], v7 offset:20480\n"
"v_mfma_f32_16x16x32_bf16 v[60:63], a[120:123], a[68:71], v[60:63]\n"
"s_sub_u32 s66, s66, s36\n"
"v_mfma_f32_16x16x32_bf16 v[60:63], a[124:127], a[96:99], v[60:63]\n"
"s_add_u32 m0, s44, 70656\n"
"ds_read_b64_tr_b16 a[118:119], v7 offset:20736\n"
"v_mfma_f32_16x16x32_bf16 v[52:55], a[124:127], a[92:95], v[52:55]\n"
"buffer_load_dwordx4 v2, s[8:11], s66 offen lds\n"
"v_mfma_f32_16x16x32_bf16 v[52:55], a[120:123], a[64:67], v[52:55]\n"
"v_mfma_f32_16x16x32_bf16 v[44:47], a[120:123], a[60:63], v[44:47]\n"
"v_mfma_f32_16x16x32_bf16 v[44:47], a[124:127], a[88:91], v[44:47]\n"
"v_mfma_f32_16x16x32_bf16 v[36:39], a[124:127], a[84:87], v[36:39]\n"
"s_sub_u32 s66, s66, s36\n"
"v_mfma_f32_16x16x32_bf16 v[36:39], a[120:123], a[56:59], v[36:39]\n"
"s_add_u32 m0, s44, 69632\n"
"v_mfma_f32_16x16x32_bf16 v[88:91], a[128:131], a[56:59], v[88:91]\n"
"buffer_load_dwordx4 v2, s[8:11], s66 offen lds\n"
"v_mfma_f32_16x16x32_bf16 v[88:91], a[132:135], a[84:87], v[88:91]\n"
"ds_read_b64_tr_b16 a[120:121], v7 offset:16392\n"
"v_mfma_f32_16x16x32_bf16 v[96:99], a[132:135], a[88:91], v[96:99]\n"
"v_mfma_f32_16x16x32_bf16 v[96:99], a[128:131], a[60:63], v[96:99]\n"
"ds_read_b64_tr_b16 a[122:123], v7 offset:16648\n"
"v_mfma_f32_16x16x32_bf16 v[104:107], a[128:131], a[64:67], v[104:107]\n"
"v_mfma_f32_16x16x32_bf16 v[104:107], a[132:135], a[92:95], v[104:107]\n"
"ds_read_b64_tr_b16 a[124:125], v7 offset:20488\n"
"v_mfma_f32_16x16x32_bf16 v[112:115], a[132:135], a[96:99], v[112:115]\n"
"s_sub_u32 s66, s66, s36\n"
"v_mfma_f32_16x16x32_bf16 v[112:115], a[128:131], a[68:71], v[112:115]\n"
"s_add_u32 m0, s44, 68608\n"
"ds_read_b64_tr_b16 a[126:127], v7 offset:20744\n"
"v_mfma_f32_16x16x32_bf16 v[120:123], a[128:131], a[72:75], v[120:123]\n"
"buffer_load_dwordx4 v2, s[8:11], s66 offen lds\n"
"v_mfma_f32_16x16x32_bf16 v[120:123], a[132:135], a[100:103], v[120:123]\n"
"v_mfma_f32_16x16x32_bf16 v[128:131], a[132:135], a[104:107], v[128:131]\n"
"v_mfma_f32_16x16x32_bf16 v[128:131], a[128:131], a[76:79], v[128:131]\n"
"v_mfma_f32_16x16x32_bf16 v[136:139], a[128:131], a[80:83], v[136:139]\n"
"s_xor_b32 s75, s65, 0\n"
"v_mfma_f32_16x16x32_bf16 v[136:139], a[132:135], a[108:111], v[136:139]\n"
"s_xor_b32 m0, s45, 196608\n"
"v_mfma_f32_16x16x32_bf16 v[140:143], a[140:143], a[108:111], v[140:143]\n"
"buffer_load_dwordx4 v3, s[12:15], s75 offen lds\n"
"v_mfma_f32_16x16x32_bf16 v[140:143], a[136:139], a[80:83], v[140:143]\n"
"ds_read_b64_tr_b16 a[128:129], v7 offset:16896\n"
"v_mfma_f32_16x16x32_bf16 v[132:135], a[136:139], a[76:79], v[132:135]\n"
"v_mfma_f32_16x16x32_bf16 v[132:135], a[140:143], a[104:107], v[132:135]\n"
"ds_read_b64_tr_b16 a[130:131], v7 offset:17152\n"
"v_mfma_f32_16x16x32_bf16 v[124:127], a[140:143], a[100:103], v[124:127]\n"
"v_mfma_f32_16x16x32_bf16 v[124:127], a[136:139], a[72:75], v[124:127]\n"
"ds_read_b64_tr_b16 a[132:133], v7 offset:20992\n"
"v_mfma_f32_16x16x32_bf16 v[116:119], a[136:139], a[68:71], v[116:119]\n"
"s_xor_b32 s74, s65, 65536\n"
"v_mfma_f32_16x16x32_bf16 v[116:119], a[140:143], a[96:99], v[116:119]\n"
"s_xor_b32 m0, s45, 197632\n"
"ds_read_b64_tr_b16 a[134:135], v7 offset:21248\n"
"v_mfma_f32_16x16x32_bf16 v[108:111], a[140:143], a[92:95], v[108:111]\n"
"buffer_load_dwordx4 v3, s[12:15], s74 offen lds\n"
"v_mfma_f32_16x16x32_bf16 v[108:111], a[136:139], a[64:67], v[108:111]\n"
"v_mfma_f32_16x16x32_bf16 v[100:103], a[136:139], a[60:63], v[100:103]\n"
"v_mfma_f32_16x16x32_bf16 v[100:103], a[140:143], a[88:91], v[100:103]\n"
"v_mfma_f32_16x16x32_bf16 v[92:95], a[140:143], a[84:87], v[92:95]\n"
"s_xor_b32 s75, s65, 131072\n"
"v_mfma_f32_16x16x32_bf16 v[92:95], a[136:139], a[56:59], v[92:95]\n"
"s_waitcnt lgkmcnt(10)\n"
"s_xor_b32 m0, s45, 198656\n"
"v_mfma_f32_16x16x32_bf16 v[144:147], a[112:115], a[56:59], v[144:147]\n"
"s_waitcnt lgkmcnt(8)\n"
"buffer_load_dwordx4 v3, s[12:15], s75 offen lds\n"
"v_mfma_f32_16x16x32_bf16 v[144:147], a[116:119], a[84:87], v[144:147]\n"
"ds_read_b64_tr_b16 a[136:137], v7 offset:16904\n"
"v_mfma_f32_16x16x32_bf16 v[152:155], a[116:119], a[88:91], v[152:155]\n"
"v_mfma_f32_16x16x32_bf16 v[152:155], a[112:115], a[60:63], v[152:155]\n"
"ds_read_b64_tr_b16 a[138:139], v7 offset:17160\n"
"v_mfma_f32_16x16x32_bf16 v[160:163], a[112:115], a[64:67], v[160:163]\n"
"v_mfma_f32_16x16x32_bf16 v[160:163], a[116:119], a[92:95], v[160:163]\n"
"ds_read_b64_tr_b16 a[140:141], v7 offset:21000\n"
"v_mfma_f32_16x16x32_bf16 v[168:171], a[116:119], a[96:99], v[168:171]\n"
"s_xor_b32 s74, s65, 196608\n"
"v_mfma_f32_16x16x32_bf16 v[168:171], a[112:115], a[68:71], v[168:171]\n"
"s_xor_b32 m0, s45, 199680\n"
"ds_read_b64_tr_b16 a[142:143], v7 offset:21256\n"
"v_mfma_f32_16x16x32_bf16 v[176:179], a[112:115], a[72:75], v[176:179]\n"
"buffer_load_dwordx4 v3, s[12:15], s74 offen lds\n"
"v_mfma_f32_16x16x32_bf16 v[176:179], a[116:119], a[100:103], v[176:179]\n"
"v_mfma_f32_16x16x32_bf16 v[184:187], a[116:119], a[104:107], v[184:187]\n"
"v_mfma_f32_16x16x32_bf16 v[184:187], a[112:115], a[76:79], v[184:187]\n"
"v_mfma_f32_16x16x32_bf16 v[192:195], a[112:115], a[80:83], v[192:195]\n"
"v_xor_b32 v6, 65536, v6\n"
"v_mfma_f32_16x16x32_bf16 v[192:195], a[116:119], a[108:111], v[192:195]\n"
"s_waitcnt lgkmcnt(8)\n"
"v_xor_b32 v7, 196608, v7\n"
"v_mfma_f32_16x16x32_bf16 v[196:199], a[124:127], a[108:111], v[196:199]\n"
"s_waitcnt vmcnt(14) lgkmcnt(0)\n"
"s_barrier\n"
"v_mfma_f32_16x16x32_bf16 v[196:199], a[120:123], a[80:83], v[196:199]\n"
"s_mov_b32 s67, 0\n"
"ds_read_b64_tr_b16 a[112:113], v7 offset:0\n"
"v_mfma_f32_16x16x32_bf16 v[188:191], a[120:123], a[76:79], v[188:191]\n"
"s_load_dword s67, s[20:21], s42 glc\n"
"ds_read_b128 a[0:3], v6 offset:0\n"
"v_mfma_f32_16x16x32_bf16 v[188:191], a[124:127], a[104:107], v[188:191]\n"
"ds_read_b64_tr_b16 a[114:115], v7 offset:256\n"
"v_mfma_f32_16x16x32_bf16 v[180:183], a[124:127], a[100:103], v[180:183]\n"
"ds_read_b128 a[28:31], v6 offset:512\n"
"v_mfma_f32_16x16x32_bf16 v[180:183], a[120:123], a[72:75], v[180:183]\n"
"s_xor_b32 s42, s42, 8192\n"
"ds_read_b64_tr_b16 a[116:117], v7 offset:4096\n"
"v_mfma_f32_16x16x32_bf16 v[172:175], a[120:123], a[68:71], v[172:175]\n"
"ds_read_b128 a[4:7], v6 offset:2048\n"
"v_mfma_f32_16x16x32_bf16 v[172:175], a[124:127], a[96:99], v[172:175]\n"
"s_sub_u32 s66, s66, s36\n"
"ds_read_b64_tr_b16 a[118:119], v7 offset:4352\n"
"v_mfma_f32_16x16x32_bf16 v[164:167], a[124:127], a[92:95], v[164:167]\n"
"s_add_u32 m0, s44, 67584\n"
"ds_read_b128 a[32:35], v6 offset:2560\n"
"v_mfma_f32_16x16x32_bf16 v[164:167], a[120:123], a[64:67], v[164:167]\n"
"buffer_load_dwordx4 v2, s[8:11], s66 offen lds\n"
"v_mfma_f32_16x16x32_bf16 v[156:159], a[120:123], a[60:63], v[156:159]\n"
"s_sub_u32 s66, s66, s36\n"
"s_add_u32 m0, s44, 66560\n"
"ds_read_b128 a[8:11], v6 offset:4096\n"
"v_mfma_f32_16x16x32_bf16 v[156:159], a[124:127], a[88:91], v[156:159]\n"
"buffer_load_dwordx4 v2, s[8:11], s66 offen lds\n"
"v_mfma_f32_16x16x32_bf16 v[148:151], a[124:127], a[84:87], v[148:151]\n"
"s_sub_u32 s66, s66, s36\n"
"s_add_u32 m0, s44, 65536\n"
"ds_read_b128 a[36:39], v6 offset:4608\n"
"v_mfma_f32_16x16x32_bf16 v[148:151], a[120:123], a[56:59], v[148:151]\n"
"buffer_load_dwordx4 v2, s[8:11], s66 offen lds\n"
"v_mfma_f32_16x16x32_bf16 v[200:203], a[128:131], a[56:59], v[200:203]\n"
"v_mfma_f32_16x16x32_bf16 v[200:203], a[132:135], a[84:87], v[200:203]\n"
"ds_read_b64_tr_b16 a[120:121], v7 offset:8\n"
"v_mfma_f32_16x16x32_bf16 v[208:211], a[132:135], a[88:91], v[208:211]\n"
"ds_read_b128 a[12:15], v6 offset:6144\n"
"v_mfma_f32_16x16x32_bf16 v[208:211], a[128:131], a[60:63], v[208:211]\n"
"s_sub_u32 s70, s70, s67\n"
"ds_read_b64_tr_b16 a[122:123], v7 offset:264\n"
"v_mfma_f32_16x16x32_bf16 v[216:219], a[128:131], a[64:67], v[216:219]\n"
"ds_read_b128 a[40:43], v6 offset:6656\n"
"v_mfma_f32_16x16x32_bf16 v[216:219], a[132:135], a[92:95], v[216:219]\n"
"ds_read_b64_tr_b16 a[124:125], v7 offset:4104\n"
"v_mfma_f32_16x16x32_bf16 v[224:227], a[132:135], a[96:99], v[224:227]\n"
"ds_read_b128 a[16:19], v6 offset:8192\n"
"v_mfma_f32_16x16x32_bf16 v[224:227], a[128:131], a[68:71], v[224:227]\n"
"s_xor_b32 s74, s40, 4096\n"
"ds_read_b64_tr_b16 a[126:127], v7 offset:4360\n"
"v_mfma_f32_16x16x32_bf16 v[232:235], a[128:131], a[72:75], v[232:235]\n"
"s_add_u32 m0, s72, 4096\n"
"ds_read_b128 a[44:47], v6 offset:8704\n"
"v_mfma_f32_16x16x32_bf16 v[232:235], a[132:135], a[100:103], v[232:235]\n"
"s_sub_u32 s70, s70, s67\n"
"buffer_load_dwordx4 v4, s[16:19], s74 offen offset:2048 nt lds\n"
"v_mfma_f32_16x16x32_bf16 v[240:243], a[132:135], a[104:107], v[240:243]\n"
"ds_read_b128 a[20:23], v6 offset:10240\n"
"v_mfma_f32_16x16x32_bf16 v[240:243], a[128:131], a[76:79], v[240:243]\n"
"s_sub_u32 s70, s70, s67\n"
"buffer_load_dwordx4 v5, s[16:19], s74 offen offset:3072 nt lds\n"
"v_mfma_f32_16x16x32_bf16 v[248:251], a[128:131], a[80:83], v[248:251]\n"
"ds_read_b128 a[48:51], v6 offset:10752\n"
"v_mfma_f32_16x16x32_bf16 v[248:251], a[132:135], a[108:111], v[248:251]\n"
"v_mfma_f32_16x16x32_bf16 v[252:255], a[140:143], a[108:111], v[252:255]\n"
"v_mfma_f32_16x16x32_bf16 v[252:255], a[136:139], a[80:83], v[252:255]\n"
"s_sub_u32 s70, s70, s67\n"
"ds_read_b64_tr_b16 a[128:129], v7 offset:512\n"
"v_mfma_f32_16x16x32_bf16 v[244:247], a[136:139], a[76:79], v[244:247]\n"
"ds_read_b128 a[24:27], v6 offset:12288\n"
"v_mfma_f32_16x16x32_bf16 v[244:247], a[140:143], a[104:107], v[244:247]\n"
"ds_read_b64_tr_b16 a[130:131], v7 offset:768\n"
"v_mfma_f32_16x16x32_bf16 v[236:239], a[140:143], a[100:103], v[236:239]\n"
"s_xor_b32 s75, s65, 128\n"
"ds_read_b128 a[52:55], v6 offset:12800\n"
"v_mfma_f32_16x16x32_bf16 v[236:239], a[136:139], a[72:75], v[236:239]\n"
"s_xor_b32 m0, s45, 212992\n"
"ds_read_b64_tr_b16 a[132:133], v7 offset:4608\n"
"v_mfma_f32_16x16x32_bf16 v[228:231], a[136:139], a[68:71], v[228:231]\n"
"s_sub_u32 s70, s70, s67\n"
"buffer_load_dwordx4 v3, s[12:15], s75 offen lds\n"
"v_mfma_f32_16x16x32_bf16 v[228:231], a[140:143], a[96:99], v[228:231]\n"
"s_xor_b32 s74, s65, 65664\n"
"ds_read_b64_tr_b16 a[134:135], v7 offset:4864\n"
"v_mfma_f32_16x16x32_bf16 v[220:223], a[140:143], a[92:95], v[220:223]\n"
"s_xor_b32 m0, s45, 214016\n"
"v_mfma_f32_16x16x32_bf16 v[220:223], a[136:139], a[64:67], v[220:223]\n"
"s_sub_u32 s70, s70, s67\n"
"buffer_load_dwordx4 v3, s[12:15], s74 offen lds\n"
"v_mfma_f32_16x16x32_bf16 v[212:215], a[136:139], a[60:63], v[212:215]\n"
"v_mfma_f32_16x16x32_bf16 v[212:215], a[140:143], a[88:91], v[212:215]\n"
"v_mfma_f32_16x16x32_bf16 v[204:207], a[140:143], a[84:87], v[204:207]\n"
"v_mfma_f32_16x16x32_bf16 v[204:207], a[136:139], a[56:59], v[204:207]\n"
"s_waitcnt lgkmcnt(15)\n"
"s_sub_u32 s70, s70, s67\n"
"v_mfma_f32_16x16x32_bf16 v[32:35], a[112:115], a[0:3], v[32:35]\n"
"s_waitcnt lgkmcnt(15)\n"
"s_xor_b32 s75, s65, 131200\n"
"v_mfma_f32_16x16x32_bf16 v[32:35], a[116:119], a[28:31], v[32:35]\n"
"s_waitcnt lgkmcnt(15)\n"
"s_xor_b32 m0, s45, 215040\n"
"ds_read_b64_tr_b16 a[136:137], v7 offset:520\n"
"v_mfma_f32_16x16x32_bf16 v[40:43], a[116:119], a[32:35], v[40:43]\n"
"buffer_load_dwordx4 v3, s[12:15], s75 offen lds\n"
"v_mfma_f32_16x16x32_bf16 v[40:43], a[112:115], a[4:7], v[40:43]\n"
"s_waitcnt lgkmcnt(15)\n"
"s_sub_u32 s70, s70, s67\n"
"ds_read_b64_tr_b16 a[138:139], v7 offset:776\n"
"v_mfma_f32_16x16x32_bf16 v[48:51], a[112:115], a[8:11], v[48:51]\n"
"s_waitcnt lgkmcnt(15)\n"
"v_mfma_f32_16x16x32_bf16 v[48:51], a[116:119], a[36:39], v[48:51]\n"
"s_waitcnt lgkmcnt(14)\n"
"ds_read_b64_tr_b16 a[140:141], v7 offset:4616\n"
"v_mfma_f32_16x16x32_bf16 v[56:59], a[116:119], a[40:43], v[56:59]\n"
"s_xor_b32 s74, s65, 196736\n"
"v_mfma_f32_16x16x32_bf16 v[56:59], a[112:115], a[12:15], v[56:59]\n"
"s_waitcnt lgkmcnt(13)\n"
"s_xor_b32 m0, s45, 216064\n"
"ds_read_b64_tr_b16 a[142:143], v7 offset:4872\n"
"v_mfma_f32_16x16x32_bf16 v[64:67], a[112:115], a[16:19], v[64:67]\n"
"s_waitcnt lgkmcnt(12)\n"
"s_sub_u32 s70, s70, s67\n"
"buffer_load_dwordx4 v3, s[12:15], s74 offen lds\n"
"v_mfma_f32_16x16x32_bf16 v[64:67], a[116:119], a[44:47], v[64:67]\n"
"s_waitcnt lgkmcnt(10)\n"
"v_mfma_f32_16x16x32_bf16 v[72:75], a[116:119], a[48:51], v[72:75]\n"
"v_mfma_f32_16x16x32_bf16 v[72:75], a[112:115], a[20:23], v[72:75]\n"
"s_waitcnt lgkmcnt(8)\n"
"v_mfma_f32_16x16x32_bf16 v[80:83], a[112:115], a[24:27], v[80:83]\n"
"s_waitcnt lgkmcnt(6)\n"
"v_mfma_f32_16x16x32_bf16 v[80:83], a[116:119], a[52:55], v[80:83]\n"
"v_mfma_f32_16x16x32_bf16 v[84:87], a[124:127], a[52:55], v[84:87]\n"
"s_waitcnt vmcnt(17) lgkmcnt(0)\n"
"s_barrier\n"
"s_xor_b32 s75, s40, 8192\n"
"v_mfma_f32_16x16x32_bf16 v[84:87], a[120:123], a[24:27], v[84:87]\n"
"s_add_u32 m0, s72, 32768\n"
"ds_read_b64_tr_b16 a[112:113], v7 offset:16384\n"
"v_mfma_f32_16x16x32_bf16 v[76:79], a[120:123], a[20:23], v[76:79]\n"
"s_lshr_b32 s70, s70, 31\n"
"buffer_load_dwordx4 v4, s[16:19], s75 offen nt lds\n"
"v_mfma_f32_16x16x32_bf16 v[76:79], a[124:127], a[48:51], v[76:79]\n"
"s_bfe_u32 s67, s52, 65548\n"
"ds_read_b64_tr_b16 a[114:115], v7 offset:16640\n"
"v_mfma_f32_16x16x32_bf16 v[68:71], a[124:127], a[44:47], v[68:71]\n"
"s_xor_b32 s70, s70, s67\n"
"v_mfma_f32_16x16x32_bf16 v[68:71], a[120:123], a[16:19], v[68:71]\n"
"s_xor_b32 s70, s70, s69\n"
"ds_read_b64_tr_b16 a[116:117], v7 offset:20480\n"
"v_mfma_f32_16x16x32_bf16 v[60:63], a[120:123], a[12:15], v[60:63]\n"
"v_mfma_f32_16x16x32_bf16 v[60:63], a[124:127], a[40:43], v[60:63]\n"
"ds_read_b64_tr_b16 a[118:119], v7 offset:20736\n"
"v_mfma_f32_16x16x32_bf16 v[52:55], a[124:127], a[36:39], v[52:55]\n"
"v_mfma_f32_16x16x32_bf16 v[52:55], a[120:123], a[8:11], v[52:55]\n"
"buffer_load_dwordx4 v5, s[16:19], s75 offen offset:1024 nt lds\n"
"v_mfma_f32_16x16x32_bf16 v[44:47], a[120:123], a[4:7], v[44:47]\n"
"v_mfma_f32_16x16x32_bf16 v[44:47], a[124:127], a[32:35], v[44:47]\n"
"v_mfma_f32_16x16x32_bf16 v[36:39], a[124:127], a[28:31], v[36:39]\n"
"v_mfma_f32_16x16x32_bf16 v[36:39], a[120:123], a[0:3], v[36:39]\n"
"v_mfma_f32_16x16x32_bf16 v[88:91], a[128:131], a[0:3], v[88:91]\n"
"v_mfma_f32_16x16x32_bf16 v[88:91], a[132:135], a[28:31], v[88:91]\n"
"ds_read_b64_tr_b16 a[120:121], v7 offset:16392\n"
"v_mfma_f32_16x16x32_bf16 v[96:99], a[132:135], a[32:35], v[96:99]\n"
"s_xor_b32 s74, s40, 8192\n"
"v_mfma_f32_16x16x32_bf16 v[96:99], a[128:131], a[4:7], v[96:99]\n"
"s_add_u32 m0, s72, 32768\n"
"ds_read_b64_tr_b16 a[122:123], v7 offset:16648\n"
"v_mfma_f32_16x16x32_bf16 v[104:107], a[128:131], a[8:11], v[104:107]\n"
"buffer_load_dwordx4 v4, s[16:19], s74 offen offset:2048 nt lds\n"
"v_mfma_f32_16x16x32_bf16 v[104:107], a[132:135], a[36:39], v[104:107]\n"
"ds_read_b64_tr_b16 a[124:125], v7 offset:20488\n"
"v_mfma_f32_16x16x32_bf16 v[112:115], a[132:135], a[40:43], v[112:115]\n"
"v_mfma_f32_16x16x32_bf16 v[112:115], a[128:131], a[12:15], v[112:115]\n"
"ds_read_b64_tr_b16 a[126:127], v7 offset:20744\n"
"v_mfma_f32_16x16x32_bf16 v[120:123], a[128:131], a[16:19], v[120:123]\n"
"v_mfma_f32_16x16x32_bf16 v[120:123], a[132:135], a[44:47], v[120:123]\n"
"v_mfma_f32_16x16x32_bf16 v[128:131], a[132:135], a[48:51], v[128:131]\n"
"v_mfma_f32_16x16x32_bf16 v[128:131], a[128:131], a[20:23], v[128:131]\n"
"buffer_load_dwordx4 v5, s[16:19], s74 offen offset:3072 nt lds\n"
"v_mfma_f32_16x16x32_bf16 v[136:139], a[128:131], a[24:27], v[136:139]\n"
"v_mfma_f32_16x16x32_bf16 v[136:139], a[132:135], a[52:55], v[136:139]\n"
"v_mfma_f32_16x16x32_bf16 v[140:143], a[140:143], a[52:55], v[140:143]\n"
"v_mfma_f32_16x16x32_bf16 v[140:143], a[136:139], a[24:27], v[140:143]\n"
"ds_read_b64_tr_b16 a[128:129], v7 offset:16896\n"
"v_mfma_f32_16x16x32_bf16 v[132:135], a[136:139], a[20:23], v[132:135]\n"
"v_mfma_f32_16x16x32_bf16 v[132:135], a[140:143], a[48:51], v[132:135]\n"
"ds_read_b64_tr_b16 a[130:131], v7 offset:17152\n"
"v_mfma_f32_16x16x32_bf16 v[124:127], a[140:143], a[44:47], v[124:127]\n"
"s_xor_b32 s75, s40, 12288\n"
"v_mfma_f32_16x16x32_bf16 v[124:127], a[136:139], a[16:19], v[124:127]\n"
"s_add_u32 m0, s72, 36864\n"
"ds_read_b64_tr_b16 a[132:133], v7 offset:20992\n"
"v_mfma_f32_16x16x32_bf16 v[116:119], a[136:139], a[12:15], v[116:119]\n"
"buffer_load_dwordx4 v4, s[16:19], s75 offen nt lds\n"
"v_mfma_f32_16x16x32_bf16 v[116:119], a[140:143], a[40:43], v[116:119]\n"
"ds_read_b64_tr_b16 a[134:135], v7 offset:21248\n"
"v_mfma_f32_16x16x32_bf16 v[108:111], a[140:143], a[36:39], v[108:111]\n"
"v_mfma_f32_16x16x32_bf16 v[108:111], a[136:139], a[8:11], v[108:111]\n"
"v_mfma_f32_16x16x32_bf16 v[100:103], a[136:139], a[4:7], v[100:103]\n"
"v_mfma_f32_16x16x32_bf16 v[100:103], a[140:143], a[32:35], v[100:103]\n"
"v_mfma_f32_16x16x32_bf16 v[92:95], a[140:143], a[28:31], v[92:95]\n"
"v_mfma_f32_16x16x32_bf16 v[92:95], a[136:139], a[0:3], v[92:95]\n"
"s_waitcnt lgkmcnt(10)\n"
"buffer_load_dwordx4 v5, s[16:19], s75 offen offset:1024 nt lds\n"
"v_mfma_f32_16x16x32_bf16 v[144:147], a[112:115], a[0:3], v[144:147]\n"
"s_waitcnt lgkmcnt(8)\n"
"v_mfma_f32_16x16x32_bf16 v[144:147], a[116:119], a[28:31], v[144:147]\n"
"ds_read_b64_tr_b16 a[136:137], v7 offset:16904\n"
"v_mfma_f32_16x16x32_bf16 v[152:155], a[116:119], a[32:35], v[152:155]\n"
"v_mfma_f32_16x16x32_bf16 v[152:155], a[112:115], a[4:7], v[152:155]\n"
"ds_read_b64_tr_b16 a[138:139], v7 offset:17160\n"
"v_mfma_f32_16x16x32_bf16 v[160:163], a[112:115], a[8:11], v[160:163]\n"
"v_mfma_f32_16x16x32_bf16 v[160:163], a[116:119], a[36:39], v[160:163]\n"
"ds_read_b64_tr_b16 a[140:141], v7 offset:21000\n"
"v_mfma_f32_16x16x32_bf16 v[168:171], a[116:119], a[40:43], v[168:171]\n"
"s_xor_b32 s74, s40, 12288\n"
"v_mfma_f32_16x16x32_bf16 v[168:171], a[112:115], a[12:15], v[168:171]\n"
"s_add_u32 m0, s72, 36864\n"
"ds_read_b64_tr_b16 a[142:143], v7 offset:21256\n"
"v_mfma_f32_16x16x32_bf16 v[176:179], a[112:115], a[16:19], v[176:179]\n"
"buffer_load_dwordx4 v4, s[16:19], s74 offen offset:2048 nt lds\n"
"v_mfma_f32_16x16x32_bf16 v[176:179], a[116:119], a[44:47], v[176:179]\n"
"v_mfma_f32_16x16x32_bf16 v[184:187], a[116:119], a[48:51], v[184:187]\n"
"v_mfma_f32_16x16x32_bf16 v[184:187], a[112:115], a[20:23], v[184:187]\n"
"v_mfma_f32_16x16x32_bf16 v[192:195], a[112:115], a[24:27], v[192:195]\n"
"v_xor_b32 v6, 65536, v6\n"
"v_mfma_f32_16x16x32_bf16 v[192:195], a[116:119], a[52:55], v[192:195]\n"
"s_waitcnt lgkmcnt(8)\n"
"v_xor_b32 v7, 196608, v7\n"
"v_mfma_f32_16x16x32_bf16 v[196:199], a[124:127], a[52:55], v[196:199]\n"
"s_waitcnt vmcnt(13) lgkmcnt(0)\n"
"s_barrier\n"
"v_mfma_f32_16x16x32_bf16 v[196:199], a[120:123], a[24:27], v[196:199]\n"
"ds_read_b64_tr_b16 a[112:113], v7 offset:0\n"
"v_mfma_f32_16x16x32_bf16 v[188:191], a[120:123], a[20:23], v[188:191]\n"
"ds_read_b128 a[56:59], v6 offset:0\n"
"v_mfma_f32_16x16x32_bf16 v[188:191], a[124:127], a[48:51], v[188:191]\n"
"ds_read_b64_tr_b16 a[114:115], v7 offset:256\n"
"v_mfma_f32_16x16x32_bf16 v[180:183], a[124:127], a[44:47], v[180:183]\n"
"ds_read_b128 a[84:87], v6 offset:512\n"
"v_mfma_f32_16x16x32_bf16 v[180:183], a[120:123], a[16:19], v[180:183]\n"
"ds_read_b64_tr_b16 a[116:117], v7 offset:4096\n"
"v_mfma_f32_16x16x32_bf16 v[172:175], a[120:123], a[12:15], v[172:175]\n"
"ds_read_b128 a[60:63], v6 offset:2048\n"
"v_mfma_f32_16x16x32_bf16 v[172:175], a[124:127], a[40:43], v[172:175]\n"
"ds_read_b64_tr_b16 a[118:119], v7 offset:4352\n"
"v_mfma_f32_16x16x32_bf16 v[164:167], a[124:127], a[36:39], v[164:167]\n"
"ds_read_b128 a[88:91], v6 offset:2560\n"
"v_mfma_f32_16x16x32_bf16 v[164:167], a[120:123], a[8:11], v[164:167]\n"
"buffer_load_dwordx4 v5, s[16:19], s74 offen offset:3072 nt lds\n"
"v_mfma_f32_16x16x32_bf16 v[156:159], a[120:123], a[4:7], v[156:159]\n"
"s_xor_b32 s75, s40, 16384\n"
"s_add_u32 m0, s72, 65536\n"
"ds_read_b128 a[64:67], v6 offset:4096\n"
"v_mfma_f32_16x16x32_bf16 v[156:159], a[124:127], a[32:35], v[156:159]\n"
"buffer_load_dwordx4 v4, s[16:19], s75 offen nt lds\n"
"v_mfma_f32_16x16x32_bf16 v[148:151], a[124:127], a[28:31], v[148:151]\n"
"ds_read_b128 a[92:95], v6 offset:4608\n"
"v_mfma_f32_16x16x32_bf16 v[148:151], a[120:123], a[0:3], v[148:151]\n"
"v_mfma_f32_16x16x32_bf16 v[200:203], a[128:131], a[0:3], v[200:203]\n"
"v_mfma_f32_16x16x32_bf16 v[200:203], a[132:135], a[28:31], v[200:203]\n"
"ds_read_b64_tr_b16 a[120:121], v7 offset:8\n"
"v_mfma_f32_16x16x32_bf16 v[208:211], a[132:135], a[32:35], v[208:211]\n"
"ds_read_b128 a[68:71], v6 offset:6144\n"
"v_mfma_f32_16x16x32_bf16 v[208:211], a[128:131], a[4:7], v[208:211]\n"
"ds_read_b64_tr_b16 a[122:123], v7 offset:264\n"
"v_mfma_f32_16x16x32_bf16 v[216:219], a[128:131], a[8:11], v[216:219]\n"
"ds_read_b128 a[96:99], v6 offset:6656\n"
"v_mfma_f32_16x16x32_bf16 v[216:219], a[132:135], a[36:39], v[216:219]\n"
"ds_read_b64_tr_b16 a[124:125], v7 offset:4104\n"
"v_mfma_f32_16x16x32_bf16 v[224:227], a[132:135], a[40:43], v[224:227]\n"
"ds_read_b128 a[72:75], v6 offset:8192\n"
"v_mfma_f32_16x16x32_bf16 v[224:227], a[128:131], a[12:15], v[224:227]\n"
"ds_read_b64_tr_b16 a[126:127], v7 offset:4360\n"
"v_mfma_f32_16x16x32_bf16 v[232:235], a[128:131], a[16:19], v[232:235]\n"
"ds_read_b128 a[100:103], v6 offset:8704\n"
"v_mfma_f32_16x16x32_bf16 v[232:235], a[132:135], a[44:47], v[232:235]\n"
"buffer_load_dwordx4 v5, s[16:19], s75 offen offset:1024 nt lds\n"
"v_mfma_f32_16x16x32_bf16 v[240:243], a[132:135], a[48:51], v[240:243]\n"
"ds_read_b128 a[76:79], v6 offset:10240\n"
"v_mfma_f32_16x16x32_bf16 v[240:243], a[128:131], a[20:23], v[240:243]\n"
"v_mfma_f32_16x16x32_bf16 v[248:251], a[128:131], a[24:27], v[248:251]\n"
"ds_read_b128 a[104:107], v6 offset:10752\n"
"v_mfma_f32_16x16x32_bf16 v[248:251], a[132:135], a[52:55], v[248:251]\n"
"v_mfma_f32_16x16x32_bf16 v[252:255], a[140:143], a[52:55], v[252:255]\n"
"v_mfma_f32_16x16x32_bf16 v[252:255], a[136:139], a[24:27], v[252:255]\n"
"ds_read_b64_tr_b16 a[128:129], v7 offset:512\n"
"v_mfma_f32_16x16x32_bf16 v[244:247], a[136:139], a[20:23], v[244:247]\n"
"ds_read_b128 a[80:83], v6 offset:12288\n"
"v_mfma_f32_16x16x32_bf16 v[244:247], a[140:143], a[48:51], v[244:247]\n"
"ds_read_b64_tr_b16 a[130:131], v7 offset:768\n"
"v_mfma_f32_16x16x32_bf16 v[236:239], a[140:143], a[44:47], v[236:239]\n"
"s_xor_b32 s74, s40, 16384\n"
"ds_read_b128 a[108:111], v6 offset:12800\n"
"v_mfma_f32_16x16x32_bf16 v[236:239], a[136:139], a[16:19], v[236:239]\n"
"s_add_u32 m0, s72, 65536\n"
"ds_read_b64_tr_b16 a[132:133], v7 offset:4608\n"
"v_mfma_f32_16x16x32_bf16 v[228:231], a[136:139], a[12:15], v[228:231]\n"
"buffer_load_dwordx4 v4, s[16:19], s74 offen offset:2048 nt lds\n"
"v_mfma_f32_16x16x32_bf16 v[228:231], a[140:143], a[40:43], v[228:231]\n"
"ds_read_b64_tr_b16 a[134:135], v7 offset:4864\n"
"v_mfma_f32_16x16x32_bf16 v[220:223], a[140:143], a[36:39], v[220:223]\n"
"v_mfma_f32_16x16x32_bf16 v[220:223], a[136:139], a[8:11], v[220:223]\n"
"v_mfma_f32_16x16x32_bf16 v[212:215], a[136:139], a[4:7], v[212:215]\n"
"v_mfma_f32_16x16x32_bf16 v[212:215], a[140:143], a[32:35], v[212:215]\n"
"v_mfma_f32_16x16x32_bf16 v[204:207], a[140:143], a[28:31], v[204:207]\n"
"buffer_load_dwordx4 v5, s[16:19], s74 offen offset:3072 nt lds\n"
"v_mfma_f32_16x16x32_bf16 v[204:207], a[136:139], a[0:3], v[204:207]\n"
"s_waitcnt lgkmcnt(15)\n"
"v_mfma_f32_16x16x32_bf16 v[32:35], a[112:115], a[56:59], v[32:35]\n"
"s_waitcnt lgkmcnt(15)\n"
"v_mfma_f32_16x16x32_bf16 v[32:35], a[116:119], a[84:87], v[32:35]\n"
"s_waitcnt lgkmcnt(15)\n"
"ds_read_b64_tr_b16 a[136:137], v7 offset:520\n"
"v_mfma_f32_16x16x32_bf16 v[40:43], a[116:119], a[88:91], v[40:43]\n"
"v_mfma_f32_16x16x32_bf16 v[40:43], a[112:115], a[60:63], v[40:43]\n"
"s_waitcnt lgkmcnt(15)\n"
"ds_read_b64_tr_b16 a[138:139], v7 offset:776\n"
"v_mfma_f32_16x16x32_bf16 v[48:51], a[112:115], a[64:67], v[48:51]\n"
"s_waitcnt lgkmcnt(15)\n"
"v_mfma_f32_16x16x32_bf16 v[48:51], a[116:119], a[92:95], v[48:51]\n"
"s_waitcnt lgkmcnt(14)\n"
"ds_read_b64_tr_b16 a[140:141], v7 offset:4616\n"
"v_mfma_f32_16x16x32_bf16 v[56:59], a[116:119], a[96:99], v[56:59]\n"
"s_xor_b32 s75, s40, 20480\n"
"v_mfma_f32_16x16x32_bf16 v[56:59], a[112:115], a[68:71], v[56:59]\n"
"s_waitcnt lgkmcnt(13)\n"
"s_add_u32 m0, s72, 69632\n"
"ds_read_b64_tr_b16 a[142:143], v7 offset:4872\n"
"v_mfma_f32_16x16x32_bf16 v[64:67], a[112:115], a[72:75], v[64:67]\n"
"s_waitcnt lgkmcnt(12)\n"
"buffer_load_dwordx4 v4, s[16:19], s75 offen nt lds\n"
"v_mfma_f32_16x16x32_bf16 v[64:67], a[116:119], a[100:103], v[64:67]\n"
"s_waitcnt lgkmcnt(10)\n"
"v_mfma_f32_16x16x32_bf16 v[72:75], a[116:119], a[104:107], v[72:75]\n"
"v_mfma_f32_16x16x32_bf16 v[72:75], a[112:115], a[76:79], v[72:75]\n"
"s_waitcnt lgkmcnt(8)\n"
"v_mfma_f32_16x16x32_bf16 v[80:83], a[112:115], a[80:83], v[80:83]\n"
"s_waitcnt lgkmcnt(6)\n"
"v_mfma_f32_16x16x32_bf16 v[80:83], a[116:119], a[108:111], v[80:83]\n"
"v_mfma_f32_16x16x32_bf16 v[84:87], a[124:127], a[108:111], v[84:87]\n"
"s_waitcnt vmcnt(13) lgkmcnt(0)\n"
"s_barrier\n"
"v_mfma_f32_16x16x32_bf16 v[84:87], a[120:123], a[80:83], v[84:87]\n"
"ds_read_b64_tr_b16 a[112:113], v7 offset:16384\n"
"v_mfma_f32_16x16x32_bf16 v[76:79], a[120:123], a[76:79], v[76:79]\n"
"v_mfma_f32_16x16x32_bf16 v[76:79], a[124:127], a[104:107], v[76:79]\n"
"ds_read_b64_tr_b16 a[114:115], v7 offset:16640\n"
"v_mfma_f32_16x16x32_bf16 v[68:71], a[124:127], a[100:103], v[68:71]\n"
"buffer_load_dwordx4 v5, s[16:19], s75 offen offset:1024 nt lds\n"
"v_mfma_f32_16x16x32_bf16 v[68:71], a[120:123], a[72:75], v[68:71]\n"
"ds_read_b64_tr_b16 a[116:117], v7 offset:20480\n"
"v_mul_f32 v80, s78, v80\n"
"v_mfma_f32_16x16x32_bf16 v[60:63], a[120:123], a[68:71], v[60:63]\n"
"v_mul_f32 v81, s78, v81\n"
"v_mul_f32 v82, s78, v82\n"
"v_mfma_f32_16x16x32_bf16 v[60:63], a[124:127], a[96:99], v[60:63]\n"
"ds_read_b64_tr_b16 a[118:119], v7 offset:20736\n"
"v_mul_f32 v83, s78, v83\n"
"v_mfma_f32_16x16x32_bf16 v[52:55], a[124:127], a[92:95], v[52:55]\n"
"v_mul_f32 v84, s78, v84\n"
"v_mul_f32 v85, s78, v85\n"
"v_mfma_f32_16x16x32_bf16 v[52:55], a[120:123], a[64:67], v[52:55]\n"
"v_mul_f32 v86, s78, v86\n"
"v_mul_f32 v87, s78, v87\n"
"v_mfma_f32_16x16x32_bf16 v[44:47], a[120:123], a[60:63], v[44:47]\n"
"v_cvt_pk_bf16_f32 v80, v80, v81\n"
"v_cvt_pk_bf16_f32 v81, v82, v83\n"
"v_mfma_f32_16x16x32_bf16 v[44:47], a[124:127], a[88:91], v[44:47]\n"
"v_cvt_pk_bf16_f32 v82, v84, v85\n"
"v_cvt_pk_bf16_f32 v83, v86, v87\n"
"v_mfma_f32_16x16x32_bf16 v[36:39], a[124:127], a[84:87], v[36:39]\n"
"s_xor_b32 s74, s40, 20480\n"
"v_mul_f32 v72, s78, v72\n"
"v_mul_f32 v73, s78, v73\n"
"v_mfma_f32_16x16x32_bf16 v[36:39], a[120:123], a[56:59], v[36:39]\n"
"s_add_u32 m0, s72, 69632\n"
"v_mul_f32 v74, s78, v74\n"
"v_mul_f32 v75, s78, v75\n"
"v_mfma_f32_16x16x32_bf16 v[88:91], a[128:131], a[56:59], v[88:91]\n"
"buffer_load_dwordx4 v4, s[16:19], s74 offen offset:2048 nt lds\n"
"v_mul_f32 v76, s78, v76\n"
"v_mfma_f32_16x16x32_bf16 v[88:91], a[132:135], a[84:87], v[88:91]\n"
"ds_read_b64_tr_b16 a[120:121], v7 offset:16392\n"
"v_mul_f32 v77, s78, v77\n"
"v_mfma_f32_16x16x32_bf16 v[96:99], a[132:135], a[88:91], v[96:99]\n"
"v_mul_f32 v78, s78, v78\n"
"v_mul_f32 v79, s78, v79\n"
"v_mfma_f32_16x16x32_bf16 v[96:99], a[128:131], a[60:63], v[96:99]\n"
"ds_read_b64_tr_b16 a[122:123], v7 offset:16648\n"
"v_cvt_pk_bf16_f32 v72, v72, v73\n"
"v_mfma_f32_16x16x32_bf16 v[104:107], a[128:131], a[64:67], v[104:107]\n"
"v_cvt_pk_bf16_f32 v73, v74, v75\n"
"v_cvt_pk_bf16_f32 v74, v76, v77\n"
"v_mfma_f32_16x16x32_bf16 v[104:107], a[132:135], a[92:95], v[104:107]\n"
"ds_read_b64_tr_b16 a[124:125], v7 offset:20488\n"
"v_cvt_pk_bf16_f32 v75, v78, v79\n"
"v_mfma_f32_16x16x32_bf16 v[112:115], a[132:135], a[96:99], v[112:115]\n"
"v_mul_f32 v64, s78, v64\n"
"v_mul_f32 v65, s78, v65\n"
"v_mfma_f32_16x16x32_bf16 v[112:115], a[128:131], a[68:71], v[112:115]\n"
"ds_read_b64_tr_b16 a[126:127], v7 offset:20744\n"
"v_mul_f32 v66, s78, v66\n"
"v_mfma_f32_16x16x32_bf16 v[120:123], a[128:131], a[72:75], v[120:123]\n"
"v_mul_f32 v67, s78, v67\n"
"v_mul_f32 v68, s78, v68\n"
"v_mfma_f32_16x16x32_bf16 v[120:123], a[132:135], a[100:103], v[120:123]\n"
"v_mul_f32 v69, s78, v69\n"
"v_mul_f32 v70, s78, v70\n"
"v_mfma_f32_16x16x32_bf16 v[128:131], a[132:135], a[104:107], v[128:131]\n"
"buffer_load_dwordx4 v5, s[16:19], s74 offen offset:3072 nt lds\n"
"v_mul_f32 v71, s78, v71\n"
"v_mfma_f32_16x16x32_bf16 v[128:131], a[128:131], a[76:79], v[128:131]\n"
"v_cvt_pk_bf16_f32 v64, v64, v65\n"
"v_cvt_pk_bf16_f32 v65, v66, v67\n"
"v_mfma_f32_16x16x32_bf16 v[136:139], a[128:131], a[80:83], v[136:139]\n"
"v_cvt_pk_bf16_f32 v66, v68, v69\n"
"v_cvt_pk_bf16_f32 v67, v70, v71\n"
"v_mfma_f32_16x16x32_bf16 v[136:139], a[132:135], a[108:111], v[136:139]\n"
"v_mul_f32 v56, s78, v56\n"
"v_mul_f32 v57, s78, v57\n"
"v_mfma_f32_16x16x32_bf16 v[140:143], a[140:143], a[108:111], v[140:143]\n"
"v_mul_f32 v58, s78, v58\n"
"v_mul_f32 v59, s78, v59\n"
"v_mfma_f32_16x16x32_bf16 v[140:143], a[136:139], a[80:83], v[140:143]\n"
"ds_read_b64_tr_b16 a[128:129], v7 offset:16896\n"
"v_mul_f32 v60, s78, v60\n"
"v_mfma_f32_16x16x32_bf16 v[132:135], a[136:139], a[76:79], v[132:135]\n"
"v_mul_f32 v61, s78, v61\n"
"v_mul_f32 v62, s78, v62\n"
"v_mfma_f32_16x16x32_bf16 v[132:135], a[140:143], a[104:107], v[132:135]\n"
"ds_read_b64_tr_b16 a[130:131], v7 offset:17152\n"
"v_mul_f32 v63, s78, v63\n"
"v_mfma_f32_16x16x32_bf16 v[124:127], a[140:143], a[100:103], v[124:127]\n"
"v_cvt_pk_bf16_f32 v56, v56, v57\n"
"v_cvt_pk_bf16_f32 v57, v58, v59\n"
"v_mfma_f32_16x16x32_bf16 v[124:127], a[136:139], a[72:75], v[124:127]\n"
"ds_read_b64_tr_b16 a[132:133], v7 offset:20992\n"
"v_cvt_pk_bf16_f32 v58, v60, v61\n"
"v_mfma_f32_16x16x32_bf16 v[116:119], a[136:139], a[68:71], v[116:119]\n"
"s_xor_b32 s75, s40, 24576\n"
"v_cvt_pk_bf16_f32 v59, v62, v63\n"
"v_mul_f32 v48, s78, v48\n"
"v_mfma_f32_16x16x32_bf16 v[116:119], a[140:143], a[96:99], v[116:119]\n"
"s_add_u32 m0, s72, 98304\n"
"ds_read_b64_tr_b16 a[134:135], v7 offset:21248\n"
"v_mul_f32 v49, s78, v49\n"
"v_mfma_f32_16x16x32_bf16 v[108:111], a[140:143], a[92:95], v[108:111]\n"
"buffer_load_dwordx4 v4, s[16:19], s75 offen nt lds\n"
"v_mul_f32 v50, s78, v50\n"
"v_mfma_f32_16x16x32_bf16 v[108:111], a[136:139], a[64:67], v[108:111]\n"
"v_mul_f32 v51, s78, v51\n"
"v_mul_f32 v52, s78, v52\n"
"v_mfma_f32_16x16x32_bf16 v[100:103], a[136:139], a[60:63], v[100:103]\n"
"v_mul_f32 v53, s78, v53\n"
"v_mul_f32 v54, s78, v54\n"
"v_mfma_f32_16x16x32_bf16 v[100:103], a[140:143], a[88:91], v[100:103]\n"
"v_mul_f32 v55, s78, v55\n"
"v_cvt_pk_bf16_f32 v48, v48, v49\n"
"v_mfma_f32_16x16x32_bf16 v[92:95], a[140:143], a[84:87], v[92:95]\n"
"v_cvt_pk_bf16_f32 v49, v50, v51\n"
"v_cvt_pk_bf16_f32 v50, v52, v53\n"
"v_mfma_f32_16x16x32_bf16 v[92:95], a[136:139], a[56:59], v[92:95]\n"
"s_waitcnt lgkmcnt(10)\n"
"v_cvt_pk_bf16_f32 v51, v54, v55\n"
"v_mul_f32 v40, s78, v40\n"
"v_mfma_f32_16x16x32_bf16 v[144:147], a[112:115], a[56:59], v[144:147]\n"
"s_waitcnt lgkmcnt(8)\n"
"v_mul_f32 v41, s78, v41\n"
"v_mul_f32 v42, s78, v42\n"
"v_mfma_f32_16x16x32_bf16 v[144:147], a[116:119], a[84:87], v[144:147]\n"
"ds_read_b64_tr_b16 a[136:137], v7 offset:16904\n"
"v_mul_f32 v43, s78, v43\n"
"v_mfma_f32_16x16x32_bf16 v[152:155], a[116:119], a[88:91], v[152:155]\n"
"v_mul_f32 v44, s78, v44\n"
"v_mul_f32 v45, s78, v45\n"
"v_mfma_f32_16x16x32_bf16 v[152:155], a[112:115], a[60:63], v[152:155]\n"
"ds_read_b64_tr_b16 a[138:139], v7 offset:17160\n"
"v_mul_f32 v46, s78, v46\n"
"v_mfma_f32_16x16x32_bf16 v[160:163], a[112:115], a[64:67], v[160:163]\n"
"v_mul_f32 v47, s78, v47\n"
"v_cvt_pk_bf16_f32 v40, v40, v41\n"
"v_mfma_f32_16x16x32_bf16 v[160:163], a[116:119], a[92:95], v[160:163]\n"
"ds_read_b64_tr_b16 a[140:141], v7 offset:21000\n"
"v_cvt_pk_bf16_f32 v41, v42, v43\n"
"v_mfma_f32_16x16x32_bf16 v[168:171], a[116:119], a[96:99], v[168:171]\n"
"v_cvt_pk_bf16_f32 v42, v44, v45\n"
"v_cvt_pk_bf16_f32 v43, v46, v47\n"
"v_mfma_f32_16x16x32_bf16 v[168:171], a[112:115], a[68:71], v[168:171]\n"
"ds_read_b64_tr_b16 a[142:143], v7 offset:21256\n"
"v_mul_f32 v32, s78, v32\n"
"v_mfma_f32_16x16x32_bf16 v[176:179], a[112:115], a[72:75], v[176:179]\n"
"buffer_load_dwordx4 v5, s[16:19], s75 offen offset:1024 nt lds\n"
"v_mul_f32 v33, s78, v33\n"
"v_mfma_f32_16x16x32_bf16 v[176:179], a[116:119], a[100:103], v[176:179]\n"
"v_mul_f32 v34, s78, v34\n"
"v_mul_f32 v35, s78, v35\n"
"v_mfma_f32_16x16x32_bf16 v[184:187], a[116:119], a[104:107], v[184:187]\n"
"v_mul_f32 v36, s78, v36\n"
"v_mul_f32 v37, s78, v37\n"
"v_mfma_f32_16x16x32_bf16 v[184:187], a[112:115], a[76:79], v[184:187]\n"
"v_mul_f32 v38, s78, v38\n"
"v_mul_f32 v39, s78, v39\n"
"v_mfma_f32_16x16x32_bf16 v[192:195], a[112:115], a[80:83], v[192:195]\n"
"v_cvt_pk_bf16_f32 v32, v32, v33\n"
"v_cvt_pk_bf16_f32 v33, v34, v35\n"
"v_mfma_f32_16x16x32_bf16 v[192:195], a[116:119], a[108:111], v[192:195]\n"
"s_waitcnt lgkmcnt(8)\n"
"v_cvt_pk_bf16_f32 v34, v36, v37\n"
"v_cvt_pk_bf16_f32 v35, v38, v39\n"
"v_mfma_f32_16x16x32_bf16 v[196:199], a[124:127], a[108:111], v[196:199]\n"
"v_mul_f32 v136, s78, v136\n"
"v_mul_f32 v137, s78, v137\n"
"v_mfma_f32_16x16x32_bf16 v[196:199], a[120:123], a[80:83], v[196:199]\n"
"v_mul_f32 v138, s78, v138\n"
"v_mul_f32 v139, s78, v139\n"
"v_mfma_f32_16x16x32_bf16 v[188:191], a[120:123], a[76:79], v[188:191]\n"
"v_mul_f32 v140, s78, v140\n"
"v_mul_f32 v141, s78, v141\n"
"v_mfma_f32_16x16x32_bf16 v[188:191], a[124:127], a[104:107], v[188:191]\n"
"v_mul_f32 v142, s78, v142\n"
"v_mul_f32 v143, s78, v143\n"
"v_mfma_f32_16x16x32_bf16 v[180:183], a[124:127], a[100:103], v[180:183]\n"
"v_cvt_pk_bf16_f32 v136, v136, v137\n"
"v_cvt_pk_bf16_f32 v137, v138, v139\n"
"v_mfma_f32_16x16x32_bf16 v[180:183], a[120:123], a[72:75], v[180:183]\n"
"v_cvt_pk_bf16_f32 v138, v140, v141\n"
"v_cvt_pk_bf16_f32 v139, v142, v143\n"
"v_mfma_f32_16x16x32_bf16 v[172:175], a[120:123], a[68:71], v[172:175]\n"
"s_xor_b32 s74, s40, 24576\n"
"v_mul_f32 v128, s78, v128\n"
"v_mul_f32 v129, s78, v129\n"
"v_mfma_f32_16x16x32_bf16 v[172:175], a[124:127], a[96:99], v[172:175]\n"
"s_add_u32 m0, s72, 98304\n"
"v_mul_f32 v130, s78, v130\n"
"v_mul_f32 v131, s78, v131\n"
"v_mfma_f32_16x16x32_bf16 v[164:167], a[124:127], a[92:95], v[164:167]\n"
"buffer_load_dwordx4 v4, s[16:19], s74 offen offset:2048 nt lds\n"
"v_mul_f32 v132, s78, v132\n"
"v_mfma_f32_16x16x32_bf16 v[164:167], a[120:123], a[64:67], v[164:167]\n"
"v_mul_f32 v133, s78, v133\n"
"v_mul_f32 v134, s78, v134\n"
"v_mfma_f32_16x16x32_bf16 v[156:159], a[120:123], a[60:63], v[156:159]\n"
"v_mul_f32 v135, s78, v135\n"
"v_cvt_pk_bf16_f32 v128, v128, v129\n"
"v_mfma_f32_16x16x32_bf16 v[156:159], a[124:127], a[88:91], v[156:159]\n"
"v_cvt_pk_bf16_f32 v129, v130, v131\n"
"v_cvt_pk_bf16_f32 v130, v132, v133\n"
"v_mfma_f32_16x16x32_bf16 v[148:151], a[124:127], a[84:87], v[148:151]\n"
"v_cvt_pk_bf16_f32 v131, v134, v135\n"
"v_mul_f32 v120, s78, v120\n"
"v_mfma_f32_16x16x32_bf16 v[148:151], a[120:123], a[56:59], v[148:151]\n"
"s_waitcnt lgkmcnt(6)\n"
"v_mul_f32 v121, s78, v121\n"
"v_mul_f32 v122, s78, v122\n"
"v_mfma_f32_16x16x32_bf16 v[200:203], a[128:131], a[56:59], v[200:203]\n"
"s_waitcnt lgkmcnt(4)\n"
"v_mul_f32 v123, s78, v123\n"
"v_mul_f32 v124, s78, v124\n"
"v_mfma_f32_16x16x32_bf16 v[200:203], a[132:135], a[84:87], v[200:203]\n"
"v_mul_f32 v125, s78, v125\n"
"v_mul_f32 v126, s78, v126\n"
"v_mfma_f32_16x16x32_bf16 v[208:211], a[132:135], a[88:91], v[208:211]\n"
"v_mul_f32 v127, s78, v127\n"
"v_cvt_pk_bf16_f32 v120, v120, v121\n"
"v_mfma_f32_16x16x32_bf16 v[208:211], a[128:131], a[60:63], v[208:211]\n"
"v_cvt_pk_bf16_f32 v121, v122, v123\n"
"v_cvt_pk_bf16_f32 v122, v124, v125\n"
"v_mfma_f32_16x16x32_bf16 v[216:219], a[128:131], a[64:67], v[216:219]\n"
"v_cvt_pk_bf16_f32 v123, v126, v127\n"
"v_mul_f32 v112, s78, v112\n"
"v_mfma_f32_16x16x32_bf16 v[216:219], a[132:135], a[92:95], v[216:219]\n"
"v_mul_f32 v113, s78, v113\n"
"v_mul_f32 v114, s78, v114\n"
"v_mfma_f32_16x16x32_bf16 v[224:227], a[132:135], a[96:99], v[224:227]\n"
"ds_read_b128 a[224:227], v8 offset:0\n"
"v_mul_f32 v115, s78, v115\n"
"v_mfma_f32_16x16x32_bf16 v[224:227], a[128:131], a[68:71], v[224:227]\n"
"v_mul_f32 v116, s78, v116\n"
"v_mul_f32 v117, s78, v117\n"
"v_mfma_f32_16x16x32_bf16 v[232:235], a[128:131], a[72:75], v[232:235]\n"
"v_mul_f32 v118, s78, v118\n"
"v_mul_f32 v119, s78, v119\n"
"v_mfma_f32_16x16x32_bf16 v[232:235], a[132:135], a[100:103], v[232:235]\n"
"buffer_load_dwordx4 v5, s[16:19], s74 offen offset:3072 nt lds\n"
"v_cvt_pk_bf16_f32 v112, v112, v113\n"
"v_mfma_f32_16x16x32_bf16 v[240:243], a[132:135], a[104:107], v[240:243]\n"
"ds_read_b128 a[228:231], v8 offset:256\n"
"v_cvt_pk_bf16_f32 v113, v114, v115\n"
"v_mfma_f32_16x16x32_bf16 v[240:243], a[128:131], a[76:79], v[240:243]\n"
"v_cvt_pk_bf16_f32 v114, v116, v117\n"
"v_cvt_pk_bf16_f32 v115, v118, v119\n"
"v_mfma_f32_16x16x32_bf16 v[248:251], a[128:131], a[80:83], v[248:251]\n"
"v_mul_f32 v104, s78, v104\n"
"v_mul_f32 v105, s78, v105\n"
"v_mfma_f32_16x16x32_bf16 v[248:251], a[132:135], a[108:111], v[248:251]\n"
"s_waitcnt lgkmcnt(2)\n"
"v_mul_f32 v106, s78, v106\n"
"v_mul_f32 v107, s78, v107\n"
"v_mfma_f32_16x16x32_bf16 v[252:255], a[140:143], a[108:111], v[252:255]\n"
"ds_read_b128 a[232:235], v8 offset:512\n"
"v_mul_f32 v108, s78, v108\n"
"v_mfma_f32_16x16x32_bf16 v[252:255], a[136:139], a[80:83], v[252:255]\n"
"v_mul_f32 v109, s78, v109\n"
"v_mul_f32 v110, s78, v110\n"
"v_mfma_f32_16x16x32_bf16 v[244:247], a[136:139], a[76:79], v[244:247]\n"
"v_mul_f32 v111, s78, v111\n"
"v_cvt_pk_bf16_f32 v104, v104, v105\n"
"v_mfma_f32_16x16x32_bf16 v[244:247], a[140:143], a[104:107], v[244:247]\n"
"v_cvt_pk_bf16_f32 v105, v106, v107\n"
"v_cvt_pk_bf16_f32 v106, v108, v109\n"
"v_mfma_f32_16x16x32_bf16 v[236:239], a[140:143], a[100:103], v[236:239]\n"
"ds_read_b128 a[236:239], v8 offset:768\n"
"v_cvt_pk_bf16_f32 v107, v110, v111\n"
"v_mfma_f32_16x16x32_bf16 v[236:239], a[136:139], a[72:75], v[236:239]\n"
"v_mul_f32 v96, s78, v96\n"
"v_mul_f32 v97, s78, v97\n"
"v_mfma_f32_16x16x32_bf16 v[228:231], a[136:139], a[68:71], v[228:231]\n"
"v_mul_f32 v98, s78, v98\n"
"v_mul_f32 v99, s78, v99\n"
"v_mfma_f32_16x16x32_bf16 v[228:231], a[140:143], a[96:99], v[228:231]\n"
"v_mul_f32 v100, s78, v100\n"
"v_mul_f32 v101, s78, v101\n"
"v_mfma_f32_16x16x32_bf16 v[220:223], a[140:143], a[92:95], v[220:223]\n"
"v_mul_f32 v102, s78, v102\n"
"v_mul_f32 v103, s78, v103\n"
"v_mfma_f32_16x16x32_bf16 v[220:223], a[136:139], a[64:67], v[220:223]\n"
"v_cvt_pk_bf16_f32 v96, v96, v97\n"
"v_cvt_pk_bf16_f32 v97, v98, v99\n"
"v_mfma_f32_16x16x32_bf16 v[212:215], a[136:139], a[60:63], v[212:215]\n"
"v_cvt_pk_bf16_f32 v98, v100, v101\n"
"v_cvt_pk_bf16_f32 v99, v102, v103\n"
"v_mfma_f32_16x16x32_bf16 v[212:215], a[140:143], a[88:91], v[212:215]\n"
"v_mul_f32 v88, s78, v88\n"
"v_mul_f32 v89, s78, v89\n"
"v_mfma_f32_16x16x32_bf16 v[204:207], a[140:143], a[84:87], v[204:207]\n"
"v_mul_f32 v90, s78, v90\n"
"v_mul_f32 v91, s78, v91\n"
"v_mfma_f32_16x16x32_bf16 v[204:207], a[136:139], a[56:59], v[204:207]\n"
"v_mul_f32 v92, s78, v92\n"
"v_mul_f32 v93, s78, v93\n"
"v_mul_f32 v94, s78, v94\n"
"v_mul_f32 v95, s78, v95\n"
"v_cvt_pk_bf16_f32 v88, v88, v89\n"
"v_cvt_pk_bf16_f32 v89, v90, v91\n"
"v_cvt_pk_bf16_f32 v90, v92, v93\n"
"v_cvt_pk_bf16_f32 v91, v94, v95\n"
"v_pk_mul_f32 v[192:193], v[192:193], s[78:79]\n"
"v_pk_mul_f32 v[194:195], v[194:195], s[78:79]\n"
"v_pk_mul_f32 v[196:197], v[196:197], s[78:79]\n"
"v_pk_mul_f32 v[198:199], v[198:199], s[78:79]\n"
"v_cvt_pk_bf16_f32 v192, v192, v193\n"
"v_cvt_pk_bf16_f32 v193, v194, v195\n"
"v_cvt_pk_bf16_f32 v194, v196, v197\n"
"v_cvt_pk_bf16_f32 v195, v198, v199\n"
"v_pk_mul_f32 v[184:185], v[184:185], s[78:79]\n"
"v_pk_mul_f32 v[186:187], v[186:187], s[78:79]\n"
"v_pk_mul_f32 v[188:189], v[188:189], s[78:79]\n"
"v_pk_mul_f32 v[190:191], v[190:191], s[78:79]\n"
"v_cvt_pk_bf16_f32 v184, v184, v185\n"
"v_cvt_pk_bf16_f32 v185, v186, v187\n"
"v_cvt_pk_bf16_f32 v186, v188, v189\n"
"v_cvt_pk_bf16_f32 v187, v190, v191\n"
"v_pk_mul_f32 v[176:177], v[176:177], s[78:79]\n"
"v_pk_mul_f32 v[178:179], v[178:179], s[78:79]\n"
"v_pk_mul_f32 v[180:181], v[180:181], s[78:79]\n"
"v_pk_mul_f32 v[182:183], v[182:183], s[78:79]\n"
"v_cvt_pk_bf16_f32 v176, v176, v177\n"
"v_cvt_pk_bf16_f32 v177, v178, v179\n"
"v_cvt_pk_bf16_f32 v178, v180, v181\n"
"v_cvt_pk_bf16_f32 v179, v182, v183\n"
"v_pk_mul_f32 v[168:169], v[168:169], s[78:79]\n"
"v_pk_mul_f32 v[170:171], v[170:171], s[78:79]\n"
"v_pk_mul_f32 v[172:173], v[172:173], s[78:79]\n"
"v_pk_mul_f32 v[174:175], v[174:175], s[78:79]\n"
"v_cvt_pk_bf16_f32 v168, v168, v169\n"
"v_cvt_pk_bf16_f32 v169, v170, v171\n"
"v_cvt_pk_bf16_f32 v170, v172, v173\n"
"v_cvt_pk_bf16_f32 v171, v174, v175\n"
"v_pk_mul_f32 v[160:161], v[160:161], s[78:79]\n"
"v_pk_mul_f32 v[162:163], v[162:163], s[78:79]\n"
"v_pk_mul_f32 v[164:165], v[164:165], s[78:79]\n"
"v_pk_mul_f32 v[166:167], v[166:167], s[78:79]\n"
"v_cvt_pk_bf16_f32 v160, v160, v161\n"
"v_cvt_pk_bf16_f32 v161, v162, v163\n"
"v_cvt_pk_bf16_f32 v162, v164, v165\n"
"v_cvt_pk_bf16_f32 v163, v166, v167\n"
"v_pk_mul_f32 v[152:153], v[152:153], s[78:79]\n"
"v_pk_mul_f32 v[154:155], v[154:155], s[78:79]\n"
"v_pk_mul_f32 v[156:157], v[156:157], s[78:79]\n"
"v_pk_mul_f32 v[158:159], v[158:159], s[78:79]\n"
"v_cvt_pk_bf16_f32 v152, v152, v153\n"
"v_cvt_pk_bf16_f32 v153, v154, v155\n"
"v_cvt_pk_bf16_f32 v154, v156, v157\n"
"v_cvt_pk_bf16_f32 v155, v158, v159\n"
"v_pk_mul_f32 v[144:145], v[144:145], s[78:79]\n"
"v_pk_mul_f32 v[146:147], v[146:147], s[78:79]\n"
"v_pk_mul_f32 v[148:149], v[148:149], s[78:79]\n"
"v_pk_mul_f32 v[150:151], v[150:151], s[78:79]\n"
"v_cvt_pk_bf16_f32 v144, v144, v145\n"
"v_cvt_pk_bf16_f32 v145, v146, v147\n"
"v_cvt_pk_bf16_f32 v146, v148, v149\n"
"v_cvt_pk_bf16_f32 v147, v150, v151\n"
"v_pk_mul_f32 v[248:249], v[248:249], s[78:79]\n"
"v_pk_mul_f32 v[250:251], v[250:251], s[78:79]\n"
"v_pk_mul_f32 v[252:253], v[252:253], s[78:79]\n"
"v_pk_mul_f32 v[254:255], v[254:255], s[78:79]\n"
"v_cvt_pk_bf16_f32 v248, v248, v249\n"
"v_cvt_pk_bf16_f32 v249, v250, v251\n"
"v_cvt_pk_bf16_f32 v250, v252, v253\n"
"v_cvt_pk_bf16_f32 v251, v254, v255\n"
"v_pk_mul_f32 v[240:241], v[240:241], s[78:79]\n"
"v_pk_mul_f32 v[242:243], v[242:243], s[78:79]\n"
"v_pk_mul_f32 v[244:245], v[244:245], s[78:79]\n"
"v_pk_mul_f32 v[246:247], v[246:247], s[78:79]\n"
"v_cvt_pk_bf16_f32 v240, v240, v241\n"
"v_cvt_pk_bf16_f32 v241, v242, v243\n"
"v_cvt_pk_bf16_f32 v242, v244, v245\n"
"v_cvt_pk_bf16_f32 v243, v246, v247\n"
"v_pk_mul_f32 v[232:233], v[232:233], s[78:79]\n"
"v_pk_mul_f32 v[234:235], v[234:235], s[78:79]\n"
"v_pk_mul_f32 v[236:237], v[236:237], s[78:79]\n"
"v_pk_mul_f32 v[238:239], v[238:239], s[78:79]\n"
"v_cvt_pk_bf16_f32 v232, v232, v233\n"
"v_cvt_pk_bf16_f32 v233, v234, v235\n"
"v_cvt_pk_bf16_f32 v234, v236, v237\n"
"v_cvt_pk_bf16_f32 v235, v238, v239\n"
"v_pk_mul_f32 v[224:225], v[224:225], s[78:79]\n"
"v_pk_mul_f32 v[226:227], v[226:227], s[78:79]\n"
"v_pk_mul_f32 v[228:229], v[228:229], s[78:79]\n"
"v_pk_mul_f32 v[230:231], v[230:231], s[78:79]\n"
"v_cvt_pk_bf16_f32 v224, v224, v225\n"
"v_cvt_pk_bf16_f32 v225, v226, v227\n"
"v_cvt_pk_bf16_f32 v226, v228, v229\n"
"v_cvt_pk_bf16_f32 v227, v230, v231\n"
"v_pk_mul_f32 v[216:217], v[216:217], s[78:79]\n"
"v_pk_mul_f32 v[218:219], v[218:219], s[78:79]\n"
"v_pk_mul_f32 v[220:221], v[220:221], s[78:79]\n"
"v_pk_mul_f32 v[222:223], v[222:223], s[78:79]\n"
"v_cvt_pk_bf16_f32 v216, v216, v217\n"
"v_cvt_pk_bf16_f32 v217, v218, v219\n"
"v_cvt_pk_bf16_f32 v218, v220, v221\n"
"v_cvt_pk_bf16_f32 v219, v222, v223\n"
"v_pk_mul_f32 v[208:209], v[208:209], s[78:79]\n"
"v_pk_mul_f32 v[210:211], v[210:211], s[78:79]\n"
"v_pk_mul_f32 v[212:213], v[212:213], s[78:79]\n"
"v_pk_mul_f32 v[214:215], v[214:215], s[78:79]\n"
"v_cvt_pk_bf16_f32 v208, v208, v209\n"
"v_cvt_pk_bf16_f32 v209, v210, v211\n"
"v_cvt_pk_bf16_f32 v210, v212, v213\n"
"v_cvt_pk_bf16_f32 v211, v214, v215\n"
"v_pk_mul_f32 v[200:201], v[200:201], s[78:79]\n"
"v_pk_mul_f32 v[202:203], v[202:203], s[78:79]\n"
"v_pk_mul_f32 v[204:205], v[204:205], s[78:79]\n"
"v_pk_mul_f32 v[206:207], v[206:207], s[78:79]\n"
"v_cvt_pk_bf16_f32 v200, v200, v201\n"
"v_cvt_pk_bf16_f32 v201, v202, v203\n"
"v_cvt_pk_bf16_f32 v202, v204, v205\n"
"s_waitcnt lgkmcnt(3)\n"
"v_cvt_pk_bf16_f32 v203, v206, v207\n"
"v_mfma_f32_16x16x32_bf16 v[36:39], a[224:227], v[32:35], 0\n"
"s_waitcnt lgkmcnt(1)\n"
"v_mfma_f32_16x16x32_bf16 v[36:39], a[228:231], v[88:91], v[36:39]\n"
"v_mfma_f32_16x16x32_bf16 v[36:39], a[232:235], v[144:147], v[36:39]\n"
"s_waitcnt lgkmcnt(0)\n"
"v_mfma_f32_16x16x32_bf16 v[36:39], a[236:239], v[200:203], v[36:39]\n"
"v_mfma_f32_16x16x32_bf16 v[44:47], a[224:227], v[40:43], 0\n"
"s_xor_b32 s75, s40, 28672\n"
"v_mfma_f32_16x16x32_bf16 v[44:47], a[228:231], v[96:99], v[44:47]\n"
"v_mfma_f32_16x16x32_bf16 v[44:47], a[232:235], v[152:155], v[44:47]\n"
"s_add_u32 m0, s72, 102400\n"
"v_mfma_f32_16x16x32_bf16 v[44:47], a[236:239], v[208:211], v[44:47]\n"
"buffer_load_dwordx4 v4, s[16:19], s75 offen nt lds\n"
"v_mfma_f32_16x16x32_bf16 v[52:55], a[224:227], v[48:51], 0\n"
"v_exp_f32 v36, v36\n"
"v_mfma_f32_16x16x32_bf16 v[52:55], a[228:231], v[104:107], v[52:55]\n"
"v_mfma_f32_16x16x32_bf16 v[52:55], a[232:235], v[160:163], v[52:55]\n"
"v_exp_f32 v37, v37\n"
"v_mfma_f32_16x16x32_bf16 v[52:55], a[236:239], v[216:219], v[52:55]\n"
"buffer_load_dwordx4 v5, s[16:19], s75 offen offset:1024 nt lds\n"
"v_add_f32 v18, v18, v36\n"
"v_mfma_f32_16x16x32_bf16 v[60:63], a[224:227], v[56:59], 0\n"
"v_exp_f32 v38, v38\n"
"v_mfma_f32_16x16x32_bf16 v[60:63], a[228:231], v[112:115], v[60:63]\n"
"v_add_f32 v18, v18, v37\n"
"v_cvt_pk_bf16_f32 v148, v36, v37\n"
"v_mfma_f32_16x16x32_bf16 v[60:63], a[232:235], v[168:171], v[60:63]\n"
"v_exp_f32 v39, v39\n"
"v_mfma_f32_16x16x32_bf16 v[60:63], a[236:239], v[224:227], v[60:63]\n"
"v_add_f32 v18, v18, v38\n"
"v_mfma_f32_16x16x32_bf16 v[68:71], a[224:227], v[64:67], 0\n"
"v_exp_f32 v44, v44\n"
"v_mfma_f32_16x16x32_bf16 v[68:71], a[228:231], v[120:123], v[68:71]\n"
"v_add_f32 v18, v18, v39\n"
"v_cvt_pk_bf16_f32 v149, v38, v39\n"
"v_mfma_f32_16x16x32_bf16 v[68:71], a[232:235], v[176:179], v[68:71]\n"
"v_exp_f32 v45, v45\n"
"v_mfma_f32_16x16x32_bf16 v[68:71], a[236:239], v[232:235], v[68:71]\n"
"v_add_f32 v20, v20, v44\n"
"v_mfma_f32_16x16x32_bf16 v[76:79], a[224:227], v[72:75], 0\n"
"v_exp_f32 v46, v46\n"
"v_mfma_f32_16x16x32_bf16 v[76:79], a[228:231], v[128:131], v[76:79]\n"
"v_add_f32 v20, v20, v45\n"
"v_cvt_pk_bf16_f32 v156, v44, v45\n"
"v_mfma_f32_16x16x32_bf16 v[76:79], a[232:235], v[184:187], v[76:79]\n"
"v_exp_f32 v47, v47\n"
"v_mfma_f32_16x16x32_bf16 v[76:79], a[236:239], v[240:243], v[76:79]\n"
"v_add_f32 v20, v20, v46\n"
"v_mfma_f32_16x16x32_bf16 v[84:87], a[224:227], v[80:83], 0\n"
"v_exp_f32 v52, v52\n"
"v_mfma_f32_16x16x32_bf16 v[84:87], a[228:231], v[136:139], v[84:87]\n"
"v_add_f32 v20, v20, v47\n"
"v_cvt_pk_bf16_f32 v157, v46, v47\n"
"v_mfma_f32_16x16x32_bf16 v[84:87], a[232:235], v[192:195], v[84:87]\n"
"v_exp_f32 v53, v53\n"
"v_mfma_f32_16x16x32_bf16 v[84:87], a[236:239], v[248:251], v[84:87]\n"
"v_add_f32 v22, v22, v52\n"
"v_exp_f32 v54, v54\n"
"v_add_f32 v22, v22, v53\n"
"v_cvt_pk_bf16_f32 v164, v52, v53\n"
"v_exp_f32 v55, v55\n"
"ds_read_b128 a[224:227], v8 offset:4096\n"
"v_add_f32 v22, v22, v54\n"
"v_exp_f32 v60, v60\n"
"v_add_f32 v22, v22, v55\n"
"v_cvt_pk_bf16_f32 v165, v54, v55\n"
"v_exp_f32 v61, v61\n"
"ds_read_b128 a[228:231], v8 offset:4352\n"
"v_add_f32 v24, v24, v60\n"
"v_exp_f32 v62, v62\n"
"v_add_f32 v24, v24, v61\n"
"v_cvt_pk_bf16_f32 v172, v60, v61\n"
"v_exp_f32 v63, v63\n"
"ds_read_b128 a[232:235], v8 offset:4608\n"
"v_add_f32 v24, v24, v62\n"
"v_exp_f32 v68, v68\n"
"v_add_f32 v24, v24, v63\n"
"v_cvt_pk_bf16_f32 v173, v62, v63\n"
"v_exp_f32 v69, v69\n"
"ds_read_b128 a[236:239], v8 offset:4864\n"
"v_add_f32 v26, v26, v68\n"
"v_exp_f32 v70, v70\n"
"v_add_f32 v26, v26, v69\n"
"v_cvt_pk_bf16_f32 v180, v68, v69\n"
"v_exp_f32 v71, v71\n"
"v_add_f32 v26, v26, v70\n"
"s_xor_b32 s74, s40, 28672\n"
"v_exp_f32 v76, v76\n"
"v_add_f32 v26, v26, v71\n"
"v_cvt_pk_bf16_f32 v181, v70, v71\n"
"s_add_u32 m0, s72, 102400\n"
"v_exp_f32 v77, v77\n"
"buffer_load_dwordx4 v4, s[16:19], s74 offen offset:2048 nt lds\n"
"v_add_f32 v28, v28, v76\n"
"v_exp_f32 v78, v78\n"
"v_add_f32 v28, v28, v77\n"
"v_cvt_pk_bf16_f32 v188, v76, v77\n"
"s_waitcnt lgkmcnt(3)\n"
"v_exp_f32 v79, v79\n"
"buffer_load_dwordx4 v5, s[16:19], s74 offen offset:3072 nt lds\n"
"v_add_f32 v28, v28, v78\n"
"v_mfma_f32_16x16x32_bf16 v[92:95], a[224:227], v[32:35], 0\n"
"s_waitcnt lgkmcnt(1)\n"
"v_exp_f32 v84, v84\n"
"v_mfma_f32_16x16x32_bf16 v[92:95], a[228:231], v[88:91], v[92:95]\n"
"v_add_f32 v28, v28, v79\n"
"v_cvt_pk_bf16_f32 v189, v78, v79\n"
"v_mov_b32 v16, s68\n"
"v_mfma_f32_16x16x32_bf16 v[92:95], a[232:235], v[144:147], v[92:95]\n"
"s_waitcnt lgkmcnt(0)\n"
"v_exp_f32 v85, v85\n"
"v_mfma_f32_16x16x32_bf16 v[92:95], a[236:239], v[200:203], v[92:95]\n"
"v_add_f32 v30, v30, v84\n"
"v_mov_b32 v17, s70\n"
"v_mfma_f32_16x16x32_bf16 v[100:103], a[224:227], v[40:43], 0\n"
"v_exp_f32 v86, v86\n"
"v_mfma_f32_16x16x32_bf16 v[100:103], a[228:231], v[96:99], v[100:103]\n"
"s_mov_b32 s72, 24576\n"
"buffer_store_dwordx2 v[16:17], v19, s[20:23], s43 offen sc1\n"
"v_add_f32 v30, v30, v85\n"
"v_cvt_pk_bf16_f32 v196, v84, v85\n"
"v_mfma_f32_16x16x32_bf16 v[100:103], a[232:235], v[152:155], v[100:103]\n"
"v_exp_f32 v87, v87\n"
"v_mfma_f32_16x16x32_bf16 v[100:103], a[236:239], v[208:211], v[100:103]\n"
"s_or_b32 s37, s46, 106495\n"
"v_add_f32 v30, v30, v86\n"
"v_mfma_f32_16x16x32_bf16 v[108:111], a[224:227], v[48:51], 0\n"
"v_exp_f32 v92, v92\n"
"v_mfma_f32_16x16x32_bf16 v[108:111], a[228:231], v[104:107], v[108:111]\n"
"s_mov_b32 s36, 6\n"
"v_add_f32 v30, v30, v87\n"
"v_cvt_pk_bf16_f32 v197, v86, v87\n"
"v_mfma_f32_16x16x32_bf16 v[108:111], a[232:235], v[160:163], v[108:111]\n"
"v_exp_f32 v93, v93\n"
"v_mfma_f32_16x16x32_bf16 v[108:111], a[236:239], v[216:219], v[108:111]\n"
"s_mov_b32 s66, 0\n"
"v_add_f32 v18, v18, v92\n"
"v_mfma_f32_16x16x32_bf16 v[116:119], a[224:227], v[56:59], 0\n"
"v_exp_f32 v94, v94\n"
"v_mfma_f32_16x16x32_bf16 v[116:119], a[228:231], v[112:115], v[116:119]\n"
"v_add_f32 v18, v18, v93\n"
"v_cvt_pk_bf16_f32 v150, v92, v93\n"
"v_mfma_f32_16x16x32_bf16 v[116:119], a[232:235], v[168:171], v[116:119]\n"
"v_exp_f32 v95, v95\n"
"v_mfma_f32_16x16x32_bf16 v[116:119], a[236:239], v[224:227], v[116:119]\n"
"v_add_f32 v18, v18, v94\n"
"v_mfma_f32_16x16x32_bf16 v[124:127], a[224:227], v[64:67], 0\n"
"v_exp_f32 v100, v100\n"
"v_mfma_f32_16x16x32_bf16 v[124:127], a[228:231], v[120:123], v[124:127]\n"
"v_add_f32 v18, v18, v95\n"
"v_cvt_pk_bf16_f32 v151, v94, v95\n"
"v_mfma_f32_16x16x32_bf16 v[124:127], a[232:235], v[176:179], v[124:127]\n"
"v_exp_f32 v101, v101\n"
"v_mfma_f32_16x16x32_bf16 v[124:127], a[236:239], v[232:235], v[124:127]\n"
"v_add_f32 v20, v20, v100\n"
"v_mfma_f32_16x16x32_bf16 v[132:135], a[224:227], v[72:75], 0\n"
"v_exp_f32 v102, v102\n"
"v_mfma_f32_16x16x32_bf16 v[132:135], a[228:231], v[128:131], v[132:135]\n"
"v_add_f32 v20, v20, v101\n"
"v_cvt_pk_bf16_f32 v158, v100, v101\n"
"v_mfma_f32_16x16x32_bf16 v[132:135], a[232:235], v[184:187], v[132:135]\n"
"v_exp_f32 v103, v103\n"
"v_mfma_f32_16x16x32_bf16 v[132:135], a[236:239], v[240:243], v[132:135]\n"
"v_add_f32 v20, v20, v102\n"
"v_mfma_f32_16x16x32_bf16 v[140:143], a[224:227], v[80:83], 0\n"
"v_exp_f32 v108, v108\n"
"v_mfma_f32_16x16x32_bf16 v[140:143], a[228:231], v[136:139], v[140:143]\n"
"v_add_f32 v20, v20, v103\n"
"v_cvt_pk_bf16_f32 v159, v102, v103\n"
"v_mfma_f32_16x16x32_bf16 v[140:143], a[232:235], v[192:195], v[140:143]\n"
"v_exp_f32 v109, v109\n"
"v_mfma_f32_16x16x32_bf16 v[140:143], a[236:239], v[248:251], v[140:143]\n"
"v_add_f32 v22, v22, v108\n"
"v_exp_f32 v110, v110\n"
"v_add_f32 v22, v22, v109\n"
"v_cvt_pk_bf16_f32 v166, v108, v109\n"
"s_waitcnt vmcnt(17) lgkmcnt(0)\n"
"s_barrier\n"
"v_exp_f32 v111, v111\n"
"\n"
"2:\n"
"v_add_u32 v6, 65536, v1\n"
"v_add_u32 v7, s63, v1\n"
"v_add_u32 v2, 131072, v11\n"
"v_add_u32 v3, 131072, v12\n"
"v_add_u32 v21, 129024, v14\n"
"v_add_u32 v23, 129024, v15\n"
"ds_read_b128 a[224:227], v8 offset:32768\n"
"v_add_f32 v22, v22, v110\n"
"v_exp_f32 v116, v116\n"
"v_add_f32 v22, v22, v111\n"
"v_cvt_pk_bf16_f32 v167, v110, v111\n"
"v_exp_f32 v117, v117\n"
"ds_read_b128 a[228:231], v8 offset:33024\n"
"v_add_f32 v24, v24, v116\n"
"v_exp_f32 v118, v118\n"
"v_add_f32 v24, v24, v117\n"
"v_cvt_pk_bf16_f32 v174, v116, v117\n"
"v_exp_f32 v119, v119\n"
"ds_read_b128 a[232:235], v8 offset:33280\n"
"v_add_f32 v24, v24, v118\n"
"v_exp_f32 v124, v124\n"
"v_add_f32 v24, v24, v119\n"
"v_cvt_pk_bf16_f32 v175, v118, v119\n"
"v_exp_f32 v125, v125\n"
"ds_read_b128 a[236:239], v8 offset:33536\n"
"v_add_f32 v26, v26, v124\n"
"v_exp_f32 v126, v126\n"
"v_add_f32 v26, v26, v125\n"
"v_cvt_pk_bf16_f32 v182, v124, v125\n"
"v_exp_f32 v127, v127\n"
"ds_read_b64_tr_b16 a[240:241], v9 offset:0\n"
"v_add_f32 v26, v26, v126\n"
"v_exp_f32 v132, v132\n"
"v_add_f32 v26, v26, v127\n"
"v_cvt_pk_bf16_f32 v183, v126, v127\n"
"v_exp_f32 v133, v133\n"
"s_and_b32 m0, s72, s37\n"
"ds_read_b64_tr_b16 a[242:243], v9 offset:4096\n"
"v_add_f32 v28, v28, v132\n"
"v_exp_f32 v134, v134\n"
"s_cmp_lg_u32 s47, 0\n"
"v_add_f32 v28, v28, v133\n"
"v_cvt_pk_bf16_f32 v190, v132, v133\n"
"s_waitcnt lgkmcnt(5)\n"
"v_exp_f32 v135, v135\n"
"s_cbranch_scc1 140f\n"
"buffer_load_dwordx4 v4, s[16:19], s41 offen nt lds\n"
"buffer_load_dwordx4 v5, s[16:19], s41 offen offset:1024 nt lds\n"
"s_add_u32 s73, s73, 2048\n"
"s_add_u32 s72, s72, 2048\n"
"s_or_b32 s72, s72, 24576\n"
"s_xor_b32 s41, s40, s73\n"
"140:\n"
"v_add_f32 v28, v28, v134\n"
"v_mfma_f32_16x16x32_bf16 v[36:39], a[224:227], v[32:35], 0\n"
"s_waitcnt lgkmcnt(3)\n"
"v_exp_f32 v140, v140\n"
"v_mfma_f32_16x16x32_bf16 v[36:39], a[228:231], v[88:91], v[36:39]\n"
"v_add_f32 v28, v28, v135\n"
"v_cvt_pk_bf16_f32 v191, v134, v135\n"
"v_mfma_f32_16x16x32_bf16 v[36:39], a[232:235], v[144:147], v[36:39]\n"
"s_waitcnt lgkmcnt(2)\n"
"v_exp_f32 v141, v141\n"
"v_mfma_f32_16x16x32_bf16 v[36:39], a[236:239], v[200:203], v[36:39]\n"
"ds_read_b64_tr_b16 a[244:245], v10 offset:0\n"
"v_add_f32 v30, v30, v140\n"
"v_mfma_f32_16x16x32_bf16 v[44:47], a[224:227], v[40:43], 0\n"
"v_exp_f32 v142, v142\n"
"v_mfma_f32_16x16x32_bf16 v[44:47], a[228:231], v[96:99], v[44:47]\n"
"v_add_f32 v30, v30, v141\n"
"v_cvt_pk_bf16_f32 v198, v140, v141\n"
"v_mfma_f32_16x16x32_bf16 v[44:47], a[232:235], v[152:155], v[44:47]\n"
"v_exp_f32 v143, v143\n"
"v_mfma_f32_16x16x32_bf16 v[44:47], a[236:239], v[208:211], v[44:47]\n"
"ds_read_b64_tr_b16 a[246:247], v10 offset:4096\n"
"v_add_f32 v30, v30, v142\n"
"v_mfma_f32_16x16x32_bf16 v[52:55], a[224:227], v[48:51], 0\n"
"v_exp_f32 v36, v36\n"
"v_mfma_f32_16x16x32_bf16 v[52:55], a[228:231], v[104:107], v[52:55]\n"
"v_add_f32 v30, v30, v143\n"
"v_cvt_pk_bf16_f32 v199, v142, v143\n"
"v_mfma_f32_16x16x32_bf16 v[52:55], a[232:235], v[160:163], v[52:55]\n"
"v_exp_f32 v37, v37\n"
"v_mfma_f32_16x16x32_bf16 v[52:55], a[236:239], v[216:219], v[52:55]\n"
"ds_read_b64_tr_b16 a[248:249], v9 offset:256\n"
"v_add_f32 v18, v18, v36\n"
"v_mfma_f32_16x16x32_bf16 v[60:63], a[224:227], v[56:59], 0\n"
"v_exp_f32 v38, v38\n"
"v_mfma_f32_16x16x32_bf16 v[60:63], a[228:231], v[112:115], v[60:63]\n"
"v_add_f32 v18, v18, v37\n"
"v_cvt_pk_bf16_f32 v204, v36, v37\n"
"v_mfma_f32_16x16x32_bf16 v[60:63], a[232:235], v[168:171], v[60:63]\n"
"v_exp_f32 v39, v39\n"
"v_mfma_f32_16x16x32_bf16 v[60:63], a[236:239], v[224:227], v[60:63]\n"
"ds_read_b64_tr_b16 a[250:251], v9 offset:4352\n"
"v_add_f32 v18, v18, v38\n"
"v_mfma_f32_16x16x32_bf16 v[68:71], a[224:227], v[64:67], 0\n"
"v_exp_f32 v44, v44\n"
"v_mfma_f32_16x16x32_bf16 v[68:71], a[228:231], v[120:123], v[68:71]\n"
"v_add_f32 v18, v18, v39\n"
"v_cvt_pk_bf16_f32 v205, v38, v39\n"
"v_mfma_f32_16x16x32_bf16 v[68:71], a[232:235], v[176:179], v[68:71]\n"
"v_exp_f32 v45, v45\n"
"v_mfma_f32_16x16x32_bf16 v[68:71], a[236:239], v[232:235], v[68:71]\n"
"ds_read_b64_tr_b16 a[252:253], v10 offset:256\n"
"v_add_f32 v20, v20, v44\n"
"v_mfma_f32_16x16x32_bf16 v[76:79], a[224:227], v[72:75], 0\n"
"v_exp_f32 v46, v46\n"
"v_mfma_f32_16x16x32_bf16 v[76:79], a[228:231], v[128:131], v[76:79]\n"
"v_add_f32 v20, v20, v45\n"
"v_cvt_pk_bf16_f32 v212, v44, v45\n"
"v_mfma_f32_16x16x32_bf16 v[76:79], a[232:235], v[184:187], v[76:79]\n"
"v_exp_f32 v47, v47\n"
"v_mfma_f32_16x16x32_bf16 v[76:79], a[236:239], v[240:243], v[76:79]\n"
"s_and_b32 m0, s72, s37\n"
"ds_read_b64_tr_b16 a[254:255], v10 offset:4352\n"
"v_add_f32 v20, v20, v46\n"
"v_mfma_f32_16x16x32_bf16 v[84:87], a[224:227], v[80:83], 0\n"
"v_exp_f32 v52, v52\n"
"v_mfma_f32_16x16x32_bf16 v[84:87], a[228:231], v[136:139], v[84:87]\n"
"s_cmp_lg_u32 s47, 0\n"
"v_add_f32 v20, v20, v47\n"
"v_cvt_pk_bf16_f32 v213, v46, v47\n"
"v_mfma_f32_16x16x32_bf16 v[84:87], a[232:235], v[192:195], v[84:87]\n"
"s_waitcnt lgkmcnt(6)\n"
"v_exp_f32 v53, v53\n"
"v_mfma_f32_16x16x32_bf16 v[84:87], a[236:239], v[248:251], v[84:87]\n"
"s_cbranch_scc1 141f\n"
"buffer_load_dwordx4 v4, s[16:19], s41 offen nt lds\n"
"buffer_load_dwordx4 v5, s[16:19], s41 offen offset:1024 nt lds\n"
"s_add_u32 s73, s73, 2048\n"
"s_add_u32 s72, s72, 2048\n"
"s_or_b32 s72, s72, 24576\n"
"s_xor_b32 s41, s40, s73\n"
"141:\n"
"v_add_f32 v22, v22, v52\n"
"v_mfma_f32_16x16x32_bf16 a[0:3], a[240:243], v[148:151], 0\n"
"v_exp_f32 v54, v54\n"
"v_mfma_f32_16x16x32_bf16 a[4:7], a[240:243], v[156:159], 0\n"
"v_add_f32 v22, v22, v53\n"
"v_cvt_pk_bf16_f32 v220, v52, v53\n"
"v_mfma_f32_16x16x32_bf16 a[8:11], a[240:243], v[164:167], 0\n"
"v_exp_f32 v55, v55\n"
"v_mfma_f32_16x16x32_bf16 a[12:15], a[240:243], v[172:175], 0\n"
"ds_read_b128 a[224:227], v8 offset:36864\n"
"v_add_f32 v22, v22, v54\n"
"v_mfma_f32_16x16x32_bf16 a[16:19], a[240:243], v[180:183], 0\n"
"v_exp_f32 v60, v60\n"
"v_mfma_f32_16x16x32_bf16 a[20:23], a[240:243], v[188:191], 0\n"
"v_add_f32 v22, v22, v55\n"
"v_cvt_pk_bf16_f32 v221, v54, v55\n"
"v_mfma_f32_16x16x32_bf16 a[24:27], a[240:243], v[196:199], 0\n"
"s_waitcnt lgkmcnt(5)\n"
"v_exp_f32 v61, v61\n"
"v_mfma_f32_16x16x32_bf16 a[28:31], a[244:247], v[148:151], 0\n"
"ds_read_b128 a[228:231], v8 offset:37120\n"
"v_add_f32 v24, v24, v60\n"
"v_mfma_f32_16x16x32_bf16 a[32:35], a[244:247], v[156:159], 0\n"
"v_exp_f32 v62, v62\n"
"v_mfma_f32_16x16x32_bf16 a[36:39], a[244:247], v[164:167], 0\n"
"v_add_f32 v24, v24, v61\n"
"v_cvt_pk_bf16_f32 v228, v60, v61\n"
"v_mfma_f32_16x16x32_bf16 a[40:43], a[244:247], v[172:175], 0\n"
"v_exp_f32 v63, v63\n"
"v_mfma_f32_16x16x32_bf16 a[44:47], a[244:247], v[180:183], 0\n"
"ds_read_b128 a[232:235], v8 offset:37376\n"
"v_add_f32 v24, v24, v62\n"
"v_mfma_f32_16x16x32_bf16 a[48:51], a[244:247], v[188:191], 0\n"
"s_waitcnt lgkmcnt(5)\n"
"v_exp_f32 v68, v68\n"
"v_mfma_f32_16x16x32_bf16 a[52:55], a[244:247], v[196:199], 0\n"
"v_add_f32 v24, v24, v63\n"
"v_cvt_pk_bf16_f32 v229, v62, v63\n"
"v_mfma_f32_16x16x32_bf16 a[56:59], a[248:251], v[148:151], 0\n"
"v_exp_f32 v69, v69\n"
"v_mfma_f32_16x16x32_bf16 a[60:63], a[248:251], v[156:159], 0\n"
"ds_read_b128 a[236:239], v8 offset:37632\n"
"v_add_f32 v26, v26, v68\n"
"v_mfma_f32_16x16x32_bf16 a[64:67], a[248:251], v[164:167], 0\n"
"v_exp_f32 v70, v70\n"
"v_mfma_f32_16x16x32_bf16 a[68:71], a[248:251], v[172:175], 0\n"
"v_add_f32 v26, v26, v69\n"
"v_cvt_pk_bf16_f32 v236, v68, v69\n"
"v_mfma_f32_16x16x32_bf16 a[72:75], a[248:251], v[180:183], 0\n"
"v_exp_f32 v71, v71\n"
"v_mfma_f32_16x16x32_bf16 a[76:79], a[248:251], v[188:191], 0\n"
"ds_read_b64_tr_b16 a[240:241], v9 offset:512\n"
"v_add_f32 v26, v26, v70\n"
"v_mfma_f32_16x16x32_bf16 a[80:83], a[248:251], v[196:199], 0\n"
"s_waitcnt lgkmcnt(5)\n"
"v_exp_f32 v76, v76\n"
"v_mfma_f32_16x16x32_bf16 a[84:87], a[252:255], v[148:151], 0\n"
"v_add_f32 v26, v26, v71\n"
"v_cvt_pk_bf16_f32 v237, v70, v71\n"
"v_mfma_f32_16x16x32_bf16 a[88:91], a[252:255], v[156:159], 0\n"
"v_exp_f32 v77, v77\n"
"v_mfma_f32_16x16x32_bf16 a[92:95], a[252:255], v[164:167], 0\n"
"s_and_b32 m0, s72, s37\n"
"ds_read_b64_tr_b16 a[242:243], v9 offset:4608\n"
"v_add_f32 v28, v28, v76\n"
"v_mfma_f32_16x16x32_bf16 a[96:99], a[252:255], v[172:175], 0\n"
"v_exp_f32 v78, v78\n"
"v_mfma_f32_16x16x32_bf16 a[100:103], a[252:255], v[180:183], 0\n"
"s_cmp_lg_u32 s47, 0\n"
"v_add_f32 v28, v28, v77\n"
"v_cvt_pk_bf16_f32 v244, v76, v77\n"
"v_mfma_f32_16x16x32_bf16 a[104:107], a[252:255], v[188:191], 0\n"
"s_waitcnt lgkmcnt(5)\n"
"v_exp_f32 v79, v79\n"
"v_mfma_f32_16x16x32_bf16 a[108:111], a[252:255], v[196:199], 0\n"
"s_cbranch_scc1 142f\n"
"buffer_load_dwordx4 v4, s[16:19], s41 offen nt lds\n"
"buffer_load_dwordx4 v5, s[16:19], s41 offen offset:1024 nt lds\n"
"s_add_u32 s73, s73, 2048\n"
"s_add_u32 s72, s72, 2048\n"
"s_or_b32 s72, s72, 24576\n"
"s_xor_b32 s41, s40, s73\n"
"142:\n"
"v_add_f32 v28, v28, v78\n"
"v_mfma_f32_16x16x32_bf16 v[92:95], a[224:227], v[32:35], 0\n"
"s_waitcnt lgkmcnt(3)\n"
"v_exp_f32 v84, v84\n"
"v_mfma_f32_16x16x32_bf16 v[92:95], a[228:231], v[88:91], v[92:95]\n"
"v_add_f32 v28, v28, v79\n"
"v_cvt_pk_bf16_f32 v245, v78, v79\n"
"v_mfma_f32_16x16x32_bf16 v[92:95], a[232:235], v[144:147], v[92:95]\n"
"s_waitcnt lgkmcnt(2)\n"
"v_exp_f32 v85, v85\n"
"v_mfma_f32_16x16x32_bf16 v[92:95], a[236:239], v[200:203], v[92:95]\n"
"ds_read_b64_tr_b16 a[244:245], v10 offset:512\n"
"v_add_f32 v30, v30, v84\n"
"v_mfma_f32_16x16x32_bf16 v[100:103], a[224:227], v[40:43], 0\n"
"v_exp_f32 v86, v86\n"
"v_mfma_f32_16x16x32_bf16 v[100:103], a[228:231], v[96:99], v[100:103]\n"
"v_add_f32 v30, v30, v85\n"
"v_cvt_pk_bf16_f32 v252, v84, v85\n"
"v_mfma_f32_16x16x32_bf16 v[100:103], a[232:235], v[152:155], v[100:103]\n"
"v_exp_f32 v87, v87\n"
"v_mfma_f32_16x16x32_bf16 v[100:103], a[236:239], v[208:211], v[100:103]\n"
"ds_read_b64_tr_b16 a[246:247], v10 offset:4608\n"
"v_add_f32 v30, v30, v86\n"
"v_mfma_f32_16x16x32_bf16 v[108:111], a[224:227], v[48:51], 0\n"
"v_exp_f32 v92, v92\n"
"v_mfma_f32_16x16x32_bf16 v[108:111], a[228:231], v[104:107], v[108:111]\n"
"v_add_f32 v30, v30, v87\n"
"v_cvt_pk_bf16_f32 v253, v86, v87\n"
"v_mfma_f32_16x16x32_bf16 v[108:111], a[232:235], v[160:163], v[108:111]\n"
"v_exp_f32 v93, v93\n"
"v_mfma_f32_16x16x32_bf16 v[108:111], a[236:239], v[216:219], v[108:111]\n"
"ds_read_b64_tr_b16 a[248:249], v9 offset:768\n"
"v_add_f32 v18, v18, v92\n"
"v_mfma_f32_16x16x32_bf16 v[116:119], a[224:227], v[56:59], 0\n"
"v_exp_f32 v94, v94\n"
"v_mfma_f32_16x16x32_bf16 v[116:119], a[228:231], v[112:115], v[116:119]\n"
"v_add_f32 v18, v18, v93\n"
"v_cvt_pk_bf16_f32 v206, v92, v93\n"
"v_mfma_f32_16x16x32_bf16 v[116:119], a[232:235], v[168:171], v[116:119]\n"
"v_exp_f32 v95, v95\n"
"v_mfma_f32_16x16x32_bf16 v[116:119], a[236:239], v[224:227], v[116:119]\n"
"s_mov_b64 vcc, -1\n"
"ds_read_b64_tr_b16 a[250:251], v9 offset:4864\n"
"v_add_f32 v18, v18, v94\n"
"v_mfma_f32_16x16x32_bf16 v[124:127], a[224:227], v[64:67], 0\n"
"v_exp_f32 v100, v100\n"
"v_mfma_f32_16x16x32_bf16 v[124:127], a[228:231], v[120:123], v[124:127]\n"
"s_memrealtime s[78:79]\n"
"v_add_f32 v18, v18, v95\n"
"v_cvt_pk_bf16_f32 v207, v94, v95\n"
"v_mfma_f32_16x16x32_bf16 v[124:127], a[232:235], v[176:179], v[124:127]\n"
"v_exp_f32 v101, v101\n"
"v_mfma_f32_16x16x32_bf16 v[124:127], a[236:239], v[232:235], v[124:127]\n"
"ds_read_b64_tr_b16 a[252:253], v10 offset:768\n"
"v_add_f32 v20, v20, v100\n"
"v_mfma_f32_16x16x32_bf16 v[132:135], a[224:227], v[72:75], 0\n"
"v_exp_f32 v102, v102\n"
"v_mfma_f32_16x16x32_bf16 v[132:135], a[228:231], v[128:131], v[132:135]\n"
"v_add_f32 v20, v20, v101\n"
"v_cvt_pk_bf16_f32 v214, v100, v101\n"
"v_mfma_f32_16x16x32_bf16 v[132:135], a[232:235], v[184:187], v[132:135]\n"
"v_exp_f32 v103, v103\n"
"v_mfma_f32_16x16x32_bf16 v[132:135], a[236:239], v[240:243], v[132:135]\n"
"s_and_b32 m0, s72, s37\n"
"ds_read_b64_tr_b16 a[254:255], v10 offset:4864\n"
"v_add_f32 v20, v20, v102\n"
"v_mfma_f32_16x16x32_bf16 v[140:143], a[224:227], v[80:83], 0\n"
"v_exp_f32 v108, v108\n"
"v_mfma_f32_16x16x32_bf16 v[140:143], a[228:231], v[136:139], v[140:143]\n"
"s_cmp_lg_u32 s47, 0\n"
"v_add_f32 v20, v20, v103\n"
"v_cvt_pk_bf16_f32 v215, v102, v103\n"
"v_mfma_f32_16x16x32_bf16 v[140:143], a[232:235], v[192:195], v[140:143]\n"
"s_waitcnt lgkmcnt(6)\n"
"v_exp_f32 v109, v109\n"
"v_mfma_f32_16x16x32_bf16 v[140:143], a[236:239], v[248:251], v[140:143]\n"
"s_cbranch_scc1 143f\n"
"buffer_load_dwordx4 v4, s[16:19], s41 offen nt lds\n"
"buffer_load_dwordx4 v5, s[16:19], s41 offen offset:1024 nt lds\n"
"s_add_u32 s73, s73, 2048\n"
"s_add_u32 s72, s72, 2048\n"
"s_or_b32 s72, s72, 24576\n"
"s_xor_b32 s41, s40, s73\n"
"143:\n"
"v_add_f32 v22, v22, v108\n"
"v_mfma_f32_16x16x32_bf16 a[112:115], a[240:243], v[148:151], 0\n"
"v_exp_f32 v110, v110\n"
"v_mfma_f32_16x16x32_bf16 a[116:119], a[240:243], v[156:159], 0\n"
"v_add_f32 v22, v22, v109\n"
"v_cvt_pk_bf16_f32 v222, v108, v109\n"
"v_xor_b32 v8, 65536, v8\n"
"v_mfma_f32_16x16x32_bf16 a[120:123], a[240:243], v[164:167], 0\n"
"s_waitcnt vmcnt(17) lgkmcnt(0)\n"
"s_barrier\n"
"s_cbranch_vccnz 323f\n"
"333:\n"
"v_exp_f32 v111, v111\n"
"v_mfma_f32_16x16x32_bf16 a[124:127], a[240:243], v[172:175], 0\n"
"s_mov_b32 s39, s78\n"
"ds_read_b128 a[224:227], v8 offset:0\n"
"v_add_f32 v22, v22, v110\n"
"v_mfma_f32_16x16x32_bf16 a[128:131], a[240:243], v[180:183], 0\n"
"v_exp_f32 v116, v116\n"
"v_mfma_f32_16x16x32_bf16 a[132:135], a[240:243], v[188:191], 0\n"
"v_add_f32 v22, v22, v111\n"
"v_cvt_pk_bf16_f32 v223, v110, v111\n"
"v_mfma_f32_16x16x32_bf16 a[136:139], a[240:243], v[196:199], 0\n"
"v_exp_f32 v117, v117\n"
"v_mfma_f32_16x16x32_bf16 a[140:143], a[244:247], v[148:151], 0\n"
"ds_read_b128 a[228:231], v8 offset:256\n"
"v_add_f32 v24, v24, v116\n"
"v_mfma_f32_16x16x32_bf16 a[144:147], a[244:247], v[156:159], 0\n"
"v_exp_f32 v118, v118\n"
"v_mfma_f32_16x16x32_bf16 a[148:151], a[244:247], v[164:167], 0\n"
"v_add_f32 v24, v24, v117\n"
"v_cvt_pk_bf16_f32 v230, v116, v117\n"
"v_mfma_f32_16x16x32_bf16 a[152:155], a[244:247], v[172:175], 0\n"
"v_exp_f32 v119, v119\n"
"v_mfma_f32_16x16x32_bf16 a[156:159], a[244:247], v[180:183], 0\n"
"ds_read_b128 a[232:235], v8 offset:512\n"
"v_add_f32 v24, v24, v118\n"
"v_mfma_f32_16x16x32_bf16 a[160:163], a[244:247], v[188:191], 0\n"
"v_exp_f32 v124, v124\n"
"v_mfma_f32_16x16x32_bf16 a[164:167], a[244:247], v[196:199], 0\n"
"v_add_f32 v24, v24, v119\n"
"v_cvt_pk_bf16_f32 v231, v118, v119\n"
"v_mfma_f32_16x16x32_bf16 a[168:171], a[248:251], v[148:151], 0\n"
"v_exp_f32 v125, v125\n"
"v_mfma_f32_16x16x32_bf16 a[172:175], a[248:251], v[156:159], 0\n"
"s_cbranch_vccnz 324f\n"
"334:\n"
"ds_read_b128 a[236:239], v8 offset:768\n"
"v_add_f32 v26, v26, v124\n"
"v_mfma_f32_16x16x32_bf16 a[176:179], a[248:251], v[164:167], 0\n"
"v_exp_f32 v126, v126\n"
"v_mfma_f32_16x16x32_bf16 a[180:183], a[248:251], v[172:175], 0\n"
"v_add_f32 v26, v26, v125\n"
"v_cvt_pk_bf16_f32 v238, v124, v125\n"
"v_mfma_f32_16x16x32_bf16 a[184:187], a[248:251], v[180:183], 0\n"
"v_exp_f32 v127, v127\n"
"v_mfma_f32_16x16x32_bf16 a[188:191], a[248:251], v[188:191], 0\n"
"ds_read_b64_tr_b16 a[240:241], v9 offset:32768\n"
"v_add_f32 v26, v26, v126\n"
"v_mfma_f32_16x16x32_bf16 a[192:195], a[248:251], v[196:199], 0\n"
"v_exp_f32 v132, v132\n"
"v_mfma_f32_16x16x32_bf16 a[196:199], a[252:255], v[148:151], 0\n"
"s_and_b32 m0, s72, s37\n"
"v_add_f32 v26, v26, v127\n"
"v_cvt_pk_bf16_f32 v239, v126, v127\n"
"v_mfma_f32_16x16x32_bf16 a[200:203], a[252:255], v[156:159], 0\n"
"v_exp_f32 v133, v133\n"
"v_mfma_f32_16x16x32_bf16 a[204:207], a[252:255], v[164:167], 0\n"
"ds_read_b64_tr_b16 a[242:243], v9 offset:36864\n"
"v_add_f32 v28, v28, v132\n"
"v_mfma_f32_16x16x32_bf16 a[208:211], a[252:255], v[172:175], 0\n"
"v_exp_f32 v134, v134\n"
"v_mfma_f32_16x16x32_bf16 a[212:215], a[252:255], v[180:183], 0\n"
"v_add_f32 v28, v28, v133\n"
"v_cvt_pk_bf16_f32 v246, v132, v133\n"
"v_mfma_f32_16x16x32_bf16 a[216:219], a[252:255], v[188:191], 0\n"
"s_waitcnt lgkmcnt(5)\n"
"v_exp_f32 v135, v135\n"
"v_mfma_f32_16x16x32_bf16 a[220:223], a[252:255], v[196:199], 0\n"
"buffer_load_dwordx4 v4, s[16:19], s41 offen nt lds\n"
"buffer_load_dwordx4 v5, s[16:19], s41 offen offset:1024 nt lds\n"
"v_add_f32 v28, v28, v134\n"
".p2align 6\n"
"3:\n"
"v_mfma_f32_16x16x32_bf16 v[36:39], a[224:227], v[32:35], 0\n"
"s_waitcnt lgkmcnt(3)\n"
"v_exp_f32 v140, v140\n"
"v_mfma_f32_16x16x32_bf16 v[36:39], a[228:231], v[88:91], v[36:39]\n"
"s_add_u32 s73, s73, 2048\n"
"s_cbranch_vccnz 340f\n"
"350:\n"
"v_add_f32 v28, v28, v135\n"
"v_cvt_pk_bf16_f32 v247, v134, v135\n"
"v_mfma_f32_16x16x32_bf16 v[36:39], a[232:235], v[144:147], v[36:39]\n"
"s_waitcnt lgkmcnt(2)\n"
"v_exp_f32 v141, v141\n"
"v_mfma_f32_16x16x32_bf16 v[36:39], a[236:239], v[200:203], v[36:39]\n"
"s_add_u32 s72, s72, 2048\n"
"ds_read_b64_tr_b16 a[244:245], v10 offset:32768\n"
"v_add_f32 v30, v30, v140\n"
"v_mfma_f32_16x16x32_bf16 v[44:47], a[224:227], v[40:43], 0\n"
"v_exp_f32 v142, v142\n"
"v_mfma_f32_16x16x32_bf16 v[44:47], a[228:231], v[96:99], v[44:47]\n"
"s_or_b32 s72, s72, 24576\n"
"v_add_f32 v30, v30, v141\n"
"v_cvt_pk_bf16_f32 v254, v140, v141\n"
"v_mfma_f32_16x16x32_bf16 v[44:47], a[232:235], v[152:155], v[44:47]\n"
"v_exp_f32 v143, v143\n"
"v_mfma_f32_16x16x32_bf16 v[44:47], a[236:239], v[208:211], v[44:47]\n"
"s_xor_b32 s41, s40, s73\n"
"ds_read_b64_tr_b16 a[246:247], v10 offset:36864\n"
"v_add_f32 v30, v30, v142\n"
"v_mfma_f32_16x16x32_bf16 v[52:55], a[224:227], v[48:51], 0\n"
"v_exp_f32 v36, v36\n"
"v_mfma_f32_16x16x32_bf16 v[52:55], a[228:231], v[104:107], v[52:55]\n"
"v_add_f32 v30, v30, v143\n"
"v_cvt_pk_bf16_f32 v255, v142, v143\n"
"v_mfma_f32_16x16x32_bf16 v[52:55], a[232:235], v[160:163], v[52:55]\n"
"v_exp_f32 v37, v37\n"
"v_mfma_f32_16x16x32_bf16 v[52:55], a[236:239], v[216:219], v[52:55]\n"
"ds_read_b64_tr_b16 a[248:249], v9 offset:33024\n"
"v_add_f32 v18, v18, v36\n"
"v_mfma_f32_16x16x32_bf16 v[60:63], a[224:227], v[56:59], 0\n"
"v_exp_f32 v38, v38\n"
"v_mfma_f32_16x16x32_bf16 v[60:63], a[228:231], v[112:115], v[60:63]\n"
"v_add_f32 v18, v18, v37\n"
"v_cvt_pk_bf16_f32 v148, v36, v37\n"
"v_mfma_f32_16x16x32_bf16 v[60:63], a[232:235], v[168:171], v[60:63]\n"
"v_exp_f32 v39, v39\n"
"v_mfma_f32_16x16x32_bf16 v[60:63], a[236:239], v[224:227], v[60:63]\n"
"ds_read_b64_tr_b16 a[250:251], v9 offset:37120\n"
"v_add_f32 v18, v18, v38\n"
"v_mfma_f32_16x16x32_bf16 v[68:71], a[224:227], v[64:67], 0\n"
"v_exp_f32 v44, v44\n"
"v_mfma_f32_16x16x32_bf16 v[68:71], a[228:231], v[120:123], v[68:71]\n"
"v_add_f32 v18, v18, v39\n"
"v_cvt_pk_bf16_f32 v149, v38, v39\n"
"v_mfma_f32_16x16x32_bf16 v[68:71], a[232:235], v[176:179], v[68:71]\n"
"v_exp_f32 v45, v45\n"
"v_mfma_f32_16x16x32_bf16 v[68:71], a[236:239], v[232:235], v[68:71]\n"
"ds_read_b64_tr_b16 a[252:253], v10 offset:33024\n"
"v_add_f32 v20, v20, v44\n"
"v_mfma_f32_16x16x32_bf16 v[76:79], a[224:227], v[72:75], 0\n"
"v_exp_f32 v46, v46\n"
"v_mfma_f32_16x16x32_bf16 v[76:79], a[228:231], v[128:131], v[76:79]\n"
"s_and_b32 m0, s72, s37\n"
"v_add_f32 v20, v20, v45\n"
"v_cvt_pk_bf16_f32 v156, v44, v45\n"
"v_mfma_f32_16x16x32_bf16 v[76:79], a[232:235], v[184:187], v[76:79]\n"
"v_exp_f32 v47, v47\n"
"v_mfma_f32_16x16x32_bf16 v[76:79], a[236:239], v[240:243], v[76:79]\n"
"s_getreg_b32 s74, hwreg(HW_REG_IB_STS, 0, 4)\n"
"ds_read_b64_tr_b16 a[254:255], v10 offset:37120\n"
"v_add_f32 v20, v20, v46\n"
"v_mfma_f32_16x16x32_bf16 v[84:87], a[224:227], v[80:83], 0\n"
"v_exp_f32 v52, v52\n"
"v_mfma_f32_16x16x32_bf16 v[84:87], a[228:231], v[136:139], v[84:87]\n"
"s_cmp_gt_u32 s74, s36\n"
"v_add_f32 v20, v20, v47\n"
"v_cvt_pk_bf16_f32 v157, v46, v47\n"
"v_mfma_f32_16x16x32_bf16 v[84:87], a[232:235], v[192:195], v[84:87]\n"
"s_waitcnt lgkmcnt(6)\n"
"v_exp_f32 v53, v53\n"
"v_mfma_f32_16x16x32_bf16 v[84:87], a[236:239], v[248:251], v[84:87]\n"
"s_cbranch_scc1 100f\n"
"buffer_load_dwordx4 v4, s[16:19], s41 offen nt lds\n"
"buffer_load_dwordx4 v5, s[16:19], s41 offen offset:1024 nt lds\n"
"110:\n"
"v_add_f32 v22, v22, v52\n"
"v_mfma_f32_16x16x32_bf16 a[0:3], a[240:243], v[204:207], a[0:3]\n"
"v_exp_f32 v54, v54\n"
"v_mfma_f32_16x16x32_bf16 a[4:7], a[240:243], v[212:215], a[4:7]\n"
"s_add_u32 s73, s73, 2048\n"
"s_cbranch_vccnz 341f\n"
"351:\n"
"v_add_f32 v22, v22, v53\n"
"v_cvt_pk_bf16_f32 v164, v52, v53\n"
"v_mfma_f32_16x16x32_bf16 a[8:11], a[240:243], v[220:223], a[8:11]\n"
"v_exp_f32 v55, v55\n"
"v_mfma_f32_16x16x32_bf16 a[12:15], a[240:243], v[228:231], a[12:15]\n"
"s_add_u32 s72, s72, 2048\n"
"ds_read_b128 a[224:227], v8 offset:4096\n"
"v_add_f32 v22, v22, v54\n"
"v_mfma_f32_16x16x32_bf16 a[16:19], a[240:243], v[236:239], a[16:19]\n"
"v_exp_f32 v60, v60\n"
"v_mfma_f32_16x16x32_bf16 a[20:23], a[240:243], v[244:247], a[20:23]\n"
"s_or_b32 s72, s72, 24576\n"
"v_add_f32 v22, v22, v55\n"
"v_cvt_pk_bf16_f32 v165, v54, v55\n"
"v_mfma_f32_16x16x32_bf16 a[24:27], a[240:243], v[252:255], a[24:27]\n"
"s_waitcnt lgkmcnt(5)\n"
"v_exp_f32 v61, v61\n"
"v_mfma_f32_16x16x32_bf16 a[28:31], a[244:247], v[204:207], a[28:31]\n"
"s_xor_b32 s41, s40, s73\n"
"ds_read_b128 a[228:231], v8 offset:4352\n"
"v_add_f32 v24, v24, v60\n"
"v_mfma_f32_16x16x32_bf16 a[32:35], a[244:247], v[212:215], a[32:35]\n"
"v_exp_f32 v62, v62\n"
"v_mfma_f32_16x16x32_bf16 a[36:39], a[244:247], v[220:223], a[36:39]\n"
"v_add_f32 v24, v24, v61\n"
"v_cvt_pk_bf16_f32 v172, v60, v61\n"
"v_mfma_f32_16x16x32_bf16 a[40:43], a[244:247], v[228:231], a[40:43]\n"
"v_exp_f32 v63, v63\n"
"v_mfma_f32_16x16x32_bf16 a[44:47], a[244:247], v[236:239], a[44:47]\n"
"ds_read_b128 a[232:235], v8 offset:4608\n"
"v_add_f32 v24, v24, v62\n"
"v_mfma_f32_16x16x32_bf16 a[48:51], a[244:247], v[244:247], a[48:51]\n"
"s_waitcnt lgkmcnt(5)\n"
"v_exp_f32 v68, v68\n"
"v_mfma_f32_16x16x32_bf16 a[52:55], a[244:247], v[252:255], a[52:55]\n"
"v_add_f32 v24, v24, v63\n"
"v_cvt_pk_bf16_f32 v173, v62, v63\n"
"v_mfma_f32_16x16x32_bf16 a[56:59], a[248:251], v[204:207], a[56:59]\n"
"v_exp_f32 v69, v69\n"
"v_mfma_f32_16x16x32_bf16 a[60:63], a[248:251], v[212:215], a[60:63]\n"
"ds_read_b128 a[236:239], v8 offset:4864\n"
"v_add_f32 v26, v26, v68\n"
"v_mfma_f32_16x16x32_bf16 a[64:67], a[248:251], v[220:223], a[64:67]\n"
"v_exp_f32 v70, v70\n"
"v_mfma_f32_16x16x32_bf16 a[68:71], a[248:251], v[228:231], a[68:71]\n"
"v_add_f32 v26, v26, v69\n"
"v_cvt_pk_bf16_f32 v180, v68, v69\n"
"v_mfma_f32_16x16x32_bf16 a[72:75], a[248:251], v[236:239], a[72:75]\n"
"v_exp_f32 v71, v71\n"
"v_mfma_f32_16x16x32_bf16 a[76:79], a[248:251], v[244:247], a[76:79]\n"
"ds_read_b64_tr_b16 a[240:241], v9 offset:33280\n"
"v_add_f32 v26, v26, v70\n"
"v_mfma_f32_16x16x32_bf16 a[80:83], a[248:251], v[252:255], a[80:83]\n"
"s_waitcnt lgkmcnt(5)\n"
"v_exp_f32 v76, v76\n"
"v_mfma_f32_16x16x32_bf16 a[84:87], a[252:255], v[204:207], a[84:87]\n"
"s_and_b32 m0, s72, s37\n"
"v_add_f32 v26, v26, v71\n"
"v_cvt_pk_bf16_f32 v181, v70, v71\n"
"v_mfma_f32_16x16x32_bf16 a[88:91], a[252:255], v[212:215], a[88:91]\n"
"v_exp_f32 v77, v77\n"
"v_mfma_f32_16x16x32_bf16 a[92:95], a[252:255], v[220:223], a[92:95]\n"
"s_getreg_b32 s74, hwreg(HW_REG_IB_STS, 0, 4)\n"
"ds_read_b64_tr_b16 a[242:243], v9 offset:37376\n"
"v_add_f32 v28, v28, v76\n"
"v_mfma_f32_16x16x32_bf16 a[96:99], a[252:255], v[228:231], a[96:99]\n"
"v_exp_f32 v78, v78\n"
"v_mfma_f32_16x16x32_bf16 a[100:103], a[252:255], v[236:239], a[100:103]\n"
"s_cmp_gt_u32 s74, s36\n"
"v_add_f32 v28, v28, v77\n"
"v_cvt_pk_bf16_f32 v188, v76, v77\n"
"v_mfma_f32_16x16x32_bf16 a[104:107], a[252:255], v[244:247], a[104:107]\n"
"s_waitcnt lgkmcnt(5)\n"
"v_exp_f32 v79, v79\n"
"v_mfma_f32_16x16x32_bf16 a[108:111], a[252:255], v[252:255], a[108:111]\n"
"s_cbranch_scc1 101f\n"
"buffer_load_dwordx4 v4, s[16:19], s41 offen nt lds\n"
"buffer_load_dwordx4 v5, s[16:19], s41 offen offset:1024 nt lds\n"
"111:\n"
"v_add_f32 v28, v28, v78\n"
"v_mfma_f32_16x16x32_bf16 v[92:95], a[224:227], v[32:35], 0\n"
"s_waitcnt lgkmcnt(3)\n"
"v_exp_f32 v84, v84\n"
"v_mfma_f32_16x16x32_bf16 v[92:95], a[228:231], v[88:91], v[92:95]\n"
"s_add_u32 s73, s73, 2048\n"
"s_cbranch_vccnz 342f\n"
"352:\n"
"v_add_f32 v28, v28, v79\n"
"v_cvt_pk_bf16_f32 v189, v78, v79\n"
"v_mfma_f32_16x16x32_bf16 v[92:95], a[232:235], v[144:147], v[92:95]\n"
"s_waitcnt lgkmcnt(2)\n"
"v_exp_f32 v85, v85\n"
"v_mfma_f32_16x16x32_bf16 v[92:95], a[236:239], v[200:203], v[92:95]\n"
"s_add_u32 s72, s72, 2048\n"
"ds_read_b64_tr_b16 a[244:245], v10 offset:33280\n"
"v_add_f32 v30, v30, v84\n"
"v_mfma_f32_16x16x32_bf16 v[100:103], a[224:227], v[40:43], 0\n"
"v_exp_f32 v86, v86\n"
"v_mfma_f32_16x16x32_bf16 v[100:103], a[228:231], v[96:99], v[100:103]\n"
"s_or_b32 s72, s72, 24576\n"
"v_add_f32 v30, v30, v85\n"
"v_cvt_pk_bf16_f32 v196, v84, v85\n"
"v_mfma_f32_16x16x32_bf16 v[100:103], a[232:235], v[152:155], v[100:103]\n"
"v_exp_f32 v87, v87\n"
"v_mfma_f32_16x16x32_bf16 v[100:103], a[236:239], v[208:211], v[100:103]\n"
"s_xor_b32 s41, s40, s73\n"
"ds_read_b64_tr_b16 a[246:247], v10 offset:37376\n"
"v_add_f32 v30, v30, v86\n"
"v_mfma_f32_16x16x32_bf16 v[108:111], a[224:227], v[48:51], 0\n"
"v_exp_f32 v92, v92\n"
"v_mfma_f32_16x16x32_bf16 v[108:111], a[228:231], v[104:107], v[108:111]\n"
"s_add_u32 s39, s39, 130\n"
"v_add_f32 v30, v30, v87\n"
"v_cvt_pk_bf16_f32 v197, v86, v87\n"
"v_mfma_f32_16x16x32_bf16 v[108:111], a[232:235], v[160:163], v[108:111]\n"
"v_exp_f32 v93, v93\n"
"v_mfma_f32_16x16x32_bf16 v[108:111], a[236:239], v[216:219], v[108:111]\n"
"ds_read_b64_tr_b16 a[248:249], v9 offset:33536\n"
"v_add_f32 v18, v18, v92\n"
"v_mfma_f32_16x16x32_bf16 v[116:119], a[224:227], v[56:59], 0\n"
"v_exp_f32 v94, v94\n"
"v_mfma_f32_16x16x32_bf16 v[116:119], a[228:231], v[112:115], v[116:119]\n"
"v_add_f32 v18, v18, v93\n"
"v_cvt_pk_bf16_f32 v150, v92, v93\n"
"v_mfma_f32_16x16x32_bf16 v[116:119], a[232:235], v[168:171], v[116:119]\n"
"v_exp_f32 v95, v95\n"
"v_mfma_f32_16x16x32_bf16 v[116:119], a[236:239], v[224:227], v[116:119]\n"
"ds_read_b64_tr_b16 a[250:251], v9 offset:37632\n"
"v_add_f32 v18, v18, v94\n"
"v_mfma_f32_16x16x32_bf16 v[124:127], a[224:227], v[64:67], 0\n"
"v_exp_f32 v100, v100\n"
"v_mfma_f32_16x16x32_bf16 v[124:127], a[228:231], v[120:123], v[124:127]\n"
"s_memrealtime s[78:79]\n"
"v_add_f32 v18, v18, v95\n"
"v_cvt_pk_bf16_f32 v151, v94, v95\n"
"v_mfma_f32_16x16x32_bf16 v[124:127], a[232:235], v[176:179], v[124:127]\n"
"v_exp_f32 v101, v101\n"
"v_mfma_f32_16x16x32_bf16 v[124:127], a[236:239], v[232:235], v[124:127]\n"
"ds_read_b64_tr_b16 a[252:253], v10 offset:33536\n"
"v_add_f32 v20, v20, v100\n"
"v_mfma_f32_16x16x32_bf16 v[132:135], a[224:227], v[72:75], 0\n"
"v_exp_f32 v102, v102\n"
"v_mfma_f32_16x16x32_bf16 v[132:135], a[228:231], v[128:131], v[132:135]\n"
"s_and_b32 m0, s72, s37\n"
"v_add_f32 v20, v20, v101\n"
"v_cvt_pk_bf16_f32 v158, v100, v101\n"
"v_mfma_f32_16x16x32_bf16 v[132:135], a[232:235], v[184:187], v[132:135]\n"
"v_exp_f32 v103, v103\n"
"v_mfma_f32_16x16x32_bf16 v[132:135], a[236:239], v[240:243], v[132:135]\n"
"s_getreg_b32 s74, hwreg(HW_REG_IB_STS, 0, 4)\n"
"ds_read_b64_tr_b16 a[254:255], v10 offset:37632\n"
"v_add_f32 v20, v20, v102\n"
"v_xor_b32 v9, 65536, v9\n"
"v_mfma_f32_16x16x32_bf16 v[140:143], a[224:227], v[80:83], 0\n"
"v_exp_f32 v108, v108\n"
"v_mfma_f32_16x16x32_bf16 v[140:143], a[228:231], v[136:139], v[140:143]\n"
"s_cmp_gt_u32 s74, s36\n"
"v_add_f32 v20, v20, v103\n"
"v_cvt_pk_bf16_f32 v159, v102, v103\n"
"v_mfma_f32_16x16x32_bf16 v[140:143], a[232:235], v[192:195], v[140:143]\n"
"s_waitcnt lgkmcnt(6)\n"
"v_exp_f32 v109, v109\n"
"v_mfma_f32_16x16x32_bf16 v[140:143], a[236:239], v[248:251], v[140:143]\n"
"s_cbranch_scc1 102f\n"
"buffer_load_dwordx4 v4, s[16:19], s41 offen nt lds\n"
"buffer_load_dwordx4 v5, s[16:19], s41 offen offset:1024 nt lds\n"
"112:\n"
"v_add_f32 v22, v22, v108\n"
"v_xor_b32 v10, 65536, v10\n"
"v_mfma_f32_16x16x32_bf16 a[112:115], a[240:243], v[204:207], a[112:115]\n"
"v_exp_f32 v110, v110\n"
"v_mfma_f32_16x16x32_bf16 a[116:119], a[240:243], v[212:215], a[116:119]\n"
"s_add_u32 s73, s73, 2048\n"
"v_add_f32 v22, v22, v109\n"
"v_cvt_pk_bf16_f32 v166, v108, v109\n"
"v_mfma_f32_16x16x32_bf16 a[120:123], a[240:243], v[220:223], a[120:123]\n"
"s_waitcnt vmcnt(8) lgkmcnt(0)\n"
"s_barrier\n"
"s_cbranch_vccnz 343f\n"
"353:\n"
"v_exp_f32 v111, v111\n"
"v_mfma_f32_16x16x32_bf16 a[124:127], a[240:243], v[228:231], a[124:127]\n"
"s_add_u32 s72, s72, 2048\n"
"ds_read_b128 a[224:227], v8 offset:32768\n"
"v_add_f32 v22, v22, v110\n"
"v_mfma_f32_16x16x32_bf16 a[128:131], a[240:243], v[236:239], a[128:131]\n"
"v_exp_f32 v116, v116\n"
"v_mfma_f32_16x16x32_bf16 a[132:135], a[240:243], v[244:247], a[132:135]\n"
"s_or_b32 s72, s72, 24576\n"
"v_add_f32 v22, v22, v111\n"
"v_cvt_pk_bf16_f32 v167, v110, v111\n"
"v_mfma_f32_16x16x32_bf16 a[136:139], a[240:243], v[252:255], a[136:139]\n"
"v_exp_f32 v117, v117\n"
"v_mfma_f32_16x16x32_bf16 a[140:143], a[244:247], v[204:207], a[140:143]\n"
"s_xor_b32 s41, s40, s73\n"
"ds_read_b128 a[228:231], v8 offset:33024\n"
"v_add_f32 v24, v24, v116\n"
"v_mfma_f32_16x16x32_bf16 a[144:147], a[244:247], v[212:215], a[144:147]\n"
"v_exp_f32 v118, v118\n"
"v_mfma_f32_16x16x32_bf16 a[148:151], a[244:247], v[220:223], a[148:151]\n"
"s_cmp_gt_u32 s39, s78\n"
"s_cselect_b64 vcc, -1, 0\n"
"v_add_f32 v24, v24, v117\n"
"v_cvt_pk_bf16_f32 v174, v116, v117\n"
"v_mfma_f32_16x16x32_bf16 a[152:155], a[244:247], v[228:231], a[152:155]\n"
"v_exp_f32 v119, v119\n"
"v_mfma_f32_16x16x32_bf16 a[156:159], a[244:247], v[236:239], a[156:159]\n"
"ds_read_b128 a[232:235], v8 offset:33280\n"
"v_add_f32 v24, v24, v118\n"
"v_mfma_f32_16x16x32_bf16 a[160:163], a[244:247], v[244:247], a[160:163]\n"
"v_exp_f32 v124, v124\n"
"v_mfma_f32_16x16x32_bf16 a[164:167], a[244:247], v[252:255], a[164:167]\n"
"v_add_f32 v24, v24, v119\n"
"v_cvt_pk_bf16_f32 v175, v118, v119\n"
"v_mfma_f32_16x16x32_bf16 a[168:171], a[248:251], v[204:207], a[168:171]\n"
"v_exp_f32 v125, v125\n"
"v_mfma_f32_16x16x32_bf16 a[172:175], a[248:251], v[212:215], a[172:175]\n"
"s_cbranch_vccnz 344f\n"
"354:\n"
"ds_read_b128 a[236:239], v8 offset:33536\n"
"v_add_f32 v26, v26, v124\n"
"v_mfma_f32_16x16x32_bf16 a[176:179], a[248:251], v[220:223], a[176:179]\n"
"v_exp_f32 v126, v126\n"
"v_mfma_f32_16x16x32_bf16 a[180:183], a[248:251], v[228:231], a[180:183]\n"
"v_add_f32 v26, v26, v125\n"
"v_cvt_pk_bf16_f32 v182, v124, v125\n"
"v_mfma_f32_16x16x32_bf16 a[184:187], a[248:251], v[236:239], a[184:187]\n"
"v_exp_f32 v127, v127\n"
"v_mfma_f32_16x16x32_bf16 a[188:191], a[248:251], v[244:247], a[188:191]\n"
"ds_read_b64_tr_b16 a[240:241], v9 offset:0\n"
"v_add_f32 v26, v26, v126\n"
"v_mfma_f32_16x16x32_bf16 a[192:195], a[248:251], v[252:255], a[192:195]\n"
"v_exp_f32 v132, v132\n"
"v_mfma_f32_16x16x32_bf16 a[196:199], a[252:255], v[204:207], a[196:199]\n"
"s_and_b32 m0, s72, s37\n"
"v_add_f32 v26, v26, v127\n"
"v_cvt_pk_bf16_f32 v183, v126, v127\n"
"v_mfma_f32_16x16x32_bf16 a[200:203], a[252:255], v[212:215], a[200:203]\n"
"v_exp_f32 v133, v133\n"
"v_mfma_f32_16x16x32_bf16 a[204:207], a[252:255], v[220:223], a[204:207]\n"
"s_getreg_b32 s74, hwreg(HW_REG_IB_STS, 0, 4)\n"
"ds_read_b64_tr_b16 a[242:243], v9 offset:4096\n"
"v_add_f32 v28, v28, v132\n"
"v_mfma_f32_16x16x32_bf16 a[208:211], a[252:255], v[228:231], a[208:211]\n"
"v_exp_f32 v134, v134\n"
"v_mfma_f32_16x16x32_bf16 a[212:215], a[252:255], v[236:239], a[212:215]\n"
"s_cmp_gt_u32 s74, s36\n"
"v_add_f32 v28, v28, v133\n"
"v_cvt_pk_bf16_f32 v190, v132, v133\n"
"v_mfma_f32_16x16x32_bf16 a[216:219], a[252:255], v[244:247], a[216:219]\n"
"s_waitcnt lgkmcnt(5)\n"
"v_exp_f32 v135, v135\n"
"v_mfma_f32_16x16x32_bf16 a[220:223], a[252:255], v[252:255], a[220:223]\n"
"s_cbranch_scc1 103f\n"
"buffer_load_dwordx4 v4, s[16:19], s41 offen nt lds\n"
"buffer_load_dwordx4 v5, s[16:19], s41 offen offset:1024 nt lds\n"
"113:\n"
"v_add_f32 v28, v28, v134\n"
"v_mfma_f32_16x16x32_bf16 v[36:39], a[224:227], v[32:35], 0\n"
"s_waitcnt lgkmcnt(3)\n"
"v_exp_f32 v140, v140\n"
"v_mfma_f32_16x16x32_bf16 v[36:39], a[228:231], v[88:91], v[36:39]\n"
"s_add_u32 s73, s73, 2048\n"
"s_cbranch_vccnz 360f\n"
"370:\n"
"v_add_f32 v28, v28, v135\n"
"v_cvt_pk_bf16_f32 v191, v134, v135\n"
"v_mfma_f32_16x16x32_bf16 v[36:39], a[232:235], v[144:147], v[36:39]\n"
"s_waitcnt lgkmcnt(2)\n"
"v_exp_f32 v141, v141\n"
"v_mfma_f32_16x16x32_bf16 v[36:39], a[236:239], v[200:203], v[36:39]\n"
"s_add_u32 s72, s72, 2048\n"
"ds_read_b64_tr_b16 a[244:245], v10 offset:0\n"
"v_add_f32 v30, v30, v140\n"
"v_mfma_f32_16x16x32_bf16 v[44:47], a[224:227], v[40:43], 0\n"
"v_exp_f32 v142, v142\n"
"v_mfma_f32_16x16x32_bf16 v[44:47], a[228:231], v[96:99], v[44:47]\n"
"s_or_b32 s72, s72, 24576\n"
"v_add_f32 v30, v30, v141\n"
"v_cvt_pk_bf16_f32 v198, v140, v141\n"
"v_mfma_f32_16x16x32_bf16 v[44:47], a[232:235], v[152:155], v[44:47]\n"
"v_exp_f32 v143, v143\n"
"v_mfma_f32_16x16x32_bf16 v[44:47], a[236:239], v[208:211], v[44:47]\n"
"s_xor_b32 s41, s40, s73\n"
"ds_read_b64_tr_b16 a[246:247], v10 offset:4096\n"
"v_add_f32 v30, v30, v142\n"
"v_mfma_f32_16x16x32_bf16 v[52:55], a[224:227], v[48:51], 0\n"
"v_exp_f32 v36, v36\n"
"v_mfma_f32_16x16x32_bf16 v[52:55], a[228:231], v[104:107], v[52:55]\n"
"v_add_f32 v30, v30, v143\n"
"v_cvt_pk_bf16_f32 v199, v142, v143\n"
"v_mfma_f32_16x16x32_bf16 v[52:55], a[232:235], v[160:163], v[52:55]\n"
"v_exp_f32 v37, v37\n"
"v_mfma_f32_16x16x32_bf16 v[52:55], a[236:239], v[216:219], v[52:55]\n"
"ds_read_b64_tr_b16 a[248:249], v9 offset:256\n"
"v_add_f32 v18, v18, v36\n"
"v_mfma_f32_16x16x32_bf16 v[60:63], a[224:227], v[56:59], 0\n"
"v_exp_f32 v38, v38\n"
"v_mfma_f32_16x16x32_bf16 v[60:63], a[228:231], v[112:115], v[60:63]\n"
"v_add_f32 v18, v18, v37\n"
"v_cvt_pk_bf16_f32 v204, v36, v37\n"
"v_mfma_f32_16x16x32_bf16 v[60:63], a[232:235], v[168:171], v[60:63]\n"
"v_exp_f32 v39, v39\n"
"v_mfma_f32_16x16x32_bf16 v[60:63], a[236:239], v[224:227], v[60:63]\n"
"ds_read_b64_tr_b16 a[250:251], v9 offset:4352\n"
"v_add_f32 v18, v18, v38\n"
"v_mfma_f32_16x16x32_bf16 v[68:71], a[224:227], v[64:67], 0\n"
"v_exp_f32 v44, v44\n"
"v_mfma_f32_16x16x32_bf16 v[68:71], a[228:231], v[120:123], v[68:71]\n"
"v_add_f32 v18, v18, v39\n"
"v_cvt_pk_bf16_f32 v205, v38, v39\n"
"v_mfma_f32_16x16x32_bf16 v[68:71], a[232:235], v[176:179], v[68:71]\n"
"v_exp_f32 v45, v45\n"
"v_mfma_f32_16x16x32_bf16 v[68:71], a[236:239], v[232:235], v[68:71]\n"
"ds_read_b64_tr_b16 a[252:253], v10 offset:256\n"
"v_add_f32 v20, v20, v44\n"
"v_mfma_f32_16x16x32_bf16 v[76:79], a[224:227], v[72:75], 0\n"
"v_exp_f32 v46, v46\n"
"v_mfma_f32_16x16x32_bf16 v[76:79], a[228:231], v[128:131], v[76:79]\n"
"s_and_b32 m0, s72, s37\n"
"v_add_f32 v20, v20, v45\n"
"v_cvt_pk_bf16_f32 v212, v44, v45\n"
"v_mfma_f32_16x16x32_bf16 v[76:79], a[232:235], v[184:187], v[76:79]\n"
"v_exp_f32 v47, v47\n"
"v_mfma_f32_16x16x32_bf16 v[76:79], a[236:239], v[240:243], v[76:79]\n"
"s_getreg_b32 s74, hwreg(HW_REG_IB_STS, 0, 4)\n"
"ds_read_b64_tr_b16 a[254:255], v10 offset:4352\n"
"v_add_f32 v20, v20, v46\n"
"v_mfma_f32_16x16x32_bf16 v[84:87], a[224:227], v[80:83], 0\n"
"v_exp_f32 v52, v52\n"
"v_mfma_f32_16x16x32_bf16 v[84:87], a[228:231], v[136:139], v[84:87]\n"
"s_cmp_gt_u32 s74, s36\n"
"v_add_f32 v20, v20, v47\n"
"v_cvt_pk_bf16_f32 v213, v46, v47\n"
"v_mfma_f32_16x16x32_bf16 v[84:87], a[232:235], v[192:195], v[84:87]\n"
"s_waitcnt lgkmcnt(6)\n"
"v_exp_f32 v53, v53\n"
"v_mfma_f32_16x16x32_bf16 v[84:87], a[236:239], v[248:251], v[84:87]\n"
"s_cbranch_scc1 104f\n"
"buffer_load_dwordx4 v4, s[16:19], s41 offen nt lds\n"
"buffer_load_dwordx4 v5, s[16:19], s41 offen offset:1024 nt lds\n"
"114:\n"
"v_add_f32 v22, v22, v52\n"
"v_mfma_f32_16x16x32_bf16 a[0:3], a[240:243], v[148:151], a[0:3]\n"
"v_exp_f32 v54, v54\n"
"v_mfma_f32_16x16x32_bf16 a[4:7], a[240:243], v[156:159], a[4:7]\n"
"s_add_u32 s73, s73, 2048\n"
"s_cbranch_vccnz 361f\n"
"371:\n"
"v_add_f32 v22, v22, v53\n"
"v_cvt_pk_bf16_f32 v220, v52, v53\n"
"v_mfma_f32_16x16x32_bf16 a[8:11], a[240:243], v[164:167], a[8:11]\n"
"v_exp_f32 v55, v55\n"
"v_mfma_f32_16x16x32_bf16 a[12:15], a[240:243], v[172:175], a[12:15]\n"
"s_add_u32 s72, s72, 2048\n"
"ds_read_b128 a[224:227], v8 offset:36864\n"
"v_add_f32 v22, v22, v54\n"
"v_mfma_f32_16x16x32_bf16 a[16:19], a[240:243], v[180:183], a[16:19]\n"
"v_exp_f32 v60, v60\n"
"v_mfma_f32_16x16x32_bf16 a[20:23], a[240:243], v[188:191], a[20:23]\n"
"s_or_b32 s72, s72, 24576\n"
"v_add_f32 v22, v22, v55\n"
"v_cvt_pk_bf16_f32 v221, v54, v55\n"
"v_mfma_f32_16x16x32_bf16 a[24:27], a[240:243], v[196:199], a[24:27]\n"
"s_waitcnt lgkmcnt(5)\n"
"v_exp_f32 v61, v61\n"
"v_mfma_f32_16x16x32_bf16 a[28:31], a[244:247], v[148:151], a[28:31]\n"
"s_xor_b32 s41, s40, s73\n"
"ds_read_b128 a[228:231], v8 offset:37120\n"
"v_add_f32 v24, v24, v60\n"
"v_mfma_f32_16x16x32_bf16 a[32:35], a[244:247], v[156:159], a[32:35]\n"
"v_exp_f32 v62, v62\n"
"v_mfma_f32_16x16x32_bf16 a[36:39], a[244:247], v[164:167], a[36:39]\n"
"v_add_f32 v24, v24, v61\n"
"v_cvt_pk_bf16_f32 v228, v60, v61\n"
"v_mfma_f32_16x16x32_bf16 a[40:43], a[244:247], v[172:175], a[40:43]\n"
"v_exp_f32 v63, v63\n"
"v_mfma_f32_16x16x32_bf16 a[44:47], a[244:247], v[180:183], a[44:47]\n"
"ds_read_b128 a[232:235], v8 offset:37376\n"
"v_add_f32 v24, v24, v62\n"
"v_mfma_f32_16x16x32_bf16 a[48:51], a[244:247], v[188:191], a[48:51]\n"
"s_waitcnt lgkmcnt(5)\n"
"v_exp_f32 v68, v68\n"
"v_mfma_f32_16x16x32_bf16 a[52:55], a[244:247], v[196:199], a[52:55]\n"
"v_add_f32 v24, v24, v63\n"
"v_cvt_pk_bf16_f32 v229, v62, v63\n"
"v_mfma_f32_16x16x32_bf16 a[56:59], a[248:251], v[148:151], a[56:59]\n"
"v_exp_f32 v69, v69\n"
"v_mfma_f32_16x16x32_bf16 a[60:63], a[248:251], v[156:159], a[60:63]\n"
"ds_read_b128 a[236:239], v8 offset:37632\n"
"v_add_f32 v26, v26, v68\n"
"v_mfma_f32_16x16x32_bf16 a[64:67], a[248:251], v[164:167], a[64:67]\n"
"v_exp_f32 v70, v70\n"
"v_mfma_f32_16x16x32_bf16 a[68:71], a[248:251], v[172:175], a[68:71]\n"
"v_add_f32 v26, v26, v69\n"
"v_cvt_pk_bf16_f32 v236, v68, v69\n"
"v_mfma_f32_16x16x32_bf16 a[72:75], a[248:251], v[180:183], a[72:75]\n"
"v_exp_f32 v71, v71\n"
"v_mfma_f32_16x16x32_bf16 a[76:79], a[248:251], v[188:191], a[76:79]\n"
"ds_read_b64_tr_b16 a[240:241], v9 offset:512\n"
"v_add_f32 v26, v26, v70\n"
"v_mfma_f32_16x16x32_bf16 a[80:83], a[248:251], v[196:199], a[80:83]\n"
"s_waitcnt lgkmcnt(5)\n"
"v_exp_f32 v76, v76\n"
"v_mfma_f32_16x16x32_bf16 a[84:87], a[252:255], v[148:151], a[84:87]\n"
"s_and_b32 m0, s72, s37\n"
"v_add_f32 v26, v26, v71\n"
"v_cvt_pk_bf16_f32 v237, v70, v71\n"
"v_mfma_f32_16x16x32_bf16 a[88:91], a[252:255], v[156:159], a[88:91]\n"
"v_exp_f32 v77, v77\n"
"v_mfma_f32_16x16x32_bf16 a[92:95], a[252:255], v[164:167], a[92:95]\n"
"s_getreg_b32 s74, hwreg(HW_REG_IB_STS, 0, 4)\n"
"ds_read_b64_tr_b16 a[242:243], v9 offset:4608\n"
"v_add_f32 v28, v28, v76\n"
"v_mfma_f32_16x16x32_bf16 a[96:99], a[252:255], v[172:175], a[96:99]\n"
"v_exp_f32 v78, v78\n"
"v_mfma_f32_16x16x32_bf16 a[100:103], a[252:255], v[180:183], a[100:103]\n"
"s_cmp_gt_u32 s74, s36\n"
"v_add_f32 v28, v28, v77\n"
"v_cvt_pk_bf16_f32 v244, v76, v77\n"
"v_mfma_f32_16x16x32_bf16 a[104:107], a[252:255], v[188:191], a[104:107]\n"
"s_waitcnt lgkmcnt(5)\n"
"v_exp_f32 v79, v79\n"
"v_mfma_f32_16x16x32_bf16 a[108:111], a[252:255], v[196:199], a[108:111]\n"
"s_cbranch_scc1 105f\n"
"buffer_load_dwordx4 v4, s[16:19], s41 offen nt lds\n"
"buffer_load_dwordx4 v5, s[16:19], s41 offen offset:1024 nt lds\n"
"115:\n"
"v_add_f32 v28, v28, v78\n"
"v_mfma_f32_16x16x32_bf16 v[92:95], a[224:227], v[32:35], 0\n"
"s_waitcnt lgkmcnt(3)\n"
"v_exp_f32 v84, v84\n"
"v_mfma_f32_16x16x32_bf16 v[92:95], a[228:231], v[88:91], v[92:95]\n"
"s_add_u32 s73, s73, 2048\n"
"s_cbranch_vccnz 362f\n"
"372:\n"
"v_add_f32 v28, v28, v79\n"
"v_cvt_pk_bf16_f32 v245, v78, v79\n"
"v_mfma_f32_16x16x32_bf16 v[92:95], a[232:235], v[144:147], v[92:95]\n"
"s_waitcnt lgkmcnt(2)\n"
"v_exp_f32 v85, v85\n"
"v_mfma_f32_16x16x32_bf16 v[92:95], a[236:239], v[200:203], v[92:95]\n"
"s_add_u32 s72, s72, 2048\n"
"ds_read_b64_tr_b16 a[244:245], v10 offset:512\n"
"v_add_f32 v30, v30, v84\n"
"v_mfma_f32_16x16x32_bf16 v[100:103], a[224:227], v[40:43], 0\n"
"v_exp_f32 v86, v86\n"
"v_mfma_f32_16x16x32_bf16 v[100:103], a[228:231], v[96:99], v[100:103]\n"
"s_or_b32 s72, s72, 24576\n"
"v_add_f32 v30, v30, v85\n"
"v_cvt_pk_bf16_f32 v252, v84, v85\n"
"v_mfma_f32_16x16x32_bf16 v[100:103], a[232:235], v[152:155], v[100:103]\n"
"v_exp_f32 v87, v87\n"
"v_mfma_f32_16x16x32_bf16 v[100:103], a[236:239], v[208:211], v[100:103]\n"
"s_xor_b32 s41, s40, s73\n"
"ds_read_b64_tr_b16 a[246:247], v10 offset:4608\n"
"v_add_f32 v30, v30, v86\n"
"v_mfma_f32_16x16x32_bf16 v[108:111], a[224:227], v[48:51], 0\n"
"v_exp_f32 v92, v92\n"
"v_mfma_f32_16x16x32_bf16 v[108:111], a[228:231], v[104:107], v[108:111]\n"
"s_add_u32 s39, s39, 130\n"
"v_add_f32 v30, v30, v87\n"
"v_cvt_pk_bf16_f32 v253, v86, v87\n"
"v_mfma_f32_16x16x32_bf16 v[108:111], a[232:235], v[160:163], v[108:111]\n"
"v_exp_f32 v93, v93\n"
"v_mfma_f32_16x16x32_bf16 v[108:111], a[236:239], v[216:219], v[108:111]\n"
"ds_read_b64_tr_b16 a[248:249], v9 offset:768\n"
"v_add_f32 v18, v18, v92\n"
"v_mfma_f32_16x16x32_bf16 v[116:119], a[224:227], v[56:59], 0\n"
"v_exp_f32 v94, v94\n"
"v_mfma_f32_16x16x32_bf16 v[116:119], a[228:231], v[112:115], v[116:119]\n"
"v_add_f32 v18, v18, v93\n"
"v_cvt_pk_bf16_f32 v206, v92, v93\n"
"v_mfma_f32_16x16x32_bf16 v[116:119], a[232:235], v[168:171], v[116:119]\n"
"v_exp_f32 v95, v95\n"
"v_mfma_f32_16x16x32_bf16 v[116:119], a[236:239], v[224:227], v[116:119]\n"
"ds_read_b64_tr_b16 a[250:251], v9 offset:4864\n"
"v_add_f32 v18, v18, v94\n"
"v_mfma_f32_16x16x32_bf16 v[124:127], a[224:227], v[64:67], 0\n"
"v_exp_f32 v100, v100\n"
"v_mfma_f32_16x16x32_bf16 v[124:127], a[228:231], v[120:123], v[124:127]\n"
"s_memrealtime s[78:79]\n"
"v_add_f32 v18, v18, v95\n"
"v_cvt_pk_bf16_f32 v207, v94, v95\n"
"v_mfma_f32_16x16x32_bf16 v[124:127], a[232:235], v[176:179], v[124:127]\n"
"v_exp_f32 v101, v101\n"
"v_mfma_f32_16x16x32_bf16 v[124:127], a[236:239], v[232:235], v[124:127]\n"
"ds_read_b64_tr_b16 a[252:253], v10 offset:768\n"
"v_add_f32 v20, v20, v100\n"
"v_mfma_f32_16x16x32_bf16 v[132:135], a[224:227], v[72:75], 0\n"
"v_exp_f32 v102, v102\n"
"v_mfma_f32_16x16x32_bf16 v[132:135], a[228:231], v[128:131], v[132:135]\n"
"s_and_b32 m0, s72, s37\n"
"v_add_f32 v20, v20, v101\n"
"v_cvt_pk_bf16_f32 v214, v100, v101\n"
"v_mfma_f32_16x16x32_bf16 v[132:135], a[232:235], v[184:187], v[132:135]\n"
"v_exp_f32 v103, v103\n"
"v_mfma_f32_16x16x32_bf16 v[132:135], a[236:239], v[240:243], v[132:135]\n"
"s_getreg_b32 s74, hwreg(HW_REG_IB_STS, 0, 4)\n"
"ds_read_b64_tr_b16 a[254:255], v10 offset:4864\n"
"v_add_f32 v20, v20, v102\n"
"v_mfma_f32_16x16x32_bf16 v[140:143], a[224:227], v[80:83], 0\n"
"v_exp_f32 v108, v108\n"
"v_mfma_f32_16x16x32_bf16 v[140:143], a[228:231], v[136:139], v[140:143]\n"
"s_cmp_gt_u32 s74, s36\n"
"v_add_f32 v20, v20, v103\n"
"v_cvt_pk_bf16_f32 v215, v102, v103\n"
"v_mfma_f32_16x16x32_bf16 v[140:143], a[232:235], v[192:195], v[140:143]\n"
"s_waitcnt lgkmcnt(6)\n"
"v_exp_f32 v109, v109\n"
"v_mfma_f32_16x16x32_bf16 v[140:143], a[236:239], v[248:251], v[140:143]\n"
"s_cbranch_scc1 106f\n"
"buffer_load_dwordx4 v4, s[16:19], s41 offen nt lds\n"
"buffer_load_dwordx4 v5, s[16:19], s41 offen offset:1024 nt lds\n"
"116:\n"
"v_add_f32 v22, v22, v108\n"
"v_mfma_f32_16x16x32_bf16 a[112:115], a[240:243], v[148:151], a[112:115]\n"
"v_exp_f32 v110, v110\n"
"v_mfma_f32_16x16x32_bf16 a[116:119], a[240:243], v[156:159], a[116:119]\n"
"s_add_u32 s73, s73, 2048\n"
"v_add_f32 v22, v22, v109\n"
"v_cvt_pk_bf16_f32 v222, v108, v109\n"
"v_xor_b32 v8, 65536, v8\n"
"v_mfma_f32_16x16x32_bf16 a[120:123], a[240:243], v[164:167], a[120:123]\n"
"s_waitcnt vmcnt(8) lgkmcnt(0)\n"
"s_barrier\n"
"s_cbranch_vccnz 363f\n"
"373:\n"
"v_exp_f32 v111, v111\n"
"v_mfma_f32_16x16x32_bf16 a[124:127], a[240:243], v[172:175], a[124:127]\n"
"s_add_u32 s72, s72, 2048\n"
"ds_read_b128 a[224:227], v8 offset:0\n"
"v_add_f32 v22, v22, v110\n"
"v_mfma_f32_16x16x32_bf16 a[128:131], a[240:243], v[180:183], a[128:131]\n"
"v_exp_f32 v116, v116\n"
"v_mfma_f32_16x16x32_bf16 a[132:135], a[240:243], v[188:191], a[132:135]\n"
"s_or_b32 s72, s72, 24576\n"
"v_add_f32 v22, v22, v111\n"
"v_cvt_pk_bf16_f32 v223, v110, v111\n"
"v_mfma_f32_16x16x32_bf16 a[136:139], a[240:243], v[196:199], a[136:139]\n"
"v_exp_f32 v117, v117\n"
"v_mfma_f32_16x16x32_bf16 a[140:143], a[244:247], v[148:151], a[140:143]\n"
"s_xor_b32 s41, s40, s73\n"
"ds_read_b128 a[228:231], v8 offset:256\n"
"v_add_f32 v24, v24, v116\n"
"v_mfma_f32_16x16x32_bf16 a[144:147], a[244:247], v[156:159], a[144:147]\n"
"v_exp_f32 v118, v118\n"
"v_mfma_f32_16x16x32_bf16 a[148:151], a[244:247], v[164:167], a[148:151]\n"
"s_cmp_gt_u32 s39, s78\n"
"s_cselect_b64 vcc, -1, 0\n"
"v_add_f32 v24, v24, v117\n"
"v_cvt_pk_bf16_f32 v230, v116, v117\n"
"v_mfma_f32_16x16x32_bf16 a[152:155], a[244:247], v[172:175], a[152:155]\n"
"v_exp_f32 v119, v119\n"
"v_mfma_f32_16x16x32_bf16 a[156:159], a[244:247], v[180:183], a[156:159]\n"
"ds_read_b128 a[232:235], v8 offset:512\n"
"v_add_f32 v24, v24, v118\n"
"v_mfma_f32_16x16x32_bf16 a[160:163], a[244:247], v[188:191], a[160:163]\n"
"v_exp_f32 v124, v124\n"
"v_mfma_f32_16x16x32_bf16 a[164:167], a[244:247], v[196:199], a[164:167]\n"
"v_add_f32 v24, v24, v119\n"
"v_cvt_pk_bf16_f32 v231, v118, v119\n"
"v_mfma_f32_16x16x32_bf16 a[168:171], a[248:251], v[148:151], a[168:171]\n"
"v_exp_f32 v125, v125\n"
"v_mfma_f32_16x16x32_bf16 a[172:175], a[248:251], v[156:159], a[172:175]\n"
"s_cbranch_vccnz 364f\n"
"374:\n"
"ds_read_b128 a[236:239], v8 offset:768\n"
"v_add_f32 v26, v26, v124\n"
"v_mfma_f32_16x16x32_bf16 a[176:179], a[248:251], v[164:167], a[176:179]\n"
"v_exp_f32 v126, v126\n"
"v_mfma_f32_16x16x32_bf16 a[180:183], a[248:251], v[172:175], a[180:183]\n"
"v_add_f32 v26, v26, v125\n"
"v_cvt_pk_bf16_f32 v238, v124, v125\n"
"v_mfma_f32_16x16x32_bf16 a[184:187], a[248:251], v[180:183], a[184:187]\n"
"v_exp_f32 v127, v127\n"
"v_mfma_f32_16x16x32_bf16 a[188:191], a[248:251], v[188:191], a[188:191]\n"
"ds_read_b64_tr_b16 a[240:241], v9 offset:32768\n"
"v_add_f32 v26, v26, v126\n"
"v_mfma_f32_16x16x32_bf16 a[192:195], a[248:251], v[196:199], a[192:195]\n"
"v_exp_f32 v132, v132\n"
"v_mfma_f32_16x16x32_bf16 a[196:199], a[252:255], v[148:151], a[196:199]\n"
"s_and_b32 m0, s72, s37\n"
"v_add_f32 v26, v26, v127\n"
"v_cvt_pk_bf16_f32 v239, v126, v127\n"
"v_mfma_f32_16x16x32_bf16 a[200:203], a[252:255], v[156:159], a[200:203]\n"
"v_exp_f32 v133, v133\n"
"v_mfma_f32_16x16x32_bf16 a[204:207], a[252:255], v[164:167], a[204:207]\n"
"s_getreg_b32 s74, hwreg(HW_REG_IB_STS, 0, 4)\n"
"ds_read_b64_tr_b16 a[242:243], v9 offset:36864\n"
"v_add_f32 v28, v28, v132\n"
"v_mfma_f32_16x16x32_bf16 a[208:211], a[252:255], v[172:175], a[208:211]\n"
"v_exp_f32 v134, v134\n"
"v_mfma_f32_16x16x32_bf16 a[212:215], a[252:255], v[180:183], a[212:215]\n"
"s_cmp_gt_u32 s74, s36\n"
"v_add_f32 v28, v28, v133\n"
"v_cvt_pk_bf16_f32 v246, v132, v133\n"
"v_mfma_f32_16x16x32_bf16 a[216:219], a[252:255], v[188:191], a[216:219]\n"
"v_exp_f32 v135, v135\n"
"v_mfma_f32_16x16x32_bf16 a[220:223], a[252:255], v[196:199], a[220:223]\n"
"s_cbranch_scc1 107f\n"
"buffer_load_dwordx4 v4, s[16:19], s41 offen nt lds\n"
"buffer_load_dwordx4 v5, s[16:19], s41 offen offset:1024 nt lds\n"
"117:\n"
"v_add_f32 v28, v28, v134\n"
"s_sub_u32 s76, s76, 1\n"
"s_waitcnt lgkmcnt(5)\n"
"s_cbranch_scc0 3b\n"
"s_branch 6f\n"
"100:\n"
"s_sub_u32 s73, s73, 2048\n"
"s_sub_u32 s72, s72, 2048\n"
"s_add_u32 s66, s66, 1\n"
"s_cmp_eq_u32 s66, 4\n"
"s_cselect_b32 s36, 63, s36\n"
"s_branch 110b\n"
"101:\n"
"s_sub_u32 s73, s73, 2048\n"
"s_sub_u32 s72, s72, 2048\n"
"s_add_u32 s66, s66, 1\n"
"s_cmp_eq_u32 s66, 4\n"
"s_cselect_b32 s36, 63, s36\n"
"s_branch 111b\n"
"102:\n"
"s_sub_u32 s73, s73, 2048\n"
"s_sub_u32 s72, s72, 2048\n"
"s_add_u32 s66, s66, 1\n"
"s_cmp_eq_u32 s66, 4\n"
"s_cselect_b32 s36, 63, s36\n"
"s_branch 112b\n"
"103:\n"
"s_sub_u32 s73, s73, 2048\n"
"s_sub_u32 s72, s72, 2048\n"
"s_add_u32 s66, s66, 1\n"
"s_cmp_eq_u32 s66, 4\n"
"s_cselect_b32 s36, 63, s36\n"
"s_branch 113b\n"
"104:\n"
"s_sub_u32 s73, s73, 2048\n"
"s_sub_u32 s72, s72, 2048\n"
"s_add_u32 s66, s66, 1\n"
"s_cmp_eq_u32 s66, 4\n"
"s_cselect_b32 s36, 63, s36\n"
"s_branch 114b\n"
"105:\n"
"s_sub_u32 s73, s73, 2048\n"
"s_sub_u32 s72, s72, 2048\n"
"s_add_u32 s66, s66, 1\n"
"s_cmp_eq_u32 s66, 4\n"
"s_cselect_b32 s36, 63, s36\n"
"s_branch 115b\n"
"106:\n"
"s_sub_u32 s73, s73, 2048\n"
"s_sub_u32 s72, s72, 2048\n"
"s_add_u32 s66, s66, 1\n"
"s_cmp_eq_u32 s66, 4\n"
"s_cselect_b32 s36, 63, s36\n"
"s_branch 116b\n"
"107:\n"
"s_sub_u32 s73, s73, 2048\n"
"s_sub_u32 s72, s72, 2048\n"
"s_add_u32 s66, s66, 1\n"
"s_cmp_eq_u32 s66, 4\n"
"s_cselect_b32 s36, 63, s36\n"
"s_branch 117b\n"
"120:\n"
"s_and_b32 m0, s72, s37\n"
"s_nop 0\n"
"buffer_load_dwordx4 v4, s[16:19], s41 offen nt lds\n"
"buffer_load_dwordx4 v5, s[16:19], s41 offen offset:1024 nt lds\n"
"s_add_u32 s73, s73, 2048\n"
"s_add_u32 s72, s72, 2048\n"
"s_or_b32 s72, s72, 24576\n"
"s_xor_b32 s41, s40, s73\n"
"s_branch 130f\n"
"121:\n"
"s_and_b32 m0, s72, s37\n"
"s_nop 0\n"
"buffer_load_dwordx4 v4, s[16:19], s41 offen nt lds\n"
"buffer_load_dwordx4 v5, s[16:19], s41 offen offset:1024 nt lds\n"
"s_add_u32 s73, s73, 2048\n"
"s_add_u32 s72, s72, 2048\n"
"s_or_b32 s72, s72, 24576\n"
"s_xor_b32 s41, s40, s73\n"
"s_branch 131f\n"
"122:\n"
"s_and_b32 m0, s72, s37\n"
"s_nop 0\n"
"buffer_load_dwordx4 v4, s[16:19], s41 offen nt lds\n"
"buffer_load_dwordx4 v5, s[16:19], s41 offen offset:1024 nt lds\n"
"s_add_u32 s73, s73, 2048\n"
"s_add_u32 s72, s72, 2048\n"
"s_or_b32 s72, s72, 24576\n"
"s_xor_b32 s41, s40, s73\n"
"s_branch 132f\n"
"123:\n"
"s_and_b32 m0, s72, s37\n"
"s_nop 0\n"
"buffer_load_dwordx4 v4, s[16:19], s41 offen nt lds\n"
"buffer_load_dwordx4 v5, s[16:19], s41 offen offset:1024 nt lds\n"
"s_add_u32 s73, s73, 2048\n"
"s_add_u32 s72, s72, 2048\n"
"s_or_b32 s72, s72, 24576\n"
"s_xor_b32 s41, s40, s73\n"
"s_branch 133f\n"
"323:\n"
"s_sleep 1\n"
"s_branch 333b\n"
"324:\n"
"s_sleep 1\n"
"s_branch 334b\n"
"340:\n"
"s_sleep 1\n"
"s_branch 350b\n"
"341:\n"
"s_sleep 1\n"
"s_branch 351b\n"
"342:\n"
"s_sleep 1\n"
"s_branch 352b\n"
"343:\n"
"s_sleep 1\n"
"s_branch 353b\n"
"344:\n"
"s_sleep 1\n"
"s_branch 354b\n"
"360:\n"
"s_sleep 1\n"
"s_branch 370b\n"
"361:\n"
"s_sleep 1\n"
"s_branch 371b\n"
"362:\n"
"s_sleep 1\n"
"s_branch 372b\n"
"363:\n"
"s_sleep 1\n"
"s_branch 373b\n"
"364:\n"
"s_sleep 1\n"
"s_branch 374b\n"
"540:\n"
"s_sleep 1\n"
"s_branch 550f\n"
"541:\n"
"s_sleep 1\n"
"s_branch 551f\n"
"542:\n"
"s_sleep 1\n"
"s_branch 552f\n"
"543:\n"
"s_sleep 1\n"
"s_branch 553f\n"
"544:\n"
"s_sleep 1\n"
"s_branch 554f\n"
"560:\n"
"s_sleep 1\n"
"s_branch 570f\n"
"561:\n"
"s_sleep 1\n"
"s_branch 571f\n"
"562:\n"
"s_sleep 1\n"
"s_branch 572f\n"
"563:\n"
"s_sleep 1\n"
"s_branch 573f\n"
"564:\n"
"s_sleep 1\n"
"s_branch 574f\n"
"580:\n"
"s_sleep 1\n"
"s_branch 590f\n"
"581:\n"
"s_sleep 1\n"
"s_branch 591f\n"
"6:\n"
"v_mfma_f32_16x16x32_bf16 v[36:39], a[224:227], v[32:35], 0\n"
"s_waitcnt lgkmcnt(3)\n"
"v_exp_f32 v140, v140\n"
"v_mfma_f32_16x16x32_bf16 v[36:39], a[228:231], v[88:91], v[36:39]\n"
"s_add_u32 s73, s73, 2048\n"
"s_cbranch_vccnz 540b\n"
"550:\n"
"v_add_f32 v28, v28, v135\n"
"v_cvt_pk_bf16_f32 v247, v134, v135\n"
"v_mfma_f32_16x16x32_bf16 v[36:39], a[232:235], v[144:147], v[36:39]\n"
"s_waitcnt lgkmcnt(2)\n"
"v_exp_f32 v141, v141\n"
"v_mfma_f32_16x16x32_bf16 v[36:39], a[236:239], v[200:203], v[36:39]\n"
"s_add_u32 s72, s72, 2048\n"
"ds_read_b64_tr_b16 a[244:245], v10 offset:32768\n"
"v_add_f32 v30, v30, v140\n"
"v_mfma_f32_16x16x32_bf16 v[44:47], a[224:227], v[40:43], 0\n"
"v_exp_f32 v142, v142\n"
"v_mfma_f32_16x16x32_bf16 v[44:47], a[228:231], v[96:99], v[44:47]\n"
"s_or_b32 s72, s72, 24576\n"
"v_add_f32 v30, v30, v141\n"
"v_cvt_pk_bf16_f32 v254, v140, v141\n"
"v_mfma_f32_16x16x32_bf16 v[44:47], a[232:235], v[152:155], v[44:47]\n"
"v_exp_f32 v143, v143\n"
"v_mfma_f32_16x16x32_bf16 v[44:47], a[236:239], v[208:211], v[44:47]\n"
"s_xor_b32 s41, s40, s73\n"
"ds_read_b64_tr_b16 a[246:247], v10 offset:36864\n"
"v_add_f32 v30, v30, v142\n"
"v_mfma_f32_16x16x32_bf16 v[52:55], a[224:227], v[48:51], 0\n"
"v_exp_f32 v36, v36\n"
"v_mfma_f32_16x16x32_bf16 v[52:55], a[228:231], v[104:107], v[52:55]\n"
"s_or_b32 s67, s60, 2096128\n"
"v_add_f32 v30, v30, v143\n"
"v_cvt_pk_bf16_f32 v255, v142, v143\n"
"v_mfma_f32_16x16x32_bf16 v[52:55], a[232:235], v[160:163], v[52:55]\n"
"v_exp_f32 v37, v37\n"
"v_mfma_f32_16x16x32_bf16 v[52:55], a[236:239], v[216:219], v[52:55]\n"
"s_mov_b32 s42, s68\n"
"ds_read_b64_tr_b16 a[248:249], v9 offset:33024\n"
"v_add_f32 v18, v18, v36\n"
"v_mfma_f32_16x16x32_bf16 v[60:63], a[224:227], v[56:59], 0\n"
"v_exp_f32 v38, v38\n"
"v_mfma_f32_16x16x32_bf16 v[60:63], a[228:231], v[112:115], v[60:63]\n"
"s_xor_b32 s43, s69, 1\n"
"buffer_load_dwordx2 v[16:17], v1, s[20:23], s67 offen sc1\n"
"v_add_f32 v18, v18, v37\n"
"v_cvt_pk_bf16_f32 v148, v36, v37\n"
"v_mfma_f32_16x16x32_bf16 v[60:63], a[232:235], v[168:171], v[60:63]\n"
"v_exp_f32 v39, v39\n"
"v_mfma_f32_16x16x32_bf16 v[60:63], a[236:239], v[224:227], v[60:63]\n"
"ds_read_b64_tr_b16 a[250:251], v9 offset:37120\n"
"v_add_f32 v18, v18, v38\n"
"v_mfma_f32_16x16x32_bf16 v[68:71], a[224:227], v[64:67], 0\n"
"v_exp_f32 v44, v44\n"
"v_mfma_f32_16x16x32_bf16 v[68:71], a[228:231], v[120:123], v[68:71]\n"
"v_add_f32 v18, v18, v39\n"
"v_cvt_pk_bf16_f32 v149, v38, v39\n"
"v_mfma_f32_16x16x32_bf16 v[68:71], a[232:235], v[176:179], v[68:71]\n"
"v_exp_f32 v45, v45\n"
"v_mfma_f32_16x16x32_bf16 v[68:71], a[236:239], v[232:235], v[68:71]\n"
"ds_read_b64_tr_b16 a[252:253], v10 offset:33024\n"
"v_add_f32 v20, v20, v44\n"
"v_mfma_f32_16x16x32_bf16 v[76:79], a[224:227], v[72:75], 0\n"
"v_exp_f32 v46, v46\n"
"v_mfma_f32_16x16x32_bf16 v[76:79], a[228:231], v[128:131], v[76:79]\n"
"s_and_b32 m0, s72, s37\n"
"v_add_f32 v20, v20, v45\n"
"v_cvt_pk_bf16_f32 v156, v44, v45\n"
"v_mfma_f32_16x16x32_bf16 v[76:79], a[232:235], v[184:187], v[76:79]\n"
"v_exp_f32 v47, v47\n"
"v_mfma_f32_16x16x32_bf16 v[76:79], a[236:239], v[240:243], v[76:79]\n"
"ds_read_b64_tr_b16 a[254:255], v10 offset:37120\n"
"v_add_f32 v20, v20, v46\n"
"v_mfma_f32_16x16x32_bf16 v[84:87], a[224:227], v[80:83], 0\n"
"v_exp_f32 v52, v52\n"
"v_mfma_f32_16x16x32_bf16 v[84:87], a[228:231], v[136:139], v[84:87]\n"
"v_add_f32 v20, v20, v47\n"
"v_cvt_pk_bf16_f32 v157, v46, v47\n"
"v_mfma_f32_16x16x32_bf16 v[84:87], a[232:235], v[192:195], v[84:87]\n"
"s_waitcnt lgkmcnt(6)\n"
"v_exp_f32 v53, v53\n"
"v_mfma_f32_16x16x32_bf16 v[84:87], a[236:239], v[248:251], v[84:87]\n"
"buffer_load_dwordx4 v4, s[16:19], s41 offen nt lds\n"
"buffer_load_dwordx4 v5, s[16:19], s41 offen offset:1024 nt lds\n"
"v_add_f32 v22, v22, v52\n"
"v_mfma_f32_16x16x32_bf16 a[0:3], a[240:243], v[204:207], a[0:3]\n"
"v_exp_f32 v54, v54\n"
"v_mfma_f32_16x16x32_bf16 a[4:7], a[240:243], v[212:215], a[4:7]\n"
"s_add_u32 s73, s73, 2048\n"
"s_cbranch_vccnz 541b\n"
"551:\n"
"v_add_f32 v22, v22, v53\n"
"v_cvt_pk_bf16_f32 v164, v52, v53\n"
"v_mfma_f32_16x16x32_bf16 a[8:11], a[240:243], v[220:223], a[8:11]\n"
"v_exp_f32 v55, v55\n"
"v_mfma_f32_16x16x32_bf16 a[12:15], a[240:243], v[228:231], a[12:15]\n"
"s_add_u32 s72, s72, 2048\n"
"ds_read_b128 a[224:227], v8 offset:4096\n"
"v_add_f32 v22, v22, v54\n"
"v_mfma_f32_16x16x32_bf16 a[16:19], a[240:243], v[236:239], a[16:19]\n"
"v_exp_f32 v60, v60\n"
"v_mfma_f32_16x16x32_bf16 a[20:23], a[240:243], v[244:247], a[20:23]\n"
"s_or_b32 s72, s72, 24576\n"
"v_add_f32 v22, v22, v55\n"
"v_cvt_pk_bf16_f32 v165, v54, v55\n"
"v_mfma_f32_16x16x32_bf16 a[24:27], a[240:243], v[252:255], a[24:27]\n"
"s_waitcnt lgkmcnt(5)\n"
"v_exp_f32 v61, v61\n"
"v_mfma_f32_16x16x32_bf16 a[28:31], a[244:247], v[204:207], a[28:31]\n"
"s_xor_b32 s41, s40, s73\n"
"ds_read_b128 a[228:231], v8 offset:4352\n"
"v_add_f32 v24, v24, v60\n"
"v_mfma_f32_16x16x32_bf16 a[32:35], a[244:247], v[212:215], a[32:35]\n"
"v_exp_f32 v62, v62\n"
"v_mfma_f32_16x16x32_bf16 a[36:39], a[244:247], v[220:223], a[36:39]\n"
"v_add_f32 v24, v24, v61\n"
"v_cvt_pk_bf16_f32 v172, v60, v61\n"
"v_mfma_f32_16x16x32_bf16 a[40:43], a[244:247], v[228:231], a[40:43]\n"
"v_exp_f32 v63, v63\n"
"v_mfma_f32_16x16x32_bf16 a[44:47], a[244:247], v[236:239], a[44:47]\n"
"ds_read_b128 a[232:235], v8 offset:4608\n"
"v_add_f32 v24, v24, v62\n"
"v_mfma_f32_16x16x32_bf16 a[48:51], a[244:247], v[244:247], a[48:51]\n"
"s_waitcnt lgkmcnt(5)\n"
"v_exp_f32 v68, v68\n"
"v_mfma_f32_16x16x32_bf16 a[52:55], a[244:247], v[252:255], a[52:55]\n"
"v_add_f32 v24, v24, v63\n"
"v_cvt_pk_bf16_f32 v173, v62, v63\n"
"v_mfma_f32_16x16x32_bf16 a[56:59], a[248:251], v[204:207], a[56:59]\n"
"v_exp_f32 v69, v69\n"
"v_mfma_f32_16x16x32_bf16 a[60:63], a[248:251], v[212:215], a[60:63]\n"
"ds_read_b128 a[236:239], v8 offset:4864\n"
"v_add_f32 v26, v26, v68\n"
"v_mfma_f32_16x16x32_bf16 a[64:67], a[248:251], v[220:223], a[64:67]\n"
"v_exp_f32 v70, v70\n"
"v_mfma_f32_16x16x32_bf16 a[68:71], a[248:251], v[228:231], a[68:71]\n"
"v_add_f32 v26, v26, v69\n"
"v_cvt_pk_bf16_f32 v180, v68, v69\n"
"v_mfma_f32_16x16x32_bf16 a[72:75], a[248:251], v[236:239], a[72:75]\n"
"v_exp_f32 v71, v71\n"
"v_mfma_f32_16x16x32_bf16 a[76:79], a[248:251], v[244:247], a[76:79]\n"
"ds_read_b64_tr_b16 a[240:241], v9 offset:33280\n"
"v_add_f32 v26, v26, v70\n"
"v_mfma_f32_16x16x32_bf16 a[80:83], a[248:251], v[252:255], a[80:83]\n"
"s_waitcnt lgkmcnt(5)\n"
"v_exp_f32 v76, v76\n"
"v_mfma_f32_16x16x32_bf16 a[84:87], a[252:255], v[204:207], a[84:87]\n"
"s_and_b32 m0, s72, s37\n"
"v_add_f32 v26, v26, v71\n"
"v_cvt_pk_bf16_f32 v181, v70, v71\n"
"v_mfma_f32_16x16x32_bf16 a[88:91], a[252:255], v[212:215], a[88:91]\n"
"v_exp_f32 v77, v77\n"
"v_mfma_f32_16x16x32_bf16 a[92:95], a[252:255], v[220:223], a[92:95]\n"
"ds_read_b64_tr_b16 a[242:243], v9 offset:37376\n"
"v_add_f32 v28, v28, v76\n"
"v_mfma_f32_16x16x32_bf16 a[96:99], a[252:255], v[228:231], a[96:99]\n"
"v_exp_f32 v78, v78\n"
"v_mfma_f32_16x16x32_bf16 a[100:103], a[252:255], v[236:239], a[100:103]\n"
"v_add_f32 v28, v28, v77\n"
"v_cvt_pk_bf16_f32 v188, v76, v77\n"
"v_mfma_f32_16x16x32_bf16 a[104:107], a[252:255], v[244:247], a[104:107]\n"
"s_waitcnt lgkmcnt(5)\n"
"v_exp_f32 v79, v79\n"
"v_mfma_f32_16x16x32_bf16 a[108:111], a[252:255], v[252:255], a[108:111]\n"
"buffer_load_dwordx4 v4, s[16:19], s41 offen nt lds\n"
"buffer_load_dwordx4 v5, s[16:19], s41 offen offset:1024 nt lds\n"
"v_add_f32 v28, v28, v78\n"
"v_mfma_f32_16x16x32_bf16 v[92:95], a[224:227], v[32:35], 0\n"
"s_waitcnt lgkmcnt(3)\n"
"v_exp_f32 v84, v84\n"
"v_mfma_f32_16x16x32_bf16 v[92:95], a[228:231], v[88:91], v[92:95]\n"
"s_add_u32 s73, s73, 2048\n"
"s_cbranch_vccnz 542b\n"
"552:\n"
"v_add_f32 v28, v28, v79\n"
"v_cvt_pk_bf16_f32 v189, v78, v79\n"
"v_mfma_f32_16x16x32_bf16 v[92:95], a[232:235], v[144:147], v[92:95]\n"
"s_waitcnt lgkmcnt(2)\n"
"v_exp_f32 v85, v85\n"
"v_mfma_f32_16x16x32_bf16 v[92:95], a[236:239], v[200:203], v[92:95]\n"
"s_add_u32 s72, s72, 2048\n"
"ds_read_b64_tr_b16 a[244:245], v10 offset:33280\n"
"v_add_f32 v30, v30, v84\n"
"v_mfma_f32_16x16x32_bf16 v[100:103], a[224:227], v[40:43], 0\n"
"v_exp_f32 v86, v86\n"
"v_mfma_f32_16x16x32_bf16 v[100:103], a[228:231], v[96:99], v[100:103]\n"
"s_or_b32 s72, s72, 24576\n"
"v_add_f32 v30, v30, v85\n"
"v_cvt_pk_bf16_f32 v196, v84, v85\n"
"v_mfma_f32_16x16x32_bf16 v[100:103], a[232:235], v[152:155], v[100:103]\n"
"v_exp_f32 v87, v87\n"
"v_mfma_f32_16x16x32_bf16 v[100:103], a[236:239], v[208:211], v[100:103]\n"
"s_xor_b32 s41, s40, s73\n"
"ds_read_b64_tr_b16 a[246:247], v10 offset:37376\n"
"v_add_f32 v30, v30, v86\n"
"v_mfma_f32_16x16x32_bf16 v[108:111], a[224:227], v[48:51], 0\n"
"v_exp_f32 v92, v92\n"
"v_mfma_f32_16x16x32_bf16 v[108:111], a[228:231], v[104:107], v[108:111]\n"
"s_add_u32 s39, s39, 130\n"
"v_add_f32 v30, v30, v87\n"
"v_cvt_pk_bf16_f32 v197, v86, v87\n"
"v_mfma_f32_16x16x32_bf16 v[108:111], a[232:235], v[160:163], v[108:111]\n"
"v_exp_f32 v93, v93\n"
"v_mfma_f32_16x16x32_bf16 v[108:111], a[236:239], v[216:219], v[108:111]\n"
"ds_read_b64_tr_b16 a[248:249], v9 offset:33536\n"
"v_add_f32 v18, v18, v92\n"
"v_mfma_f32_16x16x32_bf16 v[116:119], a[224:227], v[56:59], 0\n"
"v_exp_f32 v94, v94\n"
"v_mfma_f32_16x16x32_bf16 v[116:119], a[228:231], v[112:115], v[116:119]\n"
"v_add_f32 v18, v18, v93\n"
"v_cvt_pk_bf16_f32 v150, v92, v93\n"
"v_mfma_f32_16x16x32_bf16 v[116:119], a[232:235], v[168:171], v[116:119]\n"
"v_exp_f32 v95, v95\n"
"v_mfma_f32_16x16x32_bf16 v[116:119], a[236:239], v[224:227], v[116:119]\n"
"ds_read_b64_tr_b16 a[250:251], v9 offset:37632\n"
"v_add_f32 v18, v18, v94\n"
"v_mfma_f32_16x16x32_bf16 v[124:127], a[224:227], v[64:67], 0\n"
"v_exp_f32 v100, v100\n"
"v_mfma_f32_16x16x32_bf16 v[124:127], a[228:231], v[120:123], v[124:127]\n"
"s_memrealtime s[78:79]\n"
"v_add_f32 v18, v18, v95\n"
"v_cvt_pk_bf16_f32 v151, v94, v95\n"
"v_mfma_f32_16x16x32_bf16 v[124:127], a[232:235], v[176:179], v[124:127]\n"
"v_exp_f32 v101, v101\n"
"v_mfma_f32_16x16x32_bf16 v[124:127], a[236:239], v[232:235], v[124:127]\n"
"ds_read_b64_tr_b16 a[252:253], v10 offset:33536\n"
"v_add_f32 v20, v20, v100\n"
"v_mfma_f32_16x16x32_bf16 v[132:135], a[224:227], v[72:75], 0\n"
"v_exp_f32 v102, v102\n"
"v_mfma_f32_16x16x32_bf16 v[132:135], a[228:231], v[128:131], v[132:135]\n"
"s_and_b32 m0, s72, s37\n"
"v_add_f32 v20, v20, v101\n"
"v_cvt_pk_bf16_f32 v158, v100, v101\n"
"v_mfma_f32_16x16x32_bf16 v[132:135], a[232:235], v[184:187], v[132:135]\n"
"v_exp_f32 v103, v103\n"
"v_mfma_f32_16x16x32_bf16 v[132:135], a[236:239], v[240:243], v[132:135]\n"
"ds_read_b64_tr_b16 a[254:255], v10 offset:37632\n"
"v_add_f32 v20, v20, v102\n"
"v_xor_b32 v9, 65536, v9\n"
"v_mfma_f32_16x16x32_bf16 v[140:143], a[224:227], v[80:83], 0\n"
"v_exp_f32 v108, v108\n"
"v_mfma_f32_16x16x32_bf16 v[140:143], a[228:231], v[136:139], v[140:143]\n"
"v_add_f32 v20, v20, v103\n"
"v_cvt_pk_bf16_f32 v159, v102, v103\n"
"v_mfma_f32_16x16x32_bf16 v[140:143], a[232:235], v[192:195], v[140:143]\n"
"s_waitcnt lgkmcnt(6)\n"
"v_exp_f32 v109, v109\n"
"v_mfma_f32_16x16x32_bf16 v[140:143], a[236:239], v[248:251], v[140:143]\n"
"buffer_load_dwordx4 v4, s[16:19], s41 offen nt lds\n"
"buffer_load_dwordx4 v5, s[16:19], s41 offen offset:1024 nt lds\n"
"v_add_f32 v22, v22, v108\n"
"v_xor_b32 v10, 65536, v10\n"
"v_mfma_f32_16x16x32_bf16 a[112:115], a[240:243], v[204:207], a[112:115]\n"
"v_exp_f32 v110, v110\n"
"v_mfma_f32_16x16x32_bf16 a[116:119], a[240:243], v[212:215], a[116:119]\n"
"s_add_u32 s73, s73, 2048\n"
"v_add_f32 v22, v22, v109\n"
"v_cvt_pk_bf16_f32 v166, v108, v109\n"
"v_mfma_f32_16x16x32_bf16 a[120:123], a[240:243], v[220:223], a[120:123]\n"
"s_waitcnt vmcnt(9) lgkmcnt(0)\n"
"s_barrier\n"
"s_cbranch_vccnz 543b\n"
"553:\n"
"v_exp_f32 v111, v111\n"
"v_mfma_f32_16x16x32_bf16 a[124:127], a[240:243], v[228:231], a[124:127]\n"
"s_add_u32 s72, s72, 2048\n"
"ds_read_b128 a[224:227], v8 offset:32768\n"
"v_add_f32 v22, v22, v110\n"
"v_mfma_f32_16x16x32_bf16 a[128:131], a[240:243], v[236:239], a[128:131]\n"
"v_exp_f32 v116, v116\n"
"v_mfma_f32_16x16x32_bf16 a[132:135], a[240:243], v[244:247], a[132:135]\n"
"s_or_b32 s72, s72, 24576\n"
"v_add_f32 v22, v22, v111\n"
"v_cvt_pk_bf16_f32 v167, v110, v111\n"
"v_mfma_f32_16x16x32_bf16 a[136:139], a[240:243], v[252:255], a[136:139]\n"
"v_exp_f32 v117, v117\n"
"v_mfma_f32_16x16x32_bf16 a[140:143], a[244:247], v[204:207], a[140:143]\n"
"s_xor_b32 s41, s40, s73\n"
"ds_read_b128 a[228:231], v8 offset:33024\n"
"v_add_f32 v24, v24, v116\n"
"v_mfma_f32_16x16x32_bf16 a[144:147], a[244:247], v[212:215], a[144:147]\n"
"v_exp_f32 v118, v118\n"
"v_mfma_f32_16x16x32_bf16 a[148:151], a[244:247], v[220:223], a[148:151]\n"
"s_cmp_gt_u32 s39, s78\n"
"s_cselect_b64 vcc, -1, 0\n"
"v_add_f32 v24, v24, v117\n"
"v_cvt_pk_bf16_f32 v174, v116, v117\n"
"v_mfma_f32_16x16x32_bf16 a[152:155], a[244:247], v[228:231], a[152:155]\n"
"v_exp_f32 v119, v119\n"
"v_mfma_f32_16x16x32_bf16 a[156:159], a[244:247], v[236:239], a[156:159]\n"
"ds_read_b128 a[232:235], v8 offset:33280\n"
"v_add_f32 v24, v24, v118\n"
"v_mfma_f32_16x16x32_bf16 a[160:163], a[244:247], v[244:247], a[160:163]\n"
"v_exp_f32 v124, v124\n"
"v_mfma_f32_16x16x32_bf16 a[164:167], a[244:247], v[252:255], a[164:167]\n"
"v_add_f32 v24, v24, v119\n"
"v_cvt_pk_bf16_f32 v175, v118, v119\n"
"v_mfma_f32_16x16x32_bf16 a[168:171], a[248:251], v[204:207], a[168:171]\n"
"v_exp_f32 v125, v125\n"
"v_mfma_f32_16x16x32_bf16 a[172:175], a[248:251], v[212:215], a[172:175]\n"
"s_cbranch_vccnz 544b\n"
"554:\n"
"ds_read_b128 a[236:239], v8 offset:33536\n"
"v_add_f32 v26, v26, v124\n"
"v_mfma_f32_16x16x32_bf16 a[176:179], a[248:251], v[220:223], a[176:179]\n"
"v_exp_f32 v126, v126\n"
"v_mfma_f32_16x16x32_bf16 a[180:183], a[248:251], v[228:231], a[180:183]\n"
"v_add_f32 v26, v26, v125\n"
"v_cvt_pk_bf16_f32 v182, v124, v125\n"
"v_mfma_f32_16x16x32_bf16 a[184:187], a[248:251], v[236:239], a[184:187]\n"
"v_exp_f32 v127, v127\n"
"v_mfma_f32_16x16x32_bf16 a[188:191], a[248:251], v[244:247], a[188:191]\n"
"ds_read_b64_tr_b16 a[240:241], v9 offset:0\n"
"v_add_f32 v26, v26, v126\n"
"v_mfma_f32_16x16x32_bf16 a[192:195], a[248:251], v[252:255], a[192:195]\n"
"v_exp_f32 v132, v132\n"
"v_mfma_f32_16x16x32_bf16 a[196:199], a[252:255], v[204:207], a[196:199]\n"
"s_and_b32 m0, s72, s37\n"
"v_add_f32 v26, v26, v127\n"
"v_cvt_pk_bf16_f32 v183, v126, v127\n"
"v_mfma_f32_16x16x32_bf16 a[200:203], a[252:255], v[212:215], a[200:203]\n"
"v_exp_f32 v133, v133\n"
"v_mfma_f32_16x16x32_bf16 a[204:207], a[252:255], v[220:223], a[204:207]\n"
"ds_read_b64_tr_b16 a[242:243], v9 offset:4096\n"
"v_add_f32 v28, v28, v132\n"
"v_mfma_f32_16x16x32_bf16 a[208:211], a[252:255], v[228:231], a[208:211]\n"
"v_exp_f32 v134, v134\n"
"v_mfma_f32_16x16x32_bf16 a[212:215], a[252:255], v[236:239], a[212:215]\n"
"v_add_f32 v28, v28, v133\n"
"v_cvt_pk_bf16_f32 v190, v132, v133\n"
"v_mfma_f32_16x16x32_bf16 a[216:219], a[252:255], v[244:247], a[216:219]\n"
"s_waitcnt lgkmcnt(5)\n"
"v_exp_f32 v135, v135\n"
"v_mfma_f32_16x16x32_bf16 a[220:223], a[252:255], v[252:255], a[220:223]\n"
"buffer_load_dwordx4 v4, s[16:19], s41 offen nt lds\n"
"buffer_load_dwordx4 v5, s[16:19], s41 offen offset:1024 nt lds\n"
"v_add_f32 v28, v28, v134\n"
"v_mfma_f32_16x16x32_bf16 v[36:39], a[224:227], v[32:35], 0\n"
"s_waitcnt lgkmcnt(3)\n"
"v_exp_f32 v140, v140\n"
"v_mfma_f32_16x16x32_bf16 v[36:39], a[228:231], v[88:91], v[36:39]\n"
"s_add_u32 s73, s73, 2048\n"
"s_cbranch_vccnz 560b\n"
"570:\n"
"v_add_f32 v28, v28, v135\n"
"v_cvt_pk_bf16_f32 v191, v134, v135\n"
"v_mfma_f32_16x16x32_bf16 v[36:39], a[232:235], v[144:147], v[36:39]\n"
"s_waitcnt lgkmcnt(2)\n"
"v_exp_f32 v141, v141\n"
"v_mfma_f32_16x16x32_bf16 v[36:39], a[236:239], v[200:203], v[36:39]\n"
"s_add_u32 s72, s72, 2048\n"
"ds_read_b64_tr_b16 a[244:245], v10 offset:0\n"
"v_add_f32 v30, v30, v140\n"
"v_mfma_f32_16x16x32_bf16 v[44:47], a[224:227], v[40:43], 0\n"
"v_exp_f32 v142, v142\n"
"v_mfma_f32_16x16x32_bf16 v[44:47], a[228:231], v[96:99], v[44:47]\n"
"s_or_b32 s72, s72, 24576\n"
"v_add_f32 v30, v30, v141\n"
"v_cvt_pk_bf16_f32 v198, v140, v141\n"
"v_mfma_f32_16x16x32_bf16 v[44:47], a[232:235], v[152:155], v[44:47]\n"
"v_exp_f32 v143, v143\n"
"v_mfma_f32_16x16x32_bf16 v[44:47], a[236:239], v[208:211], v[44:47]\n"
"s_xor_b32 s41, s40, s73\n"
"ds_read_b64_tr_b16 a[246:247], v10 offset:4096\n"
"v_add_f32 v30, v30, v142\n"
"v_mfma_f32_16x16x32_bf16 v[52:55], a[224:227], v[48:51], 0\n"
"v_exp_f32 v36, v36\n"
"v_mfma_f32_16x16x32_bf16 v[52:55], a[228:231], v[104:107], v[52:55]\n"
"v_add_f32 v30, v30, v143\n"
"v_cvt_pk_bf16_f32 v199, v142, v143\n"
"v_mfma_f32_16x16x32_bf16 v[52:55], a[232:235], v[160:163], v[52:55]\n"
"v_exp_f32 v37, v37\n"
"v_mfma_f32_16x16x32_bf16 v[52:55], a[236:239], v[216:219], v[52:55]\n"
"ds_read_b64_tr_b16 a[248:249], v9 offset:256\n"
"v_add_f32 v18, v18, v36\n"
"v_mfma_f32_16x16x32_bf16 v[60:63], a[224:227], v[56:59], 0\n"
"v_exp_f32 v38, v38\n"
"v_mfma_f32_16x16x32_bf16 v[60:63], a[228:231], v[112:115], v[60:63]\n"
"v_add_f32 v18, v18, v37\n"
"v_cvt_pk_bf16_f32 v204, v36, v37\n"
"v_mfma_f32_16x16x32_bf16 v[60:63], a[232:235], v[168:171], v[60:63]\n"
"v_exp_f32 v39, v39\n"
"v_mfma_f32_16x16x32_bf16 v[60:63], a[236:239], v[224:227], v[60:63]\n"
"ds_read_b64_tr_b16 a[250:251], v9 offset:4352\n"
"v_add_f32 v18, v18, v38\n"
"v_mfma_f32_16x16x32_bf16 v[68:71], a[224:227], v[64:67], 0\n"
"v_exp_f32 v44, v44\n"
"v_mfma_f32_16x16x32_bf16 v[68:71], a[228:231], v[120:123], v[68:71]\n"
"v_add_f32 v18, v18, v39\n"
"v_cvt_pk_bf16_f32 v205, v38, v39\n"
"v_mfma_f32_16x16x32_bf16 v[68:71], a[232:235], v[176:179], v[68:71]\n"
"v_exp_f32 v45, v45\n"
"v_mfma_f32_16x16x32_bf16 v[68:71], a[236:239], v[232:235], v[68:71]\n"
"ds_read_b64_tr_b16 a[252:253], v10 offset:256\n"
"v_add_f32 v20, v20, v44\n"
"v_mfma_f32_16x16x32_bf16 v[76:79], a[224:227], v[72:75], 0\n"
"v_exp_f32 v46, v46\n"
"v_mfma_f32_16x16x32_bf16 v[76:79], a[228:231], v[128:131], v[76:79]\n"
"s_and_b32 m0, s72, s37\n"
"v_add_f32 v20, v20, v45\n"
"v_cvt_pk_bf16_f32 v212, v44, v45\n"
"v_mfma_f32_16x16x32_bf16 v[76:79], a[232:235], v[184:187], v[76:79]\n"
"v_exp_f32 v47, v47\n"
"v_mfma_f32_16x16x32_bf16 v[76:79], a[236:239], v[240:243], v[76:79]\n"
"ds_read_b64_tr_b16 a[254:255], v10 offset:4352\n"
"v_add_f32 v20, v20, v46\n"
"v_mfma_f32_16x16x32_bf16 v[84:87], a[224:227], v[80:83], 0\n"
"v_exp_f32 v52, v52\n"
"v_mfma_f32_16x16x32_bf16 v[84:87], a[228:231], v[136:139], v[84:87]\n"
"v_add_f32 v20, v20, v47\n"
"v_cvt_pk_bf16_f32 v213, v46, v47\n"
"v_mfma_f32_16x16x32_bf16 v[84:87], a[232:235], v[192:195], v[84:87]\n"
"s_waitcnt lgkmcnt(6)\n"
"v_exp_f32 v53, v53\n"
"v_mfma_f32_16x16x32_bf16 v[84:87], a[236:239], v[248:251], v[84:87]\n"
"buffer_load_dwordx4 v4, s[16:19], s41 offen nt lds\n"
"buffer_load_dwordx4 v5, s[16:19], s41 offen offset:1024 nt lds\n"
"v_add_f32 v22, v22, v52\n"
"v_mfma_f32_16x16x32_bf16 a[0:3], a[240:243], v[148:151], a[0:3]\n"
"v_exp_f32 v54, v54\n"
"v_mfma_f32_16x16x32_bf16 a[4:7], a[240:243], v[156:159], a[4:7]\n"
"s_add_u32 s73, s73, 2048\n"
"s_cbranch_vccnz 561b\n"
"571:\n"
"v_add_f32 v22, v22, v53\n"
"v_cvt_pk_bf16_f32 v220, v52, v53\n"
"v_mfma_f32_16x16x32_bf16 a[8:11], a[240:243], v[164:167], a[8:11]\n"
"v_exp_f32 v55, v55\n"
"v_mfma_f32_16x16x32_bf16 a[12:15], a[240:243], v[172:175], a[12:15]\n"
"s_add_u32 s72, s72, 2048\n"
"ds_read_b128 a[224:227], v8 offset:36864\n"
"v_add_f32 v22, v22, v54\n"
"v_mfma_f32_16x16x32_bf16 a[16:19], a[240:243], v[180:183], a[16:19]\n"
"v_exp_f32 v60, v60\n"
"v_mfma_f32_16x16x32_bf16 a[20:23], a[240:243], v[188:191], a[20:23]\n"
"s_or_b32 s72, s72, 24576\n"
"v_add_f32 v22, v22, v55\n"
"v_cvt_pk_bf16_f32 v221, v54, v55\n"
"v_mfma_f32_16x16x32_bf16 a[24:27], a[240:243], v[196:199], a[24:27]\n"
"s_waitcnt lgkmcnt(5)\n"
"v_exp_f32 v61, v61\n"
"v_mfma_f32_16x16x32_bf16 a[28:31], a[244:247], v[148:151], a[28:31]\n"
"s_xor_b32 s41, s40, s73\n"
"ds_read_b128 a[228:231], v8 offset:37120\n"
"v_add_f32 v24, v24, v60\n"
"v_mfma_f32_16x16x32_bf16 a[32:35], a[244:247], v[156:159], a[32:35]\n"
"s_waitcnt vmcnt(10)\n"
"v_exp_f32 v62, v62\n"
"v_mfma_f32_16x16x32_bf16 a[36:39], a[244:247], v[164:167], a[36:39]\n"
"v_add_f32 v24, v24, v61\n"
"v_cvt_pk_bf16_f32 v228, v60, v61\n"
"v_cmp_eq_u64 vcc, s[68:69], v[16:17]\n"
"v_mfma_f32_16x16x32_bf16 a[40:43], a[244:247], v[172:175], a[40:43]\n"
"v_exp_f32 v63, v63\n"
"v_mfma_f32_16x16x32_bf16 a[44:47], a[244:247], v[180:183], a[44:47]\n"
"ds_read_b128 a[232:235], v8 offset:37376\n"
"v_add_f32 v24, v24, v62\n"
"v_cmp_eq_u64 s[70:71], s[42:43], v[16:17]\n"
"v_mfma_f32_16x16x32_bf16 a[48:51], a[244:247], v[188:191], a[48:51]\n"
"s_waitcnt lgkmcnt(5)\n"
"v_exp_f32 v68, v68\n"
"v_mfma_f32_16x16x32_bf16 a[52:55], a[244:247], v[196:199], a[52:55]\n"
"s_nor_b64 vcc, vcc, s[70:71]\n"
"s_cbranch_scc1 97f\n"
"98:\n"
"s_cmp_gt_u32 s39, s78\n"
"s_cselect_b64 vcc, -1, 0\n"
"v_add_f32 v24, v24, v63\n"
"v_cvt_pk_bf16_f32 v229, v62, v63\n"
"v_mfma_f32_16x16x32_bf16 a[56:59], a[248:251], v[148:151], a[56:59]\n"
"v_exp_f32 v69, v69\n"
"v_mfma_f32_16x16x32_bf16 a[60:63], a[248:251], v[156:159], a[60:63]\n"
"s_bcnt1_i32_b64 s67, s[70:71]\n"
"ds_read_b128 a[236:239], v8 offset:37632\n"
"v_add_f32 v26, v26, v68\n"
"v_mfma_f32_16x16x32_bf16 a[64:67], a[248:251], v[164:167], a[64:67]\n"
"v_exp_f32 v70, v70\n"
"v_mfma_f32_16x16x32_bf16 a[68:71], a[248:251], v[172:175], a[68:71]\n"
"s_add_u32 s67, s67, 31\n"
"v_add_f32 v26, v26, v69\n"
"v_cvt_pk_bf16_f32 v236, v68, v69\n"
"v_mfma_f32_16x16x32_bf16 a[72:75], a[248:251], v[180:183], a[72:75]\n"
"v_exp_f32 v71, v71\n"
"v_mfma_f32_16x16x32_bf16 a[76:79], a[248:251], v[188:191], a[76:79]\n"
"s_and_b32 s67, s67, 64\n"
"ds_read_b64_tr_b16 a[240:241], v9 offset:512\n"
"v_add_f32 v26, v26, v70\n"
"v_mfma_f32_16x16x32_bf16 a[80:83], a[248:251], v[196:199], a[80:83]\n"
"s_waitcnt lgkmcnt(5)\n"
"v_exp_f32 v76, v76\n"
"v_mfma_f32_16x16x32_bf16 a[84:87], a[252:255], v[148:151], a[84:87]\n"
"s_and_b32 m0, s72, s37\n"
"v_add_f32 v26, v26, v71\n"
"v_cvt_pk_bf16_f32 v237, v70, v71\n"
"v_mfma_f32_16x16x32_bf16 a[88:91], a[252:255], v[156:159], a[88:91]\n"
"v_exp_f32 v77, v77\n"
"v_mfma_f32_16x16x32_bf16 a[92:95], a[252:255], v[164:167], a[92:95]\n"
"s_lshl_b32 s67, s67, 7\n"
"ds_read_b64_tr_b16 a[242:243], v9 offset:4608\n"
"v_add_f32 v28, v28, v76\n"
"v_mfma_f32_16x16x32_bf16 a[96:99], a[252:255], v[172:175], a[96:99]\n"
"v_exp_f32 v78, v78\n"
"v_mfma_f32_16x16x32_bf16 a[100:103], a[252:255], v[180:183], a[100:103]\n"
"s_xor_b32 s60, s60, s67\n"
"v_add_f32 v28, v28, v77\n"
"v_cvt_pk_bf16_f32 v244, v76, v77\n"
"v_mfma_f32_16x16x32_bf16 a[104:107], a[252:255], v[188:191], a[104:107]\n"
"s_waitcnt lgkmcnt(5)\n"
"v_exp_f32 v79, v79\n"
"v_mfma_f32_16x16x32_bf16 a[108:111], a[252:255], v[196:199], a[108:111]\n"
"s_xor_b32 s59, s59, s67\n"
"buffer_load_dwordx4 v4, s[16:19], s41 offen nt lds\n"
"buffer_load_dwordx4 v5, s[16:19], s41 offen offset:1024 nt lds\n"
"v_add_f32 v28, v28, v78\n"
"v_mfma_f32_16x16x32_bf16 v[92:95], a[224:227], v[32:35], 0\n"
"s_waitcnt lgkmcnt(3)\n"
"v_exp_f32 v84, v84\n"
"v_mfma_f32_16x16x32_bf16 v[92:95], a[228:231], v[88:91], v[92:95]\n"
"s_add_u32 s73, s73, 2048\n"
"s_cbranch_vccnz 562b\n"
"572:\n"
"v_add_f32 v28, v28, v79\n"
"v_cvt_pk_bf16_f32 v245, v78, v79\n"
"v_mfma_f32_16x16x32_bf16 v[92:95], a[232:235], v[144:147], v[92:95]\n"
"s_waitcnt lgkmcnt(2)\n"
"v_exp_f32 v85, v85\n"
"v_mfma_f32_16x16x32_bf16 v[92:95], a[236:239], v[200:203], v[92:95]\n"
"s_add_u32 s72, s72, 2048\n"
"ds_read_b64_tr_b16 a[244:245], v10 offset:512\n"
"v_add_f32 v30, v30, v84\n"
"v_mfma_f32_16x16x32_bf16 v[100:103], a[224:227], v[40:43], 0\n"
"v_exp_f32 v86, v86\n"
"v_mfma_f32_16x16x32_bf16 v[100:103], a[228:231], v[96:99], v[100:103]\n"
"s_or_b32 s72, s72, 24576\n"
"v_add_f32 v30, v30, v85\n"
"v_cvt_pk_bf16_f32 v252, v84, v85\n"
"v_mfma_f32_16x16x32_bf16 v[100:103], a[232:235], v[152:155], v[100:103]\n"
"v_exp_f32 v87, v87\n"
"v_mfma_f32_16x16x32_bf16 v[100:103], a[236:239], v[208:211], v[100:103]\n"
"s_xor_b32 s41, s40, s73\n"
"ds_read_b64_tr_b16 a[246:247], v10 offset:4608\n"
"v_add_f32 v30, v30, v86\n"
"v_mfma_f32_16x16x32_bf16 v[108:111], a[224:227], v[48:51], 0\n"
"v_exp_f32 v92, v92\n"
"v_mfma_f32_16x16x32_bf16 v[108:111], a[228:231], v[104:107], v[108:111]\n"
"s_xor_b32 s61, s61, s67\n"
"v_add_f32 v30, v30, v87\n"
"v_cvt_pk_bf16_f32 v253, v86, v87\n"
"v_mfma_f32_16x16x32_bf16 v[108:111], a[232:235], v[160:163], v[108:111]\n"
"v_exp_f32 v93, v93\n"
"v_mfma_f32_16x16x32_bf16 v[108:111], a[236:239], v[216:219], v[108:111]\n"
"s_xor_b32 s50, s50, s67\n"
"ds_read_b64_tr_b16 a[248:249], v9 offset:768\n"
"v_add_f32 v18, v18, v92\n"
"v_mfma_f32_16x16x32_bf16 v[116:119], a[224:227], v[56:59], 0\n"
"v_exp_f32 v94, v94\n"
"v_mfma_f32_16x16x32_bf16 v[116:119], a[228:231], v[112:115], v[116:119]\n"
"s_xor_b32 s51, s51, s67\n"
"v_add_f32 v18, v18, v93\n"
"v_cvt_pk_bf16_f32 v206, v92, v93\n"
"v_mfma_f32_16x16x32_bf16 v[116:119], a[232:235], v[168:171], v[116:119]\n"
"v_exp_f32 v95, v95\n"
"v_mfma_f32_16x16x32_bf16 v[116:119], a[236:239], v[224:227], v[116:119]\n"
"s_xor_b32 s52, s52, s67\n"
"ds_read_b64_tr_b16 a[250:251], v9 offset:4864\n"
"v_add_f32 v18, v18, v94\n"
"v_mfma_f32_16x16x32_bf16 v[124:127], a[224:227], v[64:67], 0\n"
"v_exp_f32 v100, v100\n"
"v_mfma_f32_16x16x32_bf16 v[124:127], a[228:231], v[120:123], v[124:127]\n"
"s_xor_b32 s53, s53, s67\n"
"s_memrealtime s[78:79]\n"
"v_add_f32 v18, v18, v95\n"
"v_cvt_pk_bf16_f32 v207, v94, v95\n"
"v_mfma_f32_16x16x32_bf16 v[124:127], a[232:235], v[176:179], v[124:127]\n"
"v_exp_f32 v101, v101\n"
"v_mfma_f32_16x16x32_bf16 v[124:127], a[236:239], v[232:235], v[124:127]\n"
"s_xor_b32 s54, s54, s67\n"
"ds_read_b64_tr_b16 a[252:253], v10 offset:768\n"
"v_add_f32 v20, v20, v100\n"
"v_mfma_f32_16x16x32_bf16 v[132:135], a[224:227], v[72:75], 0\n"
"v_exp_f32 v102, v102\n"
"v_mfma_f32_16x16x32_bf16 v[132:135], a[228:231], v[128:131], v[132:135]\n"
"s_and_b32 m0, s72, s37\n"
"v_add_f32 v20, v20, v101\n"
"v_cvt_pk_bf16_f32 v214, v100, v101\n"
"v_mfma_f32_16x16x32_bf16 v[132:135], a[232:235], v[184:187], v[132:135]\n"
"v_exp_f32 v103, v103\n"
"v_mfma_f32_16x16x32_bf16 v[132:135], a[236:239], v[240:243], v[132:135]\n"
"s_xor_b32 s55, s55, s67\n"
"ds_read_b64_tr_b16 a[254:255], v10 offset:4864\n"
"v_add_f32 v20, v20, v102\n"
"v_mfma_f32_16x16x32_bf16 v[140:143], a[224:227], v[80:83], 0\n"
"v_exp_f32 v108, v108\n"
"v_mfma_f32_16x16x32_bf16 v[140:143], a[228:231], v[136:139], v[140:143]\n"
"v_add_f32 v20, v20, v103\n"
"v_cvt_pk_bf16_f32 v215, v102, v103\n"
"v_mfma_f32_16x16x32_bf16 v[140:143], a[232:235], v[192:195], v[140:143]\n"
"s_waitcnt lgkmcnt(6)\n"
"v_exp_f32 v109, v109\n"
"v_mfma_f32_16x16x32_bf16 v[140:143], a[236:239], v[248:251], v[140:143]\n"
"buffer_load_dwordx4 v4, s[16:19], s41 offen nt lds\n"
"buffer_load_dwordx4 v5, s[16:19], s41 offen offset:1024 nt lds\n"
"v_add_f32 v22, v22, v108\n"
"v_mfma_f32_16x16x32_bf16 a[112:115], a[240:243], v[148:151], a[112:115]\n"
"v_exp_f32 v110, v110\n"
"v_mfma_f32_16x16x32_bf16 a[116:119], a[240:243], v[156:159], a[116:119]\n"
"s_add_u32 s73, s73, 2048\n"
"v_add_f32 v22, v22, v109\n"
"v_cvt_pk_bf16_f32 v222, v108, v109\n"
"v_xor_b32 v8, 65536, v8\n"
"v_mfma_f32_16x16x32_bf16 a[120:123], a[240:243], v[164:167], a[120:123]\n"
"s_waitcnt vmcnt(8) lgkmcnt(0)\n"
"s_barrier\n"
"s_cbranch_vccnz 563b\n"
"573:\n"
"v_exp_f32 v111, v111\n"
"v_mfma_f32_16x16x32_bf16 a[124:127], a[240:243], v[172:175], a[124:127]\n"
"s_add_u32 s72, s72, 2048\n"
"ds_read_b128 a[224:227], v8 offset:0\n"
"v_add_f32 v22, v22, v110\n"
"v_mfma_f32_16x16x32_bf16 a[128:131], a[240:243], v[180:183], a[128:131]\n"
"v_exp_f32 v116, v116\n"
"v_mfma_f32_16x16x32_bf16 a[132:135], a[240:243], v[188:191], a[132:135]\n"
"s_or_b32 s72, s72, 24576\n"
"v_add_f32 v22, v22, v111\n"
"v_cvt_pk_bf16_f32 v223, v110, v111\n"
"v_mfma_f32_16x16x32_bf16 a[136:139], a[240:243], v[196:199], a[136:139]\n"
"v_exp_f32 v117, v117\n"
"v_mfma_f32_16x16x32_bf16 a[140:143], a[244:247], v[148:151], a[140:143]\n"
"s_xor_b32 s41, s40, s73\n"
"ds_read_b128 a[228:231], v8 offset:256\n"
"v_add_f32 v24, v24, v116\n"
"v_mfma_f32_16x16x32_bf16 a[144:147], a[244:247], v[156:159], a[144:147]\n"
"v_exp_f32 v118, v118\n"
"v_mfma_f32_16x16x32_bf16 a[148:151], a[244:247], v[164:167], a[148:151]\n"
"s_add_u32 s39, s39, 130\n"
"buffer_load_dword v17, v1, s[20:23], s60 offen sc1\n"
"v_add_f32 v24, v24, v117\n"
"v_cvt_pk_bf16_f32 v230, v116, v117\n"
"v_mfma_f32_16x16x32_bf16 a[152:155], a[244:247], v[172:175], a[152:155]\n"
"v_exp_f32 v119, v119\n"
"v_mfma_f32_16x16x32_bf16 a[156:159], a[244:247], v[180:183], a[156:159]\n"
"s_cmp_gt_u32 s66, 0\n"
"ds_read_b128 a[232:235], v8 offset:512\n"
"v_add_f32 v24, v24, v118\n"
"v_mfma_f32_16x16x32_bf16 a[160:163], a[244:247], v[188:191], a[160:163]\n"
"v_exp_f32 v124, v124\n"
"v_mfma_f32_16x16x32_bf16 a[164:167], a[244:247], v[196:199], a[164:167]\n"
"s_cbranch_scc1 120b\n"
"130:\n"
"v_add_f32 v24, v24, v119\n"
"v_cvt_pk_bf16_f32 v231, v118, v119\n"
"v_mfma_f32_16x16x32_bf16 a[168:171], a[248:251], v[148:151], a[168:171]\n"
"v_exp_f32 v125, v125\n"
"v_mfma_f32_16x16x32_bf16 a[172:175], a[248:251], v[156:159], a[172:175]\n"
"s_cmp_gt_u32 s39, s78\n"
"s_cselect_b64 vcc, -1, 0\n"
"s_cbranch_vccnz 564b\n"
"574:\n"
"ds_read_b128 a[236:239], v8 offset:768\n"
"v_add_f32 v26, v26, v124\n"
"v_mfma_f32_16x16x32_bf16 a[176:179], a[248:251], v[164:167], a[176:179]\n"
"v_exp_f32 v126, v126\n"
"v_mfma_f32_16x16x32_bf16 a[180:183], a[248:251], v[172:175], a[180:183]\n"
"v_add_f32 v26, v26, v125\n"
"v_cvt_pk_bf16_f32 v238, v124, v125\n"
"v_mfma_f32_16x16x32_bf16 a[184:187], a[248:251], v[180:183], a[184:187]\n"
"v_exp_f32 v127, v127\n"
"v_mfma_f32_16x16x32_bf16 a[188:191], a[248:251], v[188:191], a[188:191]\n"
"ds_read_b64_tr_b16 a[240:241], v9 offset:32768\n"
"v_add_f32 v26, v26, v126\n"
"v_mfma_f32_16x16x32_bf16 a[192:195], a[248:251], v[196:199], a[192:195]\n"
"v_exp_f32 v132, v132\n"
"v_mfma_f32_16x16x32_bf16 a[196:199], a[252:255], v[148:151], a[196:199]\n"
"v_add_f32 v26, v26, v127\n"
"v_cvt_pk_bf16_f32 v239, v126, v127\n"
"v_mfma_f32_16x16x32_bf16 a[200:203], a[252:255], v[156:159], a[200:203]\n"
"v_exp_f32 v133, v133\n"
"v_mfma_f32_16x16x32_bf16 a[204:207], a[252:255], v[164:167], a[204:207]\n"
"ds_read_b64_tr_b16 a[242:243], v9 offset:36864\n"
"v_add_f32 v28, v28, v132\n"
"v_mfma_f32_16x16x32_bf16 a[208:211], a[252:255], v[172:175], a[208:211]\n"
"v_exp_f32 v134, v134\n"
"v_mfma_f32_16x16x32_bf16 a[212:215], a[252:255], v[180:183], a[212:215]\n"
"s_cmp_gt_u32 s66, 1\n"
"v_add_f32 v28, v28, v133\n"
"v_cvt_pk_bf16_f32 v246, v132, v133\n"
"v_mfma_f32_16x16x32_bf16 a[216:219], a[252:255], v[188:191], a[216:219]\n"
"s_waitcnt lgkmcnt(5)\n"
"v_exp_f32 v135, v135\n"
"v_mfma_f32_16x16x32_bf16 a[220:223], a[252:255], v[196:199], a[220:223]\n"
"s_cbranch_scc1 121b\n"
"131:\n"
"v_add_f32 v28, v28, v134\n"
"v_mfma_f32_16x16x32_bf16 v[36:39], a[224:227], v[32:35], 0\n"
"s_waitcnt lgkmcnt(3)\n"
"v_exp_f32 v140, v140\n"
"v_mfma_f32_16x16x32_bf16 v[36:39], a[228:231], v[88:91], v[36:39]\n"
"s_cbranch_vccnz 580b\n"
"590:\n"
"v_add_f32 v28, v28, v135\n"
"v_cvt_pk_bf16_f32 v247, v134, v135\n"
"v_lshlrev_b32 v19, 3, v1\n"
"v_mfma_f32_16x16x32_bf16 v[36:39], a[232:235], v[144:147], v[36:39]\n"
"s_waitcnt lgkmcnt(2)\n"
"v_exp_f32 v141, v141\n"
"v_mfma_f32_16x16x32_bf16 v[36:39], a[236:239], v[200:203], v[36:39]\n"
"ds_read_b64_tr_b16 a[244:245], v10 offset:32768\n"
"v_add_f32 v30, v30, v140\n"
"v_mfma_f32_16x16x32_bf16 v[44:47], a[224:227], v[40:43], 0\n"
"v_exp_f32 v142, v142\n"
"v_mfma_f32_16x16x32_bf16 v[44:47], a[228:231], v[96:99], v[44:47]\n"
"v_add_f32 v30, v30, v141\n"
"v_cvt_pk_bf16_f32 v254, v140, v141\n"
"v_mfma_f32_16x16x32_bf16 v[44:47], a[232:235], v[152:155], v[44:47]\n"
"v_exp_f32 v143, v143\n"
"v_mfma_f32_16x16x32_bf16 v[44:47], a[236:239], v[208:211], v[44:47]\n"
"ds_read_b64_tr_b16 a[246:247], v10 offset:36864\n"
"v_add_f32 v30, v30, v142\n"
"v_mfma_f32_16x16x32_bf16 v[52:55], a[224:227], v[48:51], 0\n"
"v_exp_f32 v36, v36\n"
"v_mfma_f32_16x16x32_bf16 v[52:55], a[228:231], v[104:107], v[52:55]\n"
"v_add_f32 v30, v30, v143\n"
"v_cvt_pk_bf16_f32 v255, v142, v143\n"
"v_mfma_f32_16x16x32_bf16 v[52:55], a[232:235], v[160:163], v[52:55]\n"
"v_exp_f32 v37, v37\n"
"v_mfma_f32_16x16x32_bf16 v[52:55], a[236:239], v[216:219], v[52:55]\n"
"s_cmp_gt_u32 s66, 2\n"
"ds_read_b64_tr_b16 a[248:249], v9 offset:33024\n"
"v_add_f32 v18, v18, v36\n"
"v_mfma_f32_16x16x32_bf16 v[60:63], a[224:227], v[56:59], 0\n"
"v_exp_f32 v38, v38\n"
"v_mfma_f32_16x16x32_bf16 v[60:63], a[228:231], v[112:115], v[60:63]\n"
"s_cbranch_scc1 122b\n"
"132:\n"
"v_add_f32 v18, v18, v37\n"
"v_cvt_pk_bf16_f32 v148, v36, v37\n"
"v_mfma_f32_16x16x32_bf16 v[60:63], a[232:235], v[168:171], v[60:63]\n"
"v_exp_f32 v39, v39\n"
"v_mfma_f32_16x16x32_bf16 v[60:63], a[236:239], v[224:227], v[60:63]\n"
"ds_read_b64_tr_b16 a[250:251], v9 offset:37120\n"
"v_add_f32 v18, v18, v38\n"
"v_mfma_f32_16x16x32_bf16 v[68:71], a[224:227], v[64:67], 0\n"
"v_exp_f32 v44, v44\n"
"v_mfma_f32_16x16x32_bf16 v[68:71], a[228:231], v[120:123], v[68:71]\n"
"v_add_f32 v18, v18, v39\n"
"v_cvt_pk_bf16_f32 v149, v38, v39\n"
"v_mfma_f32_16x16x32_bf16 v[68:71], a[232:235], v[176:179], v[68:71]\n"
"v_exp_f32 v45, v45\n"
"v_mfma_f32_16x16x32_bf16 v[68:71], a[236:239], v[232:235], v[68:71]\n"
"ds_read_b64_tr_b16 a[252:253], v10 offset:33024\n"
"v_add_f32 v20, v20, v44\n"
"v_mfma_f32_16x16x32_bf16 v[76:79], a[224:227], v[72:75], 0\n"
"v_exp_f32 v46, v46\n"
"v_mfma_f32_16x16x32_bf16 v[76:79], a[228:231], v[128:131], v[76:79]\n"
"v_add_f32 v20, v20, v45\n"
"v_cvt_pk_bf16_f32 v156, v44, v45\n"
"v_mfma_f32_16x16x32_bf16 v[76:79], a[232:235], v[184:187], v[76:79]\n"
"v_exp_f32 v47, v47\n"
"v_mfma_f32_16x16x32_bf16 v[76:79], a[236:239], v[240:243], v[76:79]\n"
"ds_read_b64_tr_b16 a[254:255], v10 offset:37120\n"
"v_add_f32 v20, v20, v46\n"
"v_mfma_f32_16x16x32_bf16 v[84:87], a[224:227], v[80:83], 0\n"
"v_exp_f32 v52, v52\n"
"v_mfma_f32_16x16x32_bf16 v[84:87], a[228:231], v[136:139], v[84:87]\n"
"s_cmp_gt_u32 s66, 3\n"
"v_add_f32 v20, v20, v47\n"
"v_cvt_pk_bf16_f32 v157, v46, v47\n"
"v_mfma_f32_16x16x32_bf16 v[84:87], a[232:235], v[192:195], v[84:87]\n"
"s_waitcnt lgkmcnt(6)\n"
"v_exp_f32 v53, v53\n"
"v_mfma_f32_16x16x32_bf16 v[84:87], a[236:239], v[248:251], v[84:87]\n"
"s_cbranch_scc1 123b\n"
"133:\n"
"v_add_f32 v22, v22, v52\n"
"v_mfma_f32_16x16x32_bf16 a[0:3], a[240:243], v[204:207], a[0:3]\n"
"v_exp_f32 v54, v54\n"
"v_mfma_f32_16x16x32_bf16 a[4:7], a[240:243], v[212:215], a[4:7]\n"
"s_cbranch_vccnz 581b\n"
"591:\n"
"v_add_f32 v22, v22, v53\n"
"v_cvt_pk_bf16_f32 v164, v52, v53\n"
"v_mfma_f32_16x16x32_bf16 a[8:11], a[240:243], v[220:223], a[8:11]\n"
"v_exp_f32 v55, v55\n"
"v_mfma_f32_16x16x32_bf16 a[12:15], a[240:243], v[228:231], a[12:15]\n"
"ds_read_b128 a[224:227], v8 offset:4096\n"
"v_add_f32 v22, v22, v54\n"
"v_mfma_f32_16x16x32_bf16 a[16:19], a[240:243], v[236:239], a[16:19]\n"
"v_exp_f32 v60, v60\n"
"v_mfma_f32_16x16x32_bf16 a[20:23], a[240:243], v[244:247], a[20:23]\n"
"v_add_f32 v22, v22, v55\n"
"v_cvt_pk_bf16_f32 v165, v54, v55\n"
"v_mfma_f32_16x16x32_bf16 a[24:27], a[240:243], v[252:255], a[24:27]\n"
"s_waitcnt lgkmcnt(5)\n"
"v_exp_f32 v61, v61\n"
"v_mfma_f32_16x16x32_bf16 a[28:31], a[244:247], v[204:207], a[28:31]\n"
"ds_read_b128 a[228:231], v8 offset:4352\n"
"v_add_f32 v24, v24, v60\n"
"v_mfma_f32_16x16x32_bf16 a[32:35], a[244:247], v[212:215], a[32:35]\n"
"v_exp_f32 v62, v62\n"
"v_mfma_f32_16x16x32_bf16 a[36:39], a[244:247], v[220:223], a[36:39]\n"
"v_add_f32 v24, v24, v61\n"
"v_cvt_pk_bf16_f32 v172, v60, v61\n"
"v_mfma_f32_16x16x32_bf16 a[40:43], a[244:247], v[228:231], a[40:43]\n"
"s_add_u32 s67, s49, 0\n"
"v_exp_f32 v63, v63\n"
"v_mfma_f32_16x16x32_bf16 a[44:47], a[244:247], v[236:239], a[44:47]\n"
"ds_read_b128 a[232:235], v8 offset:4608\n"
"v_add_f32 v24, v24, v62\n"
"v_mfma_f32_16x16x32_bf16 a[48:51], a[244:247], v[244:247], a[48:51]\n"
"s_waitcnt lgkmcnt(5)\n"
"v_exp_f32 v68, v68\n"
"v_mfma_f32_16x16x32_bf16 a[52:55], a[244:247], v[252:255], a[52:55]\n"
"buffer_load_dword v17, v19, s[24:27], s67 offen\n"
"v_add_f32 v24, v24, v63\n"
"v_cvt_pk_bf16_f32 v173, v62, v63\n"
"v_mfma_f32_16x16x32_bf16 a[56:59], a[248:251], v[204:207], a[56:59]\n"
"v_exp_f32 v69, v69\n"
"v_mfma_f32_16x16x32_bf16 a[60:63], a[248:251], v[212:215], a[60:63]\n"
"ds_read_b128 a[236:239], v8 offset:4864\n"
"v_add_f32 v26, v26, v68\n"
"v_mfma_f32_16x16x32_bf16 a[64:67], a[248:251], v[220:223], a[64:67]\n"
"v_exp_f32 v70, v70\n"
"v_mfma_f32_16x16x32_bf16 a[68:71], a[248:251], v[228:231], a[68:71]\n"
"v_add_f32 v26, v26, v69\n"
"v_cvt_pk_bf16_f32 v180, v68, v69\n"
"v_mfma_f32_16x16x32_bf16 a[72:75], a[248:251], v[236:239], a[72:75]\n"
"v_exp_f32 v71, v71\n"
"v_mfma_f32_16x16x32_bf16 a[76:79], a[248:251], v[244:247], a[76:79]\n"
"ds_read_b64_tr_b16 a[240:241], v9 offset:33280\n"
"v_add_f32 v26, v26, v70\n"
"v_mfma_f32_16x16x32_bf16 a[80:83], a[248:251], v[252:255], a[80:83]\n"
"s_waitcnt lgkmcnt(5)\n"
"v_exp_f32 v76, v76\n"
"v_mfma_f32_16x16x32_bf16 a[84:87], a[252:255], v[204:207], a[84:87]\n"
"v_add_f32 v26, v26, v71\n"
"v_cvt_pk_bf16_f32 v181, v70, v71\n"
"v_mfma_f32_16x16x32_bf16 a[88:91], a[252:255], v[212:215], a[88:91]\n"
"v_exp_f32 v77, v77\n"
"v_mfma_f32_16x16x32_bf16 a[92:95], a[252:255], v[220:223], a[92:95]\n"
"ds_read_b64_tr_b16 a[242:243], v9 offset:37376\n"
"v_add_f32 v28, v28, v76\n"
"v_mfma_f32_16x16x32_bf16 a[96:99], a[252:255], v[228:231], a[96:99]\n"
"v_exp_f32 v78, v78\n"
"v_mfma_f32_16x16x32_bf16 a[100:103], a[252:255], v[236:239], a[100:103]\n"
"v_add_f32 v28, v28, v77\n"
"v_cvt_pk_bf16_f32 v188, v76, v77\n"
"v_mfma_f32_16x16x32_bf16 a[104:107], a[252:255], v[244:247], a[104:107]\n"
"s_waitcnt lgkmcnt(5)\n"
"v_exp_f32 v79, v79\n"
"v_mfma_f32_16x16x32_bf16 a[108:111], a[252:255], v[252:255], a[108:111]\n"
"v_add_f32 v28, v28, v78\n"
"v_mfma_f32_16x16x32_bf16 v[92:95], a[224:227], v[32:35], 0\n"
"s_waitcnt lgkmcnt(3)\n"
"v_exp_f32 v84, v84\n"
"v_mfma_f32_16x16x32_bf16 v[92:95], a[228:231], v[88:91], v[92:95]\n"
"v_add_f32 v28, v28, v79\n"
"v_cvt_pk_bf16_f32 v189, v78, v79\n"
"v_mfma_f32_16x16x32_bf16 v[92:95], a[232:235], v[144:147], v[92:95]\n"
"s_waitcnt lgkmcnt(2)\n"
"v_exp_f32 v85, v85\n"
"v_mfma_f32_16x16x32_bf16 v[92:95], a[236:239], v[200:203], v[92:95]\n"
"ds_read_b64_tr_b16 a[244:245], v10 offset:33280\n"
"v_add_f32 v30, v30, v84\n"
"v_mfma_f32_16x16x32_bf16 v[100:103], a[224:227], v[40:43], 0\n"
"v_exp_f32 v86, v86\n"
"v_mfma_f32_16x16x32_bf16 v[100:103], a[228:231], v[96:99], v[100:103]\n"
"v_add_f32 v30, v30, v85\n"
"v_cvt_pk_bf16_f32 v196, v84, v85\n"
"v_mfma_f32_16x16x32_bf16 v[100:103], a[232:235], v[152:155], v[100:103]\n"
"v_exp_f32 v87, v87\n"
"v_mfma_f32_16x16x32_bf16 v[100:103], a[236:239], v[208:211], v[100:103]\n"
"ds_read_b64_tr_b16 a[246:247], v10 offset:37376\n"
"v_add_f32 v30, v30, v86\n"
"v_mfma_f32_16x16x32_bf16 v[108:111], a[224:227], v[48:51], 0\n"
"v_exp_f32 v92, v92\n"
"v_mfma_f32_16x16x32_bf16 v[108:111], a[228:231], v[104:107], v[108:111]\n"
"v_add_f32 v30, v30, v87\n"
"v_cvt_pk_bf16_f32 v197, v86, v87\n"
"v_mfma_f32_16x16x32_bf16 v[108:111], a[232:235], v[160:163], v[108:111]\n"
"v_exp_f32 v93, v93\n"
"v_mfma_f32_16x16x32_bf16 v[108:111], a[236:239], v[216:219], v[108:111]\n"
"ds_read_b64_tr_b16 a[248:249], v9 offset:33536\n"
"v_add_f32 v18, v18, v92\n"
"v_mfma_f32_16x16x32_bf16 v[116:119], a[224:227], v[56:59], 0\n"
"v_exp_f32 v94, v94\n"
"v_mfma_f32_16x16x32_bf16 v[116:119], a[228:231], v[112:115], v[116:119]\n"
"v_add_f32 v18, v18, v93\n"
"v_cvt_pk_bf16_f32 v150, v92, v93\n"
"v_mfma_f32_16x16x32_bf16 v[116:119], a[232:235], v[168:171], v[116:119]\n"
"v_exp_f32 v95, v95\n"
"v_mfma_f32_16x16x32_bf16 v[116:119], a[236:239], v[224:227], v[116:119]\n"
"ds_read_b64_tr_b16 a[250:251], v9 offset:37632\n"
"v_add_f32 v18, v18, v94\n"
"v_mfma_f32_16x16x32_bf16 v[124:127], a[224:227], v[64:67], 0\n"
"v_exp_f32 v100, v100\n"
"v_mfma_f32_16x16x32_bf16 v[124:127], a[228:231], v[120:123], v[124:127]\n"
"v_add_f32 v18, v18, v95\n"
"v_cvt_pk_bf16_f32 v151, v94, v95\n"
"v_mfma_f32_16x16x32_bf16 v[124:127], a[232:235], v[176:179], v[124:127]\n"
"v_exp_f32 v101, v101\n"
"v_mfma_f32_16x16x32_bf16 v[124:127], a[236:239], v[232:235], v[124:127]\n"
"ds_read_b64_tr_b16 a[252:253], v10 offset:33536\n"
"v_add_f32 v20, v20, v100\n"
"v_mfma_f32_16x16x32_bf16 v[132:135], a[224:227], v[72:75], 0\n"
"v_exp_f32 v102, v102\n"
"v_mfma_f32_16x16x32_bf16 v[132:135], a[228:231], v[128:131], v[132:135]\n"
"v_add_f32 v20, v20, v101\n"
"v_cvt_pk_bf16_f32 v158, v100, v101\n"
"v_mfma_f32_16x16x32_bf16 v[132:135], a[232:235], v[184:187], v[132:135]\n"
"v_exp_f32 v103, v103\n"
"v_mfma_f32_16x16x32_bf16 v[132:135], a[236:239], v[240:243], v[132:135]\n"
"ds_read_b64_tr_b16 a[254:255], v10 offset:37632\n"
"v_add_f32 v20, v20, v102\n"
"v_xor_b32 v9, 65536, v9\n"
"v_mfma_f32_16x16x32_bf16 v[140:143], a[224:227], v[80:83], 0\n"
"v_exp_f32 v108, v108\n"
"v_mfma_f32_16x16x32_bf16 v[140:143], a[228:231], v[136:139], v[140:143]\n"
"v_add_f32 v20, v20, v103\n"
"v_cvt_pk_bf16_f32 v159, v102, v103\n"
"v_mfma_f32_16x16x32_bf16 v[140:143], a[232:235], v[192:195], v[140:143]\n"
"s_waitcnt lgkmcnt(6)\n"
"v_exp_f32 v109, v109\n"
"v_mfma_f32_16x16x32_bf16 v[140:143], a[236:239], v[248:251], v[140:143]\n"
"v_add_f32 v22, v22, v108\n"
"v_xor_b32 v10, 65536, v10\n"
"v_mfma_f32_16x16x32_bf16 a[112:115], a[240:243], v[204:207], a[112:115]\n"
"v_exp_f32 v110, v110\n"
"v_mfma_f32_16x16x32_bf16 a[116:119], a[240:243], v[212:215], a[116:119]\n"
"v_add_f32 v22, v22, v109\n"
"v_cvt_pk_bf16_f32 v166, v108, v109\n"
"v_mfma_f32_16x16x32_bf16 a[120:123], a[240:243], v[220:223], a[120:123]\n"
"s_waitcnt vmcnt(10) lgkmcnt(0)\n"
"s_barrier\n"
"v_exp_f32 v111, v111\n"
"v_mfma_f32_16x16x32_bf16 a[124:127], a[240:243], v[228:231], a[124:127]\n"
"ds_read_b128 a[224:227], v8 offset:32768\n"
"v_add_f32 v22, v22, v110\n"
"v_mfma_f32_16x16x32_bf16 a[128:131], a[240:243], v[236:239], a[128:131]\n"
"v_exp_f32 v116, v116\n"
"v_mfma_f32_16x16x32_bf16 a[132:135], a[240:243], v[244:247], a[132:135]\n"
"v_add_f32 v22, v22, v111\n"
"v_cvt_pk_bf16_f32 v167, v110, v111\n"
"v_mfma_f32_16x16x32_bf16 a[136:139], a[240:243], v[252:255], a[136:139]\n"
"v_exp_f32 v117, v117\n"
"v_mfma_f32_16x16x32_bf16 a[140:143], a[244:247], v[204:207], a[140:143]\n"
"ds_read_b128 a[228:231], v8 offset:33024\n"
"v_add_f32 v24, v24, v116\n"
"v_mfma_f32_16x16x32_bf16 a[144:147], a[244:247], v[212:215], a[144:147]\n"
"v_exp_f32 v118, v118\n"
"v_mfma_f32_16x16x32_bf16 a[148:151], a[244:247], v[220:223], a[148:151]\n"
"v_add_f32 v24, v24, v117\n"
"v_cvt_pk_bf16_f32 v174, v116, v117\n"
"v_mfma_f32_16x16x32_bf16 a[152:155], a[244:247], v[228:231], a[152:155]\n"
"v_exp_f32 v119, v119\n"
"v_mfma_f32_16x16x32_bf16 a[156:159], a[244:247], v[236:239], a[156:159]\n"
"ds_read_b128 a[232:235], v8 offset:33280\n"
"v_add_f32 v24, v24, v118\n"
"v_mfma_f32_16x16x32_bf16 a[160:163], a[244:247], v[244:247], a[160:163]\n"
"v_exp_f32 v124, v124\n"
"v_mfma_f32_16x16x32_bf16 a[164:167], a[244:247], v[252:255], a[164:167]\n"
"v_add_f32 v24, v24, v119\n"
"v_cvt_pk_bf16_f32 v175, v118, v119\n"
"v_mfma_f32_16x16x32_bf16 a[168:171], a[248:251], v[204:207], a[168:171]\n"
"v_exp_f32 v125, v125\n"
"v_mfma_f32_16x16x32_bf16 a[172:175], a[248:251], v[212:215], a[172:175]\n"
"ds_read_b128 a[236:239], v8 offset:33536\n"
"v_add_f32 v26, v26, v124\n"
"v_mfma_f32_16x16x32_bf16 a[176:179], a[248:251], v[220:223], a[176:179]\n"
"v_exp_f32 v126, v126\n"
"v_mfma_f32_16x16x32_bf16 a[180:183], a[248:251], v[228:231], a[180:183]\n"
"v_add_f32 v26, v26, v125\n"
"v_cvt_pk_bf16_f32 v182, v124, v125\n"
"v_mfma_f32_16x16x32_bf16 a[184:187], a[248:251], v[236:239], a[184:187]\n"
"v_exp_f32 v127, v127\n"
"v_mfma_f32_16x16x32_bf16 a[188:191], a[248:251], v[244:247], a[188:191]\n"
"ds_read_b64_tr_b16 a[240:241], v9 offset:0\n"
"v_add_f32 v26, v26, v126\n"
"v_mfma_f32_16x16x32_bf16 a[192:195], a[248:251], v[252:255], a[192:195]\n"
"v_exp_f32 v132, v132\n"
"v_mfma_f32_16x16x32_bf16 a[196:199], a[252:255], v[204:207], a[196:199]\n"
"v_add_f32 v26, v26, v127\n"
"v_cvt_pk_bf16_f32 v183, v126, v127\n"
"v_mfma_f32_16x16x32_bf16 a[200:203], a[252:255], v[212:215], a[200:203]\n"
"v_exp_f32 v133, v133\n"
"v_mfma_f32_16x16x32_bf16 a[204:207], a[252:255], v[220:223], a[204:207]\n"
"ds_read_b64_tr_b16 a[242:243], v9 offset:4096\n"
"v_add_f32 v28, v28, v132\n"
"v_mfma_f32_16x16x32_bf16 a[208:211], a[252:255], v[228:231], a[208:211]\n"
"v_exp_f32 v134, v134\n"
"v_mfma_f32_16x16x32_bf16 a[212:215], a[252:255], v[236:239], a[212:215]\n"
"v_add_f32 v28, v28, v133\n"
"v_cvt_pk_bf16_f32 v190, v132, v133\n"
"v_mfma_f32_16x16x32_bf16 a[216:219], a[252:255], v[244:247], a[216:219]\n"
"s_waitcnt lgkmcnt(5)\n"
"v_exp_f32 v135, v135\n"
"v_mfma_f32_16x16x32_bf16 a[220:223], a[252:255], v[252:255], a[220:223]\n"
"v_add_f32 v28, v28, v134\n"
"v_mfma_f32_16x16x32_bf16 v[36:39], a[224:227], v[32:35], 0\n"
"s_waitcnt lgkmcnt(3)\n"
"v_exp_f32 v140, v140\n"
"v_mfma_f32_16x16x32_bf16 v[36:39], a[228:231], v[88:91], v[36:39]\n"
"v_add_f32 v28, v28, v135\n"
"v_cvt_pk_bf16_f32 v191, v134, v135\n"
"v_mfma_f32_16x16x32_bf16 v[36:39], a[232:235], v[144:147], v[36:39]\n"
"s_waitcnt lgkmcnt(2)\n"
"v_exp_f32 v141, v141\n"
"v_mfma_f32_16x16x32_bf16 v[36:39], a[236:239], v[200:203], v[36:39]\n"
"ds_read_b64_tr_b16 a[244:245], v10 offset:0\n"
"v_add_f32 v30, v30, v140\n"
"v_mfma_f32_16x16x32_bf16 v[44:47], a[224:227], v[40:43], 0\n"
"v_exp_f32 v142, v142\n"
"v_mfma_f32_16x16x32_bf16 v[44:47], a[228:231], v[96:99], v[44:47]\n"
"v_add_f32 v30, v30, v141\n"
"v_cvt_pk_bf16_f32 v198, v140, v141\n"
"v_mfma_f32_16x16x32_bf16 v[44:47], a[232:235], v[152:155], v[44:47]\n"
"s_xor_b32 s76, s48, 0\n"
"v_exp_f32 v143, v143\n"
"v_mfma_f32_16x16x32_bf16 v[44:47], a[236:239], v[208:211], v[44:47]\n"
"ds_read_b64_tr_b16 a[246:247], v10 offset:4096\n"
"v_add_f32 v30, v30, v142\n"
"v_mfma_f32_16x16x32_bf16 v[52:55], a[224:227], v[48:51], 0\n"
"s_add_u32 m0, s46, 131072\n"
"v_exp_f32 v36, v36\n"
"v_mfma_f32_16x16x32_bf16 v[52:55], a[228:231], v[104:107], v[52:55]\n"
"buffer_load_dwordx4 v14, s[24:27], s76 offen lds\n"
"v_add_f32 v30, v30, v143\n"
"v_cvt_pk_bf16_f32 v199, v142, v143\n"
"v_mfma_f32_16x16x32_bf16 v[52:55], a[232:235], v[160:163], v[52:55]\n"
"v_exp_f32 v37, v37\n"
"v_mfma_f32_16x16x32_bf16 v[52:55], a[236:239], v[216:219], v[52:55]\n"
"ds_read_b64_tr_b16 a[248:249], v9 offset:256\n"
"v_add_f32 v18, v18, v36\n"
"v_mfma_f32_16x16x32_bf16 v[60:63], a[224:227], v[56:59], 0\n"
"v_exp_f32 v38, v38\n"
"v_mfma_f32_16x16x32_bf16 v[60:63], a[228:231], v[112:115], v[60:63]\n"
"buffer_load_dwordx4 v15, s[24:27], s76 offen offset:1024 lds\n"
"v_add_f32 v18, v18, v37\n"
"v_cvt_pk_bf16_f32 v204, v36, v37\n"
"v_mfma_f32_16x16x32_bf16 v[60:63], a[232:235], v[168:171], v[60:63]\n"
"v_exp_f32 v39, v39\n"
"v_mfma_f32_16x16x32_bf16 v[60:63], a[236:239], v[224:227], v[60:63]\n"
"ds_read_b64_tr_b16 a[250:251], v9 offset:4352\n"
"v_add_f32 v18, v18, v38\n"
"v_mfma_f32_16x16x32_bf16 v[68:71], a[224:227], v[64:67], 0\n"
"v_exp_f32 v44, v44\n"
"v_mfma_f32_16x16x32_bf16 v[68:71], a[228:231], v[120:123], v[68:71]\n"
"buffer_load_dwordx4 v21, s[24:27], s76 offen offset:2048 lds\n"
"v_add_f32 v18, v18, v39\n"
"v_cvt_pk_bf16_f32 v205, v38, v39\n"
"v_mfma_f32_16x16x32_bf16 v[68:71], a[232:235], v[176:179], v[68:71]\n"
"v_exp_f32 v45, v45\n"
"v_mfma_f32_16x16x32_bf16 v[68:71], a[236:239], v[232:235], v[68:71]\n"
"ds_read_b64_tr_b16 a[252:253], v10 offset:256\n"
"v_add_f32 v20, v20, v44\n"
"v_mfma_f32_16x16x32_bf16 v[76:79], a[224:227], v[72:75], 0\n"
"v_exp_f32 v46, v46\n"
"v_mfma_f32_16x16x32_bf16 v[76:79], a[228:231], v[128:131], v[76:79]\n"
"buffer_load_dwordx4 v23, s[24:27], s76 offen offset:3072 lds\n"
"v_add_f32 v20, v20, v45\n"
"v_cvt_pk_bf16_f32 v212, v44, v45\n"
"v_mfma_f32_16x16x32_bf16 v[76:79], a[232:235], v[184:187], v[76:79]\n"
"s_xor_b32 s76, s48, 32768\n"
"v_exp_f32 v47, v47\n"
"v_mfma_f32_16x16x32_bf16 v[76:79], a[236:239], v[240:243], v[76:79]\n"
"ds_read_b64_tr_b16 a[254:255], v10 offset:4352\n"
"v_add_f32 v20, v20, v46\n"
"v_mfma_f32_16x16x32_bf16 v[84:87], a[224:227], v[80:83], 0\n"
"s_add_u32 m0, s46, 135168\n"
"v_exp_f32 v52, v52\n"
"v_mfma_f32_16x16x32_bf16 v[84:87], a[228:231], v[136:139], v[84:87]\n"
"buffer_load_dwordx4 v14, s[24:27], s76 offen lds\n"
"v_add_f32 v20, v20, v47\n"
"v_cvt_pk_bf16_f32 v213, v46, v47\n"
"v_mfma_f32_16x16x32_bf16 v[84:87], a[232:235], v[192:195], v[84:87]\n"
"s_waitcnt lgkmcnt(6)\n"
"v_exp_f32 v53, v53\n"
"v_mfma_f32_16x16x32_bf16 v[84:87], a[236:239], v[248:251], v[84:87]\n"
"v_add_f32 v22, v22, v52\n"
"v_mfma_f32_16x16x32_bf16 a[0:3], a[240:243], v[148:151], a[0:3]\n"
"v_exp_f32 v54, v54\n"
"v_mfma_f32_16x16x32_bf16 a[4:7], a[240:243], v[156:159], a[4:7]\n"
"buffer_load_dwordx4 v15, s[24:27], s76 offen offset:1024 lds\n"
"v_add_f32 v22, v22, v53\n"
"v_cvt_pk_bf16_f32 v220, v52, v53\n"
"v_mfma_f32_16x16x32_bf16 a[8:11], a[240:243], v[164:167], a[8:11]\n"
"v_exp_f32 v55, v55\n"
"v_mfma_f32_16x16x32_bf16 a[12:15], a[240:243], v[172:175], a[12:15]\n"
"ds_read_b128 a[224:227], v8 offset:36864\n"
"v_add_f32 v22, v22, v54\n"
"v_mfma_f32_16x16x32_bf16 a[16:19], a[240:243], v[180:183], a[16:19]\n"
"v_exp_f32 v60, v60\n"
"v_mfma_f32_16x16x32_bf16 a[20:23], a[240:243], v[188:191], a[20:23]\n"
"buffer_load_dwordx4 v21, s[24:27], s76 offen offset:2048 lds\n"
"v_add_f32 v22, v22, v55\n"
"v_cvt_pk_bf16_f32 v221, v54, v55\n"
"v_mfma_f32_16x16x32_bf16 a[24:27], a[240:243], v[196:199], a[24:27]\n"
"s_waitcnt lgkmcnt(5)\n"
"v_exp_f32 v61, v61\n"
"v_mfma_f32_16x16x32_bf16 a[28:31], a[244:247], v[148:151], a[28:31]\n"
"ds_read_b128 a[228:231], v8 offset:37120\n"
"v_add_f32 v24, v24, v60\n"
"v_mfma_f32_16x16x32_bf16 a[32:35], a[244:247], v[156:159], a[32:35]\n"
"v_exp_f32 v62, v62\n"
"v_mfma_f32_16x16x32_bf16 a[36:39], a[244:247], v[164:167], a[36:39]\n"
"buffer_load_dwordx4 v23, s[24:27], s76 offen offset:3072 lds\n"
"v_add_f32 v24, v24, v61\n"
"v_cvt_pk_bf16_f32 v228, v60, v61\n"
"v_mfma_f32_16x16x32_bf16 a[40:43], a[244:247], v[172:175], a[40:43]\n"
"s_xor_b32 s76, s48, 256\n"
"v_exp_f32 v63, v63\n"
"v_mfma_f32_16x16x32_bf16 a[44:47], a[244:247], v[180:183], a[44:47]\n"
"ds_read_b128 a[232:235], v8 offset:37376\n"
"v_add_f32 v24, v24, v62\n"
"v_mfma_f32_16x16x32_bf16 a[48:51], a[244:247], v[188:191], a[48:51]\n"
"s_waitcnt lgkmcnt(5)\n"
"s_add_u32 m0, s46, 0\n"
"v_exp_f32 v68, v68\n"
"v_mfma_f32_16x16x32_bf16 a[52:55], a[244:247], v[196:199], a[52:55]\n"
"buffer_load_dwordx4 v14, s[24:27], s76 offen lds\n"
"v_add_f32 v24, v24, v63\n"
"v_cvt_pk_bf16_f32 v229, v62, v63\n"
"v_mfma_f32_16x16x32_bf16 a[56:59], a[248:251], v[148:151], a[56:59]\n"
"v_exp_f32 v69, v69\n"
"v_mfma_f32_16x16x32_bf16 a[60:63], a[248:251], v[156:159], a[60:63]\n"
"ds_read_b128 a[236:239], v8 offset:37632\n"
"v_add_f32 v26, v26, v68\n"
"v_mfma_f32_16x16x32_bf16 a[64:67], a[248:251], v[164:167], a[64:67]\n"
"v_exp_f32 v70, v70\n"
"v_mfma_f32_16x16x32_bf16 a[68:71], a[248:251], v[172:175], a[68:71]\n"
"buffer_load_dwordx4 v15, s[24:27], s76 offen offset:1024 lds\n"
"v_add_f32 v26, v26, v69\n"
"v_cvt_pk_bf16_f32 v236, v68, v69\n"
"v_mfma_f32_16x16x32_bf16 a[72:75], a[248:251], v[180:183], a[72:75]\n"
"v_exp_f32 v71, v71\n"
"v_mfma_f32_16x16x32_bf16 a[76:79], a[248:251], v[188:191], a[76:79]\n"
"ds_read_b64_tr_b16 a[240:241], v9 offset:512\n"
"v_add_f32 v26, v26, v70\n"
"v_mfma_f32_16x16x32_bf16 a[80:83], a[248:251], v[196:199], a[80:83]\n"
"s_waitcnt lgkmcnt(5)\n"
"v_exp_f32 v76, v76\n"
"v_mfma_f32_16x16x32_bf16 a[84:87], a[252:255], v[148:151], a[84:87]\n"
"buffer_load_dwordx4 v21, s[24:27], s76 offen offset:2048 lds\n"
"v_add_f32 v26, v26, v71\n"
"v_cvt_pk_bf16_f32 v237, v70, v71\n"
"v_mfma_f32_16x16x32_bf16 a[88:91], a[252:255], v[156:159], a[88:91]\n"
"v_exp_f32 v77, v77\n"
"v_mfma_f32_16x16x32_bf16 a[92:95], a[252:255], v[164:167], a[92:95]\n"
"ds_read_b64_tr_b16 a[242:243], v9 offset:4608\n"
"v_add_f32 v28, v28, v76\n"
"v_mfma_f32_16x16x32_bf16 a[96:99], a[252:255], v[172:175], a[96:99]\n"
"v_exp_f32 v78, v78\n"
"v_mfma_f32_16x16x32_bf16 a[100:103], a[252:255], v[180:183], a[100:103]\n"
"buffer_load_dwordx4 v23, s[24:27], s76 offen offset:3072 lds\n"
"v_add_f32 v28, v28, v77\n"
"v_cvt_pk_bf16_f32 v244, v76, v77\n"
"v_mfma_f32_16x16x32_bf16 a[104:107], a[252:255], v[188:191], a[104:107]\n"
"s_waitcnt lgkmcnt(5)\n"
"s_xor_b32 s76, s48, 33024\n"
"v_exp_f32 v79, v79\n"
"v_mfma_f32_16x16x32_bf16 a[108:111], a[252:255], v[196:199], a[108:111]\n"
"v_add_f32 v28, v28, v78\n"
"v_mfma_f32_16x16x32_bf16 v[92:95], a[224:227], v[32:35], 0\n"
"s_waitcnt lgkmcnt(3)\n"
"s_add_u32 m0, s46, 4096\n"
"v_exp_f32 v84, v84\n"
"v_mfma_f32_16x16x32_bf16 v[92:95], a[228:231], v[88:91], v[92:95]\n"
"buffer_load_dwordx4 v14, s[24:27], s76 offen lds\n"
"v_add_f32 v28, v28, v79\n"
"v_cvt_pk_bf16_f32 v245, v78, v79\n"
"v_mfma_f32_16x16x32_bf16 v[92:95], a[232:235], v[144:147], v[92:95]\n"
"s_waitcnt lgkmcnt(2)\n"
"v_exp_f32 v85, v85\n"
"v_mfma_f32_16x16x32_bf16 v[92:95], a[236:239], v[200:203], v[92:95]\n"
"ds_read_b64_tr_b16 a[244:245], v10 offset:512\n"
"v_add_f32 v30, v30, v84\n"
"v_mfma_f32_16x16x32_bf16 v[100:103], a[224:227], v[40:43], 0\n"
"v_exp_f32 v86, v86\n"
"v_mfma_f32_16x16x32_bf16 v[100:103], a[228:231], v[96:99], v[100:103]\n"
"buffer_load_dwordx4 v15, s[24:27], s76 offen offset:1024 lds\n"
"v_add_f32 v30, v30, v85\n"
"v_cvt_pk_bf16_f32 v252, v84, v85\n"
"v_mfma_f32_16x16x32_bf16 v[100:103], a[232:235], v[152:155], v[100:103]\n"
"v_exp_f32 v87, v87\n"
"v_mfma_f32_16x16x32_bf16 v[100:103], a[236:239], v[208:211], v[100:103]\n"
"ds_read_b64_tr_b16 a[246:247], v10 offset:4608\n"
"v_add_f32 v30, v30, v86\n"
"v_mfma_f32_16x16x32_bf16 v[108:111], a[224:227], v[48:51], 0\n"
"v_exp_f32 v92, v92\n"
"v_mfma_f32_16x16x32_bf16 v[108:111], a[228:231], v[104:107], v[108:111]\n"
"buffer_load_dwordx4 v21, s[24:27], s76 offen offset:2048 lds\n"
"v_add_f32 v30, v30, v87\n"
"v_cvt_pk_bf16_f32 v253, v86, v87\n"
"v_mfma_f32_16x16x32_bf16 v[108:111], a[232:235], v[160:163], v[108:111]\n"
"v_exp_f32 v93, v93\n"
"v_mfma_f32_16x16x32_bf16 v[108:111], a[236:239], v[216:219], v[108:111]\n"
"ds_read_b64_tr_b16 a[248:249], v9 offset:768\n"
"v_add_f32 v18, v18, v92\n"
"v_mfma_f32_16x16x32_bf16 v[116:119], a[224:227], v[56:59], 0\n"
"v_exp_f32 v94, v94\n"
"v_mfma_f32_16x16x32_bf16 v[116:119], a[228:231], v[112:115], v[116:119]\n"
"buffer_load_dwordx4 v23, s[24:27], s76 offen offset:3072 lds\n"
"v_add_f32 v18, v18, v93\n"
"v_cvt_pk_bf16_f32 v206, v92, v93\n"
"v_mfma_f32_16x16x32_bf16 v[116:119], a[232:235], v[168:171], v[116:119]\n"
"s_xor_b32 s76, s48, 512\n"
"v_exp_f32 v95, v95\n"
"v_mfma_f32_16x16x32_bf16 v[116:119], a[236:239], v[224:227], v[116:119]\n"
"ds_read_b64_tr_b16 a[250:251], v9 offset:4864\n"
"v_add_f32 v18, v18, v94\n"
"v_mfma_f32_16x16x32_bf16 v[124:127], a[224:227], v[64:67], 0\n"
"s_add_u32 m0, s46, 32768\n"
"v_exp_f32 v100, v100\n"
"v_mfma_f32_16x16x32_bf16 v[124:127], a[228:231], v[120:123], v[124:127]\n"
"buffer_load_dwordx4 v14, s[24:27], s76 offen lds\n"
"v_add_f32 v18, v18, v95\n"
"v_cvt_pk_bf16_f32 v207, v94, v95\n"
"v_mfma_f32_16x16x32_bf16 v[124:127], a[232:235], v[176:179], v[124:127]\n"
"v_exp_f32 v101, v101\n"
"v_mfma_f32_16x16x32_bf16 v[124:127], a[236:239], v[232:235], v[124:127]\n"
"ds_read_b64_tr_b16 a[252:253], v10 offset:768\n"
"v_add_f32 v20, v20, v100\n"
"v_mfma_f32_16x16x32_bf16 v[132:135], a[224:227], v[72:75], 0\n"
"v_exp_f32 v102, v102\n"
"v_mfma_f32_16x16x32_bf16 v[132:135], a[228:231], v[128:131], v[132:135]\n"
"buffer_load_dwordx4 v15, s[24:27], s76 offen offset:1024 lds\n"
"v_add_f32 v20, v20, v101\n"
"v_cvt_pk_bf16_f32 v214, v100, v101\n"
"v_mfma_f32_16x16x32_bf16 v[132:135], a[232:235], v[184:187], v[132:135]\n"
"v_exp_f32 v103, v103\n"
"v_mfma_f32_16x16x32_bf16 v[132:135], a[236:239], v[240:243], v[132:135]\n"
"ds_read_b64_tr_b16 a[254:255], v10 offset:4864\n"
"v_add_f32 v20, v20, v102\n"
"v_mfma_f32_16x16x32_bf16 v[140:143], a[224:227], v[80:83], 0\n"
"v_exp_f32 v108, v108\n"
"v_mfma_f32_16x16x32_bf16 v[140:143], a[228:231], v[136:139], v[140:143]\n"
"buffer_load_dwordx4 v21, s[24:27], s76 offen offset:2048 lds\n"
"v_add_f32 v20, v20, v103\n"
"v_cvt_pk_bf16_f32 v215, v102, v103\n"
"v_mfma_f32_16x16x32_bf16 v[140:143], a[232:235], v[192:195], v[140:143]\n"
"s_waitcnt lgkmcnt(6)\n"
"v_exp_f32 v109, v109\n"
"v_mfma_f32_16x16x32_bf16 v[140:143], a[236:239], v[248:251], v[140:143]\n"
"v_add_f32 v22, v22, v108\n"
"v_mfma_f32_16x16x32_bf16 a[112:115], a[240:243], v[148:151], a[112:115]\n"
"v_exp_f32 v110, v110\n"
"v_mfma_f32_16x16x32_bf16 a[116:119], a[240:243], v[156:159], a[116:119]\n"
"buffer_load_dwordx4 v23, s[24:27], s76 offen offset:3072 lds\n"
"v_add_f32 v22, v22, v109\n"
"v_cvt_pk_bf16_f32 v222, v108, v109\n"
"v_mfma_f32_16x16x32_bf16 a[120:123], a[240:243], v[164:167], a[120:123]\n"
"s_waitcnt vmcnt(21) lgkmcnt(0)\n"
"s_barrier\n"
"s_xor_b32 s76, s48, 33280\n"
"v_exp_f32 v111, v111\n"
"v_mfma_f32_16x16x32_bf16 a[124:127], a[240:243], v[172:175], a[124:127]\n"
"ds_read_b64_tr_b16 a[224:225], v9 offset:32768\n"
"v_add_f32 v22, v22, v110\n"
"v_mfma_f32_16x16x32_bf16 a[128:131], a[240:243], v[180:183], a[128:131]\n"
"s_add_u32 m0, s46, 36864\n"
"v_exp_f32 v116, v116\n"
"v_mfma_f32_16x16x32_bf16 a[132:135], a[240:243], v[188:191], a[132:135]\n"
"buffer_load_dwordx4 v14, s[24:27], s76 offen lds\n"
"v_add_f32 v22, v22, v111\n"
"v_cvt_pk_bf16_f32 v223, v110, v111\n"
"v_mfma_f32_16x16x32_bf16 a[136:139], a[240:243], v[196:199], a[136:139]\n"
"v_exp_f32 v117, v117\n"
"v_mfma_f32_16x16x32_bf16 a[140:143], a[244:247], v[148:151], a[140:143]\n"
"ds_read_b64_tr_b16 a[226:227], v9 offset:36864\n"
"v_add_f32 v24, v24, v116\n"
"v_mfma_f32_16x16x32_bf16 a[144:147], a[244:247], v[156:159], a[144:147]\n"
"v_exp_f32 v118, v118\n"
"v_mfma_f32_16x16x32_bf16 a[148:151], a[244:247], v[164:167], a[148:151]\n"
"buffer_load_dwordx4 v15, s[24:27], s76 offen offset:1024 lds\n"
"v_add_f32 v24, v24, v117\n"
"v_cvt_pk_bf16_f32 v230, v116, v117\n"
"v_mfma_f32_16x16x32_bf16 a[152:155], a[244:247], v[172:175], a[152:155]\n"
"v_exp_f32 v119, v119\n"
"v_mfma_f32_16x16x32_bf16 a[156:159], a[244:247], v[180:183], a[156:159]\n"
"ds_read_b64_tr_b16 a[228:229], v10 offset:32768\n"
"v_add_f32 v24, v24, v118\n"
"v_mfma_f32_16x16x32_bf16 a[160:163], a[244:247], v[188:191], a[160:163]\n"
"v_exp_f32 v124, v124\n"
"v_mfma_f32_16x16x32_bf16 a[164:167], a[244:247], v[196:199], a[164:167]\n"
"buffer_load_dwordx4 v21, s[24:27], s76 offen offset:2048 lds\n"
"v_add_f32 v24, v24, v119\n"
"v_cvt_pk_bf16_f32 v231, v118, v119\n"
"v_mfma_f32_16x16x32_bf16 a[168:171], a[248:251], v[148:151], a[168:171]\n"
"v_exp_f32 v125, v125\n"
"v_mfma_f32_16x16x32_bf16 a[172:175], a[248:251], v[156:159], a[172:175]\n"
"ds_read_b64_tr_b16 a[230:231], v10 offset:36864\n"
"v_add_f32 v26, v26, v124\n"
"v_mfma_f32_16x16x32_bf16 a[176:179], a[248:251], v[164:167], a[176:179]\n"
"v_exp_f32 v126, v126\n"
"v_mfma_f32_16x16x32_bf16 a[180:183], a[248:251], v[172:175], a[180:183]\n"
"buffer_load_dwordx4 v23, s[24:27], s76 offen offset:3072 lds\n"
"v_add_f32 v26, v26, v125\n"
"v_cvt_pk_bf16_f32 v238, v124, v125\n"
"v_mfma_f32_16x16x32_bf16 a[184:187], a[248:251], v[180:183], a[184:187]\n"
"v_exp_f32 v127, v127\n"
"v_mfma_f32_16x16x32_bf16 a[188:191], a[248:251], v[188:191], a[188:191]\n"
"ds_read_b64_tr_b16 a[232:233], v9 offset:33024\n"
"v_add_f32 v26, v26, v126\n"
"v_mfma_f32_16x16x32_bf16 a[192:195], a[248:251], v[196:199], a[192:195]\n"
"v_exp_f32 v132, v132\n"
"v_mfma_f32_16x16x32_bf16 a[196:199], a[252:255], v[148:151], a[196:199]\n"
"v_add_f32 v26, v26, v127\n"
"v_cvt_pk_bf16_f32 v239, v126, v127\n"
"v_mfma_f32_16x16x32_bf16 a[200:203], a[252:255], v[156:159], a[200:203]\n"
"v_exp_f32 v133, v133\n"
"v_mfma_f32_16x16x32_bf16 a[204:207], a[252:255], v[164:167], a[204:207]\n"
"ds_read_b64_tr_b16 a[234:235], v9 offset:37120\n"
"v_add_f32 v28, v28, v132\n"
"v_mfma_f32_16x16x32_bf16 a[208:211], a[252:255], v[172:175], a[208:211]\n"
"v_exp_f32 v134, v134\n"
"v_mfma_f32_16x16x32_bf16 a[212:215], a[252:255], v[180:183], a[212:215]\n"
"v_add_f32 v28, v28, v133\n"
"v_cvt_pk_bf16_f32 v246, v132, v133\n"
"v_mfma_f32_16x16x32_bf16 a[216:219], a[252:255], v[188:191], a[216:219]\n"
"v_exp_f32 v135, v135\n"
"v_mfma_f32_16x16x32_bf16 a[220:223], a[252:255], v[196:199], a[220:223]\n"
"ds_read_b64_tr_b16 a[236:237], v10 offset:33024\n"
"v_add_f32 v28, v28, v134\n"
"s_cmp_lg_u32 s64, 0\n"
"s_waitcnt lgkmcnt(5)\n"
"s_cbranch_scc1 4f\n"
"v_mfma_f32_16x16x32_bf16 a[0:3], a[224:227], v[204:207], a[0:3]\n"
"v_exp_f32 v140, v140\n"
"v_mfma_f32_16x16x32_bf16 a[4:7], a[224:227], v[212:215], a[4:7]\n"
"s_mov_b32 s78, 0x33800000\n"
"v_add_f32 v28, v28, v135\n"
"v_cvt_pk_bf16_f32 v247, v134, v135\n"
"v_mfma_f32_16x16x32_bf16 a[8:11], a[224:227], v[220:223], a[8:11]\n"
"v_exp_f32 v141, v141\n"
"v_mfma_f32_16x16x32_bf16 a[12:15], a[224:227], v[228:231], a[12:15]\n"
"s_mov_b32 s79, 2143289344\n"
"ds_read_b64_tr_b16 a[238:239], v10 offset:37120\n"
"v_add_f32 v30, v30, v140\n"
"v_mfma_f32_16x16x32_bf16 a[16:19], a[224:227], v[236:239], a[16:19]\n"
"s_waitcnt lgkmcnt(4)\n"
"v_exp_f32 v142, v142\n"
"v_mfma_f32_16x16x32_bf16 a[44:47], a[228:231], v[236:239], a[44:47]\n"
"v_add_f32 v30, v30, v141\n"
"v_cvt_pk_bf16_f32 v254, v140, v141\n"
"v_mfma_f32_16x16x32_bf16 a[40:43], a[228:231], v[228:231], a[40:43]\n"
"v_exp_f32 v143, v143\n"
"v_mfma_f32_16x16x32_bf16 a[36:39], a[228:231], v[220:223], a[36:39]\n"
"ds_read_b64_tr_b16 a[240:241], v9 offset:33280\n"
"v_add_f32 v30, v30, v142\n"
"v_mfma_f32_16x16x32_bf16 a[32:35], a[228:231], v[212:215], a[32:35]\n"
"s_waitcnt lgkmcnt(3)\n"
"v_mfma_f32_16x16x32_bf16 a[28:31], a[228:231], v[204:207], a[28:31]\n"
"v_add_f32 v30, v30, v143\n"
"v_cvt_pk_bf16_f32 v255, v142, v143\n"
"v_mfma_f32_16x16x32_bf16 a[56:59], a[232:235], v[204:207], a[56:59]\n"
"ds_read_b64_tr_b16 a[242:243], v9 offset:37376\n"
"v_permlane32_swap_b32 v18, v20\n"
"v_mfma_f32_16x16x32_bf16 a[60:63], a[232:235], v[212:215], a[60:63]\n"
"v_permlane32_swap_b32 v22, v24\n"
"v_permlane32_swap_b32 v26, v28\n"
"v_mfma_f32_16x16x32_bf16 a[64:67], a[232:235], v[220:223], a[64:67]\n"
"ds_read_b64_tr_b16 a[244:245], v10 offset:33280\n"
"v_permlane32_swap_b32 v30, v31\n"
"v_mfma_f32_16x16x32_bf16 a[68:71], a[232:235], v[228:231], a[68:71]\n"
"v_add_f32 v18, v18, v20\n"
"v_add_f32 v22, v22, v24\n"
"v_mfma_f32_16x16x32_bf16 a[72:75], a[232:235], v[236:239], a[72:75]\n"
"s_waitcnt lgkmcnt(3)\n"
"ds_read_b64_tr_b16 a[246:247], v10 offset:37376\n"
"v_add_f32 v26, v26, v28\n"
"v_mfma_f32_16x16x32_bf16 a[100:103], a[236:239], v[236:239], a[100:103]\n"
"v_add_f32 v30, v30, v31\n"
"v_mov_b32 v197, 0x53800000\n"
"v_mfma_f32_16x16x32_bf16 a[96:99], a[236:239], v[228:231], a[96:99]\n"
"ds_read_b64_tr_b16 a[248:249], v9 offset:33536\n"
"v_mov_b32 v196, s79\n"
"v_mfma_f32_16x16x32_bf16 a[92:95], a[236:239], v[220:223], a[92:95]\n"
"v_permlane16_swap_b32 v18, v22\n"
"v_permlane16_swap_b32 v26, v30\n"
"v_mfma_f32_16x16x32_bf16 a[88:91], a[236:239], v[212:215], a[88:91]\n"
"ds_read_b64_tr_b16 a[250:251], v9 offset:37632\n"
"v_add_f32 v18, v18, v22\n"
"v_mfma_f32_16x16x32_bf16 a[84:87], a[236:239], v[204:207], a[84:87]\n"
"v_add_f32 v26, v26, v30\n"
"v_med3_f32 v19, v18, s78, v197\n"
"v_mfma_f32_16x16x32_bf16 a[20:23], a[224:227], v[244:247], a[20:23]\n"
"ds_read_b64_tr_b16 a[252:253], v10 offset:33536\n"
"v_med3_f32 v27, v26, s78, v197\n"
"v_mfma_f32_16x16x32_bf16 a[24:27], a[224:227], v[252:255], a[24:27]\n"
"v_cmp_eq_f32 vcc, v19, v18\n"
"v_cmp_eq_f32 s[70:71], v27, v26\n"
"v_mfma_f32_16x16x32_bf16 a[52:55], a[228:231], v[252:255], a[52:55]\n"
"ds_read_b64_tr_b16 a[254:255], v10 offset:37632\n"
"v_mfma_f32_16x16x32_bf16 a[48:51], a[228:231], v[244:247], a[48:51]\n"
"v_rcp_f32 v18, v18\n"
"v_mfma_f32_16x16x32_bf16 a[76:79], a[232:235], v[244:247], a[76:79]\n"
"v_rcp_f32 v26, v26\n"
"v_mfma_f32_16x16x32_bf16 a[80:83], a[232:235], v[252:255], a[80:83]\n"
"v_cndmask_b32 v18, v196, v18, vcc\n"
"v_cndmask_b32 v26, v196, v26, s[70:71]\n"
"v_mfma_f32_16x16x32_bf16 a[108:111], a[236:239], v[252:255], a[108:111]\n"
"s_waitcnt lgkmcnt(6)\n"
"v_mov_b32 v22, v18\n"
"v_mov_b32 v30, v26\n"
"v_mfma_f32_16x16x32_bf16 a[104:107], a[236:239], v[244:247], a[104:107]\n"
"v_min_u32 v19, 16, v1\n"
"v_lshlrev_b32 v19, 26, v19\n"
"v_mfma_f32_16x16x32_bf16 a[112:115], a[240:243], v[204:207], a[112:115]\n"
"v_permlane16_swap_b32 v18, v22\n"
"v_permlane16_swap_b32 v26, v30\n"
"v_mfma_f32_16x16x32_bf16 a[116:119], a[240:243], v[212:215], a[116:119]\n"
"v_mov_b32 v20, v18\n"
"v_mov_b32 v24, v22\n"
"v_mfma_f32_16x16x32_bf16 a[120:123], a[240:243], v[220:223], a[120:123]\n"
"s_waitcnt vmcnt(22)\n"
"ds_read_b64_tr_b16 a[224:225], v2 offset:0\n"
"v_mov_b32 v28, v26\n"
"v_mfma_f32_16x16x32_bf16 a[124:127], a[240:243], v[228:231], a[124:127]\n"
"v_mov_b32 v31, v30\n"
"v_permlane32_swap_b32 v18, v20\n"
"v_mfma_f32_16x16x32_bf16 a[128:131], a[240:243], v[236:239], a[128:131]\n"
"v_permlane32_swap_b32 v22, v24\n"
"v_permlane32_swap_b32 v26, v28\n"
"v_mfma_f32_16x16x32_bf16 a[132:135], a[240:243], v[244:247], a[132:135]\n"
"ds_read_b64_tr_b16 a[226:227], v2 offset:1024\n"
"v_permlane32_swap_b32 v30, v31\n"
"v_mfma_f32_16x16x32_bf16 a[136:139], a[240:243], v[252:255], a[136:139]\n"
"s_waitcnt lgkmcnt(6)\n"
"v_accvgpr_read_b32 v88, a0\n"
"v_accvgpr_read_b32 v89, a1\n"
"v_mfma_f32_16x16x32_bf16 a[164:167], a[244:247], v[252:255], a[164:167]\n"
"v_accvgpr_read_b32 v90, a2\n"
"v_accvgpr_read_b32 v91, a3\n"
"v_mfma_f32_16x16x32_bf16 a[160:163], a[244:247], v[244:247], a[160:163]\n"
"s_waitcnt vmcnt(20)\n"
"ds_read_b64_tr_b16 a[228:229], v2 offset:2048\n"
"v_accvgpr_read_b32 v92, a28\n"
"v_mfma_f32_16x16x32_bf16 a[156:159], a[244:247], v[236:239], a[156:159]\n"
"v_accvgpr_read_b32 v93, a29\n"
"v_accvgpr_read_b32 v94, a30\n"
"v_mfma_f32_16x16x32_bf16 a[152:155], a[244:247], v[228:231], a[152:155]\n"
"v_accvgpr_read_b32 v95, a31\n"
"v_mul_f32 v88, v88, v18\n"
"v_mfma_f32_16x16x32_bf16 a[148:151], a[244:247], v[220:223], a[148:151]\n"
"ds_read_b64_tr_b16 a[230:231], v2 offset:3072\n"
"v_mul_f32 v89, v89, v18\n"
"v_mfma_f32_16x16x32_bf16 a[144:147], a[244:247], v[212:215], a[144:147]\n"
"s_waitcnt lgkmcnt(6)\n"
"v_mul_f32 v90, v90, v18\n"
"v_mul_f32 v91, v91, v18\n"
"v_mfma_f32_16x16x32_bf16 a[140:143], a[244:247], v[204:207], a[140:143]\n"
"v_mul_f32 v92, v92, v18\n"
"v_mul_f32 v93, v93, v18\n"
"v_mfma_f32_16x16x32_bf16 a[168:171], a[248:251], v[204:207], a[168:171]\n"
"ds_read_b64_tr_b16 a[232:233], v3 offset:0\n"
"v_mul_f32 v94, v94, v18\n"
"v_mfma_f32_16x16x32_bf16 a[172:175], a[248:251], v[212:215], a[172:175]\n"
"v_mul_f32 v95, v95, v18\n"
"v_cvt_pk_bf16_f32 v88, v88, v89\n"
"v_mfma_f32_16x16x32_bf16 a[176:179], a[248:251], v[220:223], a[176:179]\n"
"v_cvt_pk_bf16_f32 v89, v90, v91\n"
"v_cvt_pk_bf16_f32 v90, v92, v93\n"
"v_mfma_f32_16x16x32_bf16 a[180:183], a[248:251], v[228:231], a[180:183]\n"
"ds_read_b64_tr_b16 a[234:235], v3 offset:1024\n"
"v_cvt_pk_bf16_f32 v91, v94, v95\n"
"v_mfma_f32_16x16x32_bf16 a[184:187], a[248:251], v[236:239], a[184:187]\n"
"ds_write_b128 v7, v[88:91] offset:0\n"
"v_mfma_f32_16x16x32_bf16 a[188:191], a[248:251], v[244:247], a[188:191]\n"
"v_accvgpr_read_b32 v96, a4\n"
"v_accvgpr_read_b32 v97, a5\n"
"v_mfma_f32_16x16x32_bf16 a[192:195], a[248:251], v[252:255], a[192:195]\n"
"s_waitcnt lgkmcnt(7)\n"
"ds_read_b64_tr_b16 a[236:237], v3 offset:2048\n"
"v_accvgpr_read_b32 v98, a6\n"
"v_mfma_f32_16x16x32_bf16 a[220:223], a[252:255], v[252:255], a[220:223]\n"
"v_accvgpr_read_b32 v99, a7\n"
"v_accvgpr_read_b32 v100, a32\n"
"v_mfma_f32_16x16x32_bf16 a[216:219], a[252:255], v[244:247], a[216:219]\n"
"v_accvgpr_read_b32 v101, a33\n"
"v_accvgpr_read_b32 v102, a34\n"
"v_mfma_f32_16x16x32_bf16 a[212:215], a[252:255], v[236:239], a[212:215]\n"
"ds_read_b64_tr_b16 a[238:239], v3 offset:3072\n"
"v_accvgpr_read_b32 v103, a35\n"
"v_mfma_f32_16x16x32_bf16 a[208:211], a[252:255], v[228:231], a[208:211]\n"
"v_mul_f32 v96, v96, v20\n"
"v_mul_f32 v97, v97, v20\n"
"v_mfma_f32_16x16x32_bf16 a[204:207], a[252:255], v[220:223], a[204:207]\n"
"v_mul_f32 v98, v98, v20\n"
"v_mul_f32 v99, v99, v20\n"
"v_mfma_f32_16x16x32_bf16 a[200:203], a[252:255], v[212:215], a[200:203]\n"
"v_mul_f32 v100, v100, v20\n"
"v_mul_f32 v101, v101, v20\n"
"v_mfma_f32_16x16x32_bf16 a[196:199], a[252:255], v[204:207], a[196:199]\n"
"v_mul_f32 v102, v102, v20\n"
"v_mul_f32 v103, v103, v20\n"
"v_cvt_pk_bf16_f32 v96, v96, v97\n"
"v_cvt_pk_bf16_f32 v97, v98, v99\n"
"v_cvt_pk_bf16_f32 v98, v100, v101\n"
"v_cvt_pk_bf16_f32 v99, v102, v103\n"
"ds_write_b128 v7, v[96:99] offset:4096\n"
"v_mov_b32 v8, s68\n"
"v_mov_b32 v9, s69\n"
"v_accvgpr_read_b32 v104, a8\n"
"v_accvgpr_read_b32 v105, a9\n"
"v_accvgpr_read_b32 v106, a10\n"
"v_accvgpr_read_b32 v107, a11\n"
"v_accvgpr_read_b32 v108, a36\n"
"v_accvgpr_read_b32 v109, a37\n"
"v_accvgpr_read_b32 v110, a38\n"
"v_accvgpr_read_b32 v111, a39\n"
"v_pk_mul_f32 v[104:105], v[104:105], v[22:23] op_sel_hi:[1,0]\n"
"v_pk_mul_f32 v[106:107], v[106:107], v[22:23] op_sel_hi:[1,0]\n"
"v_pk_mul_f32 v[108:109], v[108:109], v[22:23] op_sel_hi:[1,0]\n"
"v_pk_mul_f32 v[110:111], v[110:111], v[22:23] op_sel_hi:[1,0]\n"
"v_cvt_pk_bf16_f32 v104, v104, v105\n"
"v_cvt_pk_bf16_f32 v105, v106, v107\n"
"v_cvt_pk_bf16_f32 v106, v108, v109\n"
"v_cvt_pk_bf16_f32 v107, v110, v111\n"
"ds_write_b128 v7, v[104:107] offset:8192\n"
"s_waitcnt vmcnt(18)\n"
"ds_read_b64_tr_b16 a[240:241], v2 offset:4096\n"
"v_accvgpr_read_b32 v112, a12\n"
"v_accvgpr_read_b32 v113, a13\n"
"v_accvgpr_read_b32 v114, a14\n"
"v_accvgpr_read_b32 v115, a15\n"
"v_accvgpr_read_b32 v116, a40\n"
"v_accvgpr_read_b32 v117, a41\n"
"v_accvgpr_read_b32 v118, a42\n"
"v_accvgpr_read_b32 v119, a43\n"
"v_pk_mul_f32 v[112:113], v[112:113], v[24:25] op_sel_hi:[1,0]\n"
"v_pk_mul_f32 v[114:115], v[114:115], v[24:25] op_sel_hi:[1,0]\n"
"v_pk_mul_f32 v[116:117], v[116:117], v[24:25] op_sel_hi:[1,0]\n"
"v_pk_mul_f32 v[118:119], v[118:119], v[24:25] op_sel_hi:[1,0]\n"
"v_cvt_pk_bf16_f32 v112, v112, v113\n"
"v_cvt_pk_bf16_f32 v113, v114, v115\n"
"v_cvt_pk_bf16_f32 v114, v116, v117\n"
"v_cvt_pk_bf16_f32 v115, v118, v119\n"
"ds_write_b128 v7, v[112:115] offset:12288\n"
"ds_read_b64_tr_b16 a[242:243], v2 offset:5120\n"
"v_accvgpr_read_b32 v120, a16\n"
"v_accvgpr_read_b32 v121, a17\n"
"v_accvgpr_read_b32 v122, a18\n"
"v_accvgpr_read_b32 v123, a19\n"
"v_accvgpr_read_b32 v124, a44\n"
"v_accvgpr_read_b32 v125, a45\n"
"v_accvgpr_read_b32 v126, a46\n"
"v_accvgpr_read_b32 v127, a47\n"
"v_pk_mul_f32 v[120:121], v[120:121], v[26:27] op_sel_hi:[1,0]\n"
"v_pk_mul_f32 v[122:123], v[122:123], v[26:27] op_sel_hi:[1,0]\n"
"v_pk_mul_f32 v[124:125], v[124:125], v[26:27] op_sel_hi:[1,0]\n"
"v_pk_mul_f32 v[126:127], v[126:127], v[26:27] op_sel_hi:[1,0]\n"
"v_cvt_pk_bf16_f32 v120, v120, v121\n"
"v_cvt_pk_bf16_f32 v121, v122, v123\n"
"v_cvt_pk_bf16_f32 v122, v124, v125\n"
"v_cvt_pk_bf16_f32 v123, v126, v127\n"
"ds_write_b128 v7, v[120:123] offset:16384\n"
"s_waitcnt vmcnt(16)\n"
"ds_read_b64_tr_b16 a[244:245], v2 offset:6144\n"
"v_accvgpr_read_b32 v128, a20\n"
"v_accvgpr_read_b32 v129, a21\n"
"v_accvgpr_read_b32 v130, a22\n"
"v_accvgpr_read_b32 v131, a23\n"
"v_accvgpr_read_b32 v132, a48\n"
"v_accvgpr_read_b32 v133, a49\n"
"v_accvgpr_read_b32 v134, a50\n"
"v_accvgpr_read_b32 v135, a51\n"
"v_pk_mul_f32 v[128:129], v[128:129], v[28:29] op_sel_hi:[1,0]\n"
"v_pk_mul_f32 v[130:131], v[130:131], v[28:29] op_sel_hi:[1,0]\n"
"v_pk_mul_f32 v[132:133], v[132:133], v[28:29] op_sel_hi:[1,0]\n"
"v_pk_mul_f32 v[134:135], v[134:135], v[28:29] op_sel_hi:[1,0]\n"
"v_cvt_pk_bf16_f32 v128, v128, v129\n"
"v_cvt_pk_bf16_f32 v129, v130, v131\n"
"v_cvt_pk_bf16_f32 v130, v132, v133\n"
"v_cvt_pk_bf16_f32 v131, v134, v135\n"
"ds_write_b128 v7, v[128:131] offset:20480\n"
"ds_read_b64_tr_b16 a[246:247], v2 offset:7168\n"
"v_accvgpr_read_b32 v136, a24\n"
"v_accvgpr_read_b32 v137, a25\n"
"v_accvgpr_read_b32 v138, a26\n"
"v_accvgpr_read_b32 v139, a27\n"
"v_accvgpr_read_b32 v140, a52\n"
"v_accvgpr_read_b32 v141, a53\n"
"v_accvgpr_read_b32 v142, a54\n"
"v_accvgpr_read_b32 v143, a55\n"
"v_pk_mul_f32 v[136:137], v[136:137], v[30:31] op_sel_hi:[1,0]\n"
"v_pk_mul_f32 v[138:139], v[138:139], v[30:31] op_sel_hi:[1,0]\n"
"v_pk_mul_f32 v[140:141], v[140:141], v[30:31] op_sel_hi:[1,0]\n"
"v_pk_mul_f32 v[142:143], v[142:143], v[30:31] op_sel_hi:[1,0]\n"
"v_cvt_pk_bf16_f32 v136, v136, v137\n"
"v_cvt_pk_bf16_f32 v137, v138, v139\n"
"v_cvt_pk_bf16_f32 v138, v140, v141\n"
"v_cvt_pk_bf16_f32 v139, v142, v143\n"
"ds_write_b128 v7, v[136:139] offset:24576\n"
"s_xor_b32 s75, s48, 65536\n"
"s_add_u32 m0, s46, 131072\n"
"s_nop 0\n"
"s_waitcnt lgkmcnt(13)\n"
"buffer_load_dwordx4 v14, s[24:27], s75 offen lds\n"
"buffer_load_dwordx4 v15, s[24:27], s75 offen offset:1024 lds\n"
"v_accvgpr_read_b32 v88, a56\n"
"v_accvgpr_read_b32 v89, a57\n"
"v_accvgpr_read_b32 v90, a58\n"
"v_accvgpr_read_b32 v91, a59\n"
"v_accvgpr_read_b32 v92, a84\n"
"v_accvgpr_read_b32 v93, a85\n"
"v_accvgpr_read_b32 v94, a86\n"
"v_accvgpr_read_b32 v95, a87\n"
"v_pk_mul_f32 v[88:89], v[88:89], v[18:19] op_sel_hi:[1,0]\n"
"v_pk_mul_f32 v[90:91], v[90:91], v[18:19] op_sel_hi:[1,0]\n"
"v_pk_mul_f32 v[92:93], v[92:93], v[18:19] op_sel_hi:[1,0]\n"
"v_pk_mul_f32 v[94:95], v[94:95], v[18:19] op_sel_hi:[1,0]\n"
"v_cvt_pk_bf16_f32 v88, v88, v89\n"
"v_cvt_pk_bf16_f32 v89, v90, v91\n"
"v_cvt_pk_bf16_f32 v90, v92, v93\n"
"v_cvt_pk_bf16_f32 v91, v94, v95\n"
"ds_write_b128 v7, v[88:91] offset:1024\n"
"s_waitcnt lgkmcnt(11)\n"
"buffer_load_dwordx4 v21, s[24:27], s75 offen offset:2048 lds\n"
"buffer_load_dwordx4 v23, s[24:27], s75 offen offset:3072 lds\n"
"v_accvgpr_read_b32 v96, a60\n"
"v_accvgpr_read_b32 v97, a61\n"
"v_accvgpr_read_b32 v98, a62\n"
"v_accvgpr_read_b32 v99, a63\n"
"v_accvgpr_read_b32 v100, a88\n"
"v_accvgpr_read_b32 v101, a89\n"
"v_accvgpr_read_b32 v102, a90\n"
"v_accvgpr_read_b32 v103, a91\n"
"v_pk_mul_f32 v[96:97], v[96:97], v[20:21] op_sel_hi:[1,0]\n"
"v_pk_mul_f32 v[98:99], v[98:99], v[20:21] op_sel_hi:[1,0]\n"
"v_pk_mul_f32 v[100:101], v[100:101], v[20:21] op_sel_hi:[1,0]\n"
"v_pk_mul_f32 v[102:103], v[102:103], v[20:21] op_sel_hi:[1,0]\n"
"v_cvt_pk_bf16_f32 v96, v96, v97\n"
"v_cvt_pk_bf16_f32 v97, v98, v99\n"
"v_cvt_pk_bf16_f32 v98, v100, v101\n"
"v_cvt_pk_bf16_f32 v99, v102, v103\n"
"ds_write_b128 v7, v[96:99] offset:5120\n"
"v_accvgpr_read_b32 v104, a64\n"
"v_accvgpr_read_b32 v105, a65\n"
"v_accvgpr_read_b32 v106, a66\n"
"v_accvgpr_read_b32 v107, a67\n"
"v_accvgpr_read_b32 v108, a92\n"
"v_accvgpr_read_b32 v109, a93\n"
"v_accvgpr_read_b32 v110, a94\n"
"v_accvgpr_read_b32 v111, a95\n"
"v_pk_mul_f32 v[104:105], v[104:105], v[22:23] op_sel_hi:[1,0]\n"
"v_pk_mul_f32 v[106:107], v[106:107], v[22:23] op_sel_hi:[1,0]\n"
"v_pk_mul_f32 v[108:109], v[108:109], v[22:23] op_sel_hi:[1,0]\n"
"v_pk_mul_f32 v[110:111], v[110:111], v[22:23] op_sel_hi:[1,0]\n"
"v_cvt_pk_bf16_f32 v104, v104, v105\n"
"v_cvt_pk_bf16_f32 v105, v106, v107\n"
"v_cvt_pk_bf16_f32 v106, v108, v109\n"
"v_cvt_pk_bf16_f32 v107, v110, v111\n"
"ds_write_b128 v7, v[104:107] offset:9216\n"
"v_accvgpr_read_b32 v112, a68\n"
"v_accvgpr_read_b32 v113, a69\n"
"v_accvgpr_read_b32 v114, a70\n"
"v_accvgpr_read_b32 v115, a71\n"
"v_accvgpr_read_b32 v116, a96\n"
"v_accvgpr_read_b32 v117, a97\n"
"v_accvgpr_read_b32 v118, a98\n"
"v_accvgpr_read_b32 v119, a99\n"
"v_pk_mul_f32 v[112:113], v[112:113], v[24:25] op_sel_hi:[1,0]\n"
"v_pk_mul_f32 v[114:115], v[114:115], v[24:25] op_sel_hi:[1,0]\n"
"v_pk_mul_f32 v[116:117], v[116:117], v[24:25] op_sel_hi:[1,0]\n"
"v_pk_mul_f32 v[118:119], v[118:119], v[24:25] op_sel_hi:[1,0]\n"
"v_cvt_pk_bf16_f32 v112, v112, v113\n"
"v_cvt_pk_bf16_f32 v113, v114, v115\n"
"v_cvt_pk_bf16_f32 v114, v116, v117\n"
"v_cvt_pk_bf16_f32 v115, v118, v119\n"
"ds_write_b128 v7, v[112:115] offset:13312\n"
"v_accvgpr_read_b32 v120, a72\n"
"v_accvgpr_read_b32 v121, a73\n"
"v_accvgpr_read_b32 v122, a74\n"
"v_accvgpr_read_b32 v123, a75\n"
"v_accvgpr_read_b32 v124, a100\n"
"v_accvgpr_read_b32 v125, a101\n"
"v_accvgpr_read_b32 v126, a102\n"
"v_accvgpr_read_b32 v127, a103\n"
"v_pk_mul_f32 v[120:121], v[120:121], v[26:27] op_sel_hi:[1,0]\n"
"v_pk_mul_f32 v[122:123], v[122:123], v[26:27] op_sel_hi:[1,0]\n"
"v_pk_mul_f32 v[124:125], v[124:125], v[26:27] op_sel_hi:[1,0]\n"
"v_pk_mul_f32 v[126:127], v[126:127], v[26:27] op_sel_hi:[1,0]\n"
"v_cvt_pk_bf16_f32 v120, v120, v121\n"
"v_cvt_pk_bf16_f32 v121, v122, v123\n"
"v_cvt_pk_bf16_f32 v122, v124, v125\n"
"v_cvt_pk_bf16_f32 v123, v126, v127\n"
"ds_write_b128 v7, v[120:123] offset:17408\n"
"v_accvgpr_read_b32 v128, a76\n"
"v_accvgpr_read_b32 v129, a77\n"
"v_accvgpr_read_b32 v130, a78\n"
"v_accvgpr_read_b32 v131, a79\n"
"v_accvgpr_read_b32 v132, a104\n"
"v_accvgpr_read_b32 v133, a105\n"
"v_accvgpr_read_b32 v134, a106\n"
"v_accvgpr_read_b32 v135, a107\n"
"v_pk_mul_f32 v[128:129], v[128:129], v[28:29] op_sel_hi:[1,0]\n"
"v_pk_mul_f32 v[130:131], v[130:131], v[28:29] op_sel_hi:[1,0]\n"
"v_pk_mul_f32 v[132:133], v[132:133], v[28:29] op_sel_hi:[1,0]\n"
"v_pk_mul_f32 v[134:135], v[134:135], v[28:29] op_sel_hi:[1,0]\n"
"v_cvt_pk_bf16_f32 v128, v128, v129\n"
"v_cvt_pk_bf16_f32 v129, v130, v131\n"
"v_cvt_pk_bf16_f32 v130, v132, v133\n"
"v_cvt_pk_bf16_f32 v131, v134, v135\n"
"ds_write_b128 v7, v[128:131] offset:21504\n"
"v_accvgpr_read_b32 v136, a80\n"
"v_accvgpr_read_b32 v137, a81\n"
"v_accvgpr_read_b32 v138, a82\n"
"v_accvgpr_read_b32 v139, a83\n"
"v_accvgpr_read_b32 v140, a108\n"
"v_accvgpr_read_b32 v141, a109\n"
"v_accvgpr_read_b32 v142, a110\n"
"v_accvgpr_read_b32 v143, a111\n"
"v_pk_mul_f32 v[136:137], v[136:137], v[30:31] op_sel_hi:[1,0]\n"
"v_pk_mul_f32 v[138:139], v[138:139], v[30:31] op_sel_hi:[1,0]\n"
"v_pk_mul_f32 v[140:141], v[140:141], v[30:31] op_sel_hi:[1,0]\n"
"v_pk_mul_f32 v[142:143], v[142:143], v[30:31] op_sel_hi:[1,0]\n"
"v_cvt_pk_bf16_f32 v136, v136, v137\n"
"v_cvt_pk_bf16_f32 v137, v138, v139\n"
"v_cvt_pk_bf16_f32 v138, v140, v141\n"
"v_cvt_pk_bf16_f32 v139, v142, v143\n"
"ds_write_b128 v7, v[136:139] offset:25600\n"
"s_waitcnt lgkmcnt(0)\n"
"s_barrier\n"
"ds_read_b128 a[0:3], v6 offset:0\n"
"ds_read_b128 a[28:31], v6 offset:2048\n"
"ds_read_b128 a[32:35], v6 offset:6144\n"
"ds_read_b128 a[4:7], v6 offset:4096\n"
"ds_read_b128 a[8:11], v6 offset:8192\n"
"s_waitcnt lgkmcnt(4)\n"
"ds_read_b128 a[36:39], v6 offset:10240\n"
"v_mfma_f32_16x16x32_bf16 v[32:35], a[224:227], a[0:3], 0\n"
"s_waitcnt lgkmcnt(3)\n"
"ds_read_b128 a[40:43], v6 offset:14336\n"
"v_accvgpr_read_b32 v216, a112\n"
"v_mfma_f32_16x16x32_bf16 v[32:35], a[228:231], a[28:31], v[32:35]\n"
"ds_read_b128 a[12:15], v6 offset:12288\n"
"v_accvgpr_read_b32 v217, a113\n"
"v_accvgpr_read_b32 v218, a114\n"
"v_mfma_f32_16x16x32_bf16 v[40:43], a[228:231], a[32:35], 0\n"
"s_waitcnt lgkmcnt(3)\n"
"ds_read_b128 a[16:19], v6 offset:16384\n"
"v_accvgpr_read_b32 v219, a115\n"
"v_mfma_f32_16x16x32_bf16 v[40:43], a[224:227], a[4:7], v[40:43]\n"
"ds_read_b128 a[44:47], v6 offset:18432\n"
"v_accvgpr_read_b32 v220, a140\n"
"v_accvgpr_read_b32 v221, a141\n"
"v_mfma_f32_16x16x32_bf16 v[48:51], a[224:227], a[8:11], 0\n"
"s_waitcnt lgkmcnt(3)\n"
"ds_read_b128 a[48:51], v6 offset:22528\n"
"v_accvgpr_read_b32 v222, a142\n"
"v_accvgpr_read_b32 v223, a143\n"
"v_mfma_f32_16x16x32_bf16 v[48:51], a[228:231], a[36:39], v[48:51]\n"
"ds_read_b128 a[20:23], v6 offset:20480\n"
"v_mul_f32 v216, v216, v18\n"
"v_mfma_f32_16x16x32_bf16 v[56:59], a[228:231], a[40:43], 0\n"
"s_waitcnt lgkmcnt(3)\n"
"ds_read_b128 a[24:27], v6 offset:24576\n"
"v_mul_f32 v217, v217, v18\n"
"v_mul_f32 v218, v218, v18\n"
"v_mfma_f32_16x16x32_bf16 v[56:59], a[224:227], a[12:15], v[56:59]\n"
"ds_read_b128 a[52:55], v6 offset:26624\n"
"v_mul_f32 v219, v219, v18\n"
"v_mfma_f32_16x16x32_bf16 v[64:67], a[224:227], a[16:19], 0\n"
"s_waitcnt lgkmcnt(3)\n"
"ds_read_b128 a[56:59], v6 offset:1024\n"
"v_mul_f32 v220, v220, v18\n"
"v_mul_f32 v221, v221, v18\n"
"v_mfma_f32_16x16x32_bf16 v[64:67], a[228:231], a[44:47], v[64:67]\n"
"ds_read_b128 a[84:87], v6 offset:3072\n"
"v_mul_f32 v222, v222, v18\n"
"v_mul_f32 v223, v223, v18\n"
"v_mfma_f32_16x16x32_bf16 v[72:75], a[228:231], a[48:51], 0\n"
"s_waitcnt lgkmcnt(3)\n"
"v_cvt_pk_bf16_f32 v216, v216, v217\n"
"v_mfma_f32_16x16x32_bf16 v[72:75], a[224:227], a[20:23], v[72:75]\n"
"v_cvt_pk_bf16_f32 v217, v218, v219\n"
"v_cvt_pk_bf16_f32 v218, v220, v221\n"
"v_cvt_pk_bf16_f32 v219, v222, v223\n"
"v_mfma_f32_16x16x32_bf16 v[80:83], a[224:227], a[24:27], 0\n"
"s_waitcnt lgkmcnt(2)\n"
"ds_read_b128 a[88:91], v6 offset:7168\n"
"v_accvgpr_read_b32 v200, a116\n"
"v_mfma_f32_16x16x32_bf16 v[80:83], a[228:231], a[52:55], v[80:83]\n"
"ds_read_b128 a[60:63], v6 offset:5120\n"
"v_accvgpr_read_b32 v201, a117\n"
"v_accvgpr_read_b32 v202, a118\n"
"v_mfma_f32_16x16x32_bf16 v[84:87], a[236:239], a[52:55], 0\n"
"ds_read_b128 a[64:67], v6 offset:9216\n"
"v_accvgpr_read_b32 v203, a119\n"
"v_accvgpr_read_b32 v204, a144\n"
"v_mfma_f32_16x16x32_bf16 v[84:87], a[232:235], a[24:27], v[84:87]\n"
"ds_write_b128 v7, v[216:219] offset:32768\n"
"v_mfma_f32_16x16x32_bf16 v[76:79], a[232:235], a[20:23], 0\n"
"ds_read_b128 a[92:95], v6 offset:11264\n"
"v_accvgpr_read_b32 v205, a145\n"
"v_accvgpr_read_b32 v206, a146\n"
"v_mfma_f32_16x16x32_bf16 v[76:79], a[236:239], a[48:51], v[76:79]\n"
"ds_read_b128 a[96:99], v6 offset:15360\n"
"v_accvgpr_read_b32 v207, a147\n"
"v_mfma_f32_16x16x32_bf16 v[68:71], a[236:239], a[44:47], 0\n"
"ds_read_b128 a[68:71], v6 offset:13312\n"
"v_mul_f32 v200, v200, v20\n"
"v_mul_f32 v201, v201, v20\n"
"v_mfma_f32_16x16x32_bf16 v[68:71], a[232:235], a[16:19], v[68:71]\n"
"v_mul_f32 v202, v202, v20\n"
"v_mul_f32 v203, v203, v20\n"
"v_mul_f32 v204, v204, v20\n"
"v_mfma_f32_16x16x32_bf16 v[60:63], a[232:235], a[12:15], 0\n"
"ds_read_b128 a[72:75], v6 offset:17408\n"
"v_mul_f32 v205, v205, v20\n"
"v_mfma_f32_16x16x32_bf16 v[60:63], a[236:239], a[40:43], v[60:63]\n"
"ds_read_b128 a[100:103], v6 offset:19456\n"
"v_mul_f32 v206, v206, v20\n"
"v_mul_f32 v207, v207, v20\n"
"v_mfma_f32_16x16x32_bf16 v[52:55], a[236:239], a[36:39], 0\n"
"ds_read_b128 a[104:107], v6 offset:23552\n"
"v_cvt_pk_bf16_f32 v200, v200, v201\n"
"v_mfma_f32_16x16x32_bf16 v[52:55], a[232:235], a[8:11], v[52:55]\n"
"v_cvt_pk_bf16_f32 v201, v202, v203\n"
"v_cvt_pk_bf16_f32 v202, v204, v205\n"
"v_cvt_pk_bf16_f32 v203, v206, v207\n"
"v_mfma_f32_16x16x32_bf16 v[44:47], a[232:235], a[4:7], 0\n"
"ds_read_b128 a[76:79], v6 offset:21504\n"
"v_accvgpr_read_b32 v208, a120\n"
"v_accvgpr_read_b32 v209, a121\n"
"v_mfma_f32_16x16x32_bf16 v[44:47], a[236:239], a[32:35], v[44:47]\n"
"ds_read_b128 a[80:83], v6 offset:25600\n"
"v_accvgpr_read_b32 v210, a122\n"
"v_mfma_f32_16x16x32_bf16 v[36:39], a[236:239], a[28:31], 0\n"
"s_waitcnt lgkmcnt(13)\n"
"ds_read_b128 a[108:111], v6 offset:27648\n"
"v_accvgpr_read_b32 v211, a123\n"
"v_accvgpr_read_b32 v212, a148\n"
"v_mfma_f32_16x16x32_bf16 v[36:39], a[232:235], a[0:3], v[36:39]\n"
"ds_write_b128 v7, v[200:203] offset:36864\n"
"v_mfma_f32_16x16x32_bf16 v[32:35], a[240:243], a[56:59], v[32:35]\n"
"s_waitcnt lgkmcnt(13)\n"
"ds_read_b64_tr_b16 a[252:253], v3 offset:6144\n"
"v_accvgpr_read_b32 v213, a149\n"
"v_accvgpr_read_b32 v214, a150\n"
"v_mfma_f32_16x16x32_bf16 v[32:35], a[244:247], a[84:87], v[32:35]\n"
"ds_read_b64_tr_b16 a[254:255], v3 offset:7168\n"
"v_accvgpr_read_b32 v215, a151\n"
"v_mul_f32 v208, v208, v22\n"
"v_mfma_f32_16x16x32_bf16 v[40:43], a[244:247], a[88:91], v[40:43]\n"
"s_waitcnt lgkmcnt(13)\n"
"ds_read_b64_tr_b16 a[248:249], v3 offset:4096\n"
"v_mul_f32 v209, v209, v22\n"
"v_mfma_f32_16x16x32_bf16 v[40:43], a[240:243], a[60:63], v[40:43]\n"
"s_xor_b32 s74, s48, 98304\n"
"ds_read_b64_tr_b16 a[250:251], v3 offset:5120\n"
"v_mul_f32 v210, v210, v22\n"
"v_mul_f32 v211, v211, v22\n"
"v_mfma_f32_16x16x32_bf16 v[48:51], a[240:243], a[64:67], v[48:51]\n"
"s_waitcnt vmcnt(18) lgkmcnt(12)\n"
"ds_read_b64_tr_b16 a[224:225], v11 offset:0\n"
"v_mul_f32 v212, v212, v22\n"
"v_mfma_f32_16x16x32_bf16 v[48:51], a[244:247], a[92:95], v[48:51]\n"
"ds_read_b64_tr_b16 a[226:227], v11 offset:1024\n"
"v_mul_f32 v213, v213, v22\n"
"v_mul_f32 v214, v214, v22\n"
"v_mfma_f32_16x16x32_bf16 v[56:59], a[244:247], a[96:99], v[56:59]\n"
"s_waitcnt vmcnt(16) lgkmcnt(12)\n"
"ds_read_b64_tr_b16 a[228:229], v11 offset:2048\n"
"v_mul_f32 v215, v215, v22\n"
"v_cvt_pk_bf16_f32 v208, v208, v209\n"
"v_mfma_f32_16x16x32_bf16 v[56:59], a[240:243], a[68:71], v[56:59]\n"
"s_add_u32 m0, s46, 135168\n"
"v_cvt_pk_bf16_f32 v209, v210, v211\n"
"v_cvt_pk_bf16_f32 v210, v212, v213\n"
"v_mfma_f32_16x16x32_bf16 v[64:67], a[240:243], a[72:75], v[64:67]\n"
"s_waitcnt lgkmcnt(11)\n"
"ds_read_b64_tr_b16 a[230:231], v11 offset:3072\n"
"v_cvt_pk_bf16_f32 v211, v214, v215\n"
"v_accvgpr_read_b32 v216, a124\n"
"v_mfma_f32_16x16x32_bf16 v[64:67], a[244:247], a[100:103], v[64:67]\n"
"ds_read_b64_tr_b16 a[236:237], v12 offset:2048\n"
"v_accvgpr_read_b32 v217, a125\n"
"v_mfma_f32_16x16x32_bf16 v[72:75], a[244:247], a[104:107], v[72:75]\n"
"s_waitcnt lgkmcnt(5)\n"
"ds_read_b64_tr_b16 a[238:239], v12 offset:3072\n"
"v_accvgpr_read_b32 v218, a126\n"
"v_accvgpr_read_b32 v219, a127\n"
"v_mfma_f32_16x16x32_bf16 v[72:75], a[240:243], a[76:79], v[72:75]\n"
"buffer_load_dwordx4 v14, s[24:27], s74 offen lds\n"
"v_accvgpr_read_b32 v220, a152\n"
"v_accvgpr_read_b32 v221, a153\n"
"v_mfma_f32_16x16x32_bf16 v[80:83], a[240:243], a[80:83], v[80:83]\n"
"ds_read_b64_tr_b16 a[232:233], v12 offset:0\n"
"v_accvgpr_read_b32 v222, a154\n"
"v_mfma_f32_16x16x32_bf16 v[80:83], a[244:247], a[108:111], v[80:83]\n"
"ds_read_b64_tr_b16 a[234:235], v12 offset:1024\n"
"v_accvgpr_read_b32 v223, a155\n"
"v_mul_f32 v216, v216, v24\n"
"v_mfma_f32_16x16x32_bf16 v[84:87], a[252:255], a[108:111], v[84:87]\n"
"buffer_load_dwordx4 v15, s[24:27], s74 offen offset:1024 lds\n"
"v_mul_f32 v217, v217, v24\n"
"v_mfma_f32_16x16x32_bf16 v[84:87], a[248:251], a[80:83], v[84:87]\n"
"s_xor_b32 s75, s48, 65792\n"
"ds_write_b128 v7, v[208:211] offset:40960\n"
"v_mfma_f32_16x16x32_bf16 v[76:79], a[248:251], a[76:79], v[76:79]\n"
"v_mul_f32 v218, v218, v24\n"
"v_mul_f32 v219, v219, v24\n"
"v_mul_f32 v220, v220, v24\n"
"v_mfma_f32_16x16x32_bf16 v[76:79], a[252:255], a[104:107], v[76:79]\n"
"buffer_load_dwordx4 v21, s[24:27], s74 offen offset:2048 lds\n"
"v_mul_f32 v221, v221, v24\n"
"v_mfma_f32_16x16x32_bf16 v[68:71], a[252:255], a[100:103], v[68:71]\n"
"v_mul_f32 v222, v222, v24\n"
"v_mul_f32 v223, v223, v24\n"
"v_cvt_pk_bf16_f32 v216, v216, v217\n"
"v_mfma_f32_16x16x32_bf16 v[68:71], a[248:251], a[72:75], v[68:71]\n"
"v_cvt_pk_bf16_f32 v217, v218, v219\n"
"v_cvt_pk_bf16_f32 v218, v220, v221\n"
"v_mfma_f32_16x16x32_bf16 v[60:63], a[248:251], a[68:71], v[60:63]\n"
"buffer_load_dwordx4 v23, s[24:27], s74 offen offset:3072 lds\n"
"v_cvt_pk_bf16_f32 v219, v222, v223\n"
"v_accvgpr_read_b32 v200, a128\n"
"v_mfma_f32_16x16x32_bf16 v[60:63], a[252:255], a[96:99], v[60:63]\n"
"s_add_u32 m0, s46, 0\n"
"ds_write_b128 v7, v[216:219] offset:45056\n"
"v_mfma_f32_16x16x32_bf16 v[52:55], a[252:255], a[92:95], v[52:55]\n"
"s_waitcnt vmcnt(18) lgkmcnt(2)\n"
"ds_read_b64_tr_b16 a[240:241], v11 offset:4096\n"
"v_accvgpr_read_b32 v201, a129\n"
"v_mfma_f32_16x16x32_bf16 v[52:55], a[248:251], a[64:67], v[52:55]\n"
"buffer_load_dwordx4 v14, s[24:27], s75 offen lds\n"
"v_accvgpr_read_b32 v202, a130\n"
"v_accvgpr_read_b32 v203, a131\n"
"v_mfma_f32_16x16x32_bf16 v[44:47], a[248:251], a[60:63], v[44:47]\n"
"ds_read_b64_tr_b16 a[242:243], v11 offset:5120\n"
"v_accvgpr_read_b32 v204, a156\n"
"v_mfma_f32_16x16x32_bf16 v[44:47], a[252:255], a[88:91], v[44:47]\n"
"v_accvgpr_read_b32 v205, a157\n"
"v_accvgpr_read_b32 v206, a158\n"
"v_accvgpr_read_b32 v207, a159\n"
"v_mfma_f32_16x16x32_bf16 v[36:39], a[252:255], a[84:87], v[36:39]\n"
"s_waitcnt vmcnt(17)\n"
"ds_read_b64_tr_b16 a[244:245], v11 offset:6144\n"
"v_mul_f32 v200, v200, v26\n"
"v_mul_f32 v201, v201, v26\n"
"v_mfma_f32_16x16x32_bf16 v[36:39], a[248:251], a[56:59], v[36:39]\n"
"buffer_load_dwordx4 v15, s[24:27], s75 offen offset:1024 lds\n"
"v_mul_f32 v202, v202, v26\n"
"v_mfma_f32_16x16x32_bf16 v[88:91], a[224:227], a[0:3], 0\n"
"ds_read_b64_tr_b16 a[246:247], v11 offset:7168\n"
"v_mul_f32 v203, v203, v26\n"
"v_mul_f32 v204, v204, v26\n"
"v_mfma_f32_16x16x32_bf16 v[88:91], a[228:231], a[28:31], v[88:91]\n"
"v_mul_f32 v205, v205, v26\n"
"v_mul_f32 v206, v206, v26\n"
"v_mfma_f32_16x16x32_bf16 v[96:99], a[228:231], a[32:35], 0\n"
"buffer_load_dwordx4 v21, s[24:27], s75 offen offset:2048 lds\n"
"v_mul_f32 v207, v207, v26\n"
"v_cvt_pk_bf16_f32 v200, v200, v201\n"
"v_mfma_f32_16x16x32_bf16 v[96:99], a[224:227], a[4:7], v[96:99]\n"
"v_cvt_pk_bf16_f32 v201, v202, v203\n"
"v_cvt_pk_bf16_f32 v202, v204, v205\n"
"v_cvt_pk_bf16_f32 v203, v206, v207\n"
"v_mfma_f32_16x16x32_bf16 v[104:107], a[224:227], a[8:11], 0\n"
"buffer_load_dwordx2 v[16:17], off, s[20:23], s61 offset:128 sc1\n"
"v_accvgpr_read_b32 v208, a132\n"
"v_mfma_f32_16x16x32_bf16 v[104:107], a[228:231], a[36:39], v[104:107]\n"
"buffer_load_dwordx4 v23, s[24:27], s75 offen offset:3072 lds\n"
"v_accvgpr_read_b32 v209, a133\n"
"v_accvgpr_read_b32 v210, a134\n"
"v_mfma_f32_16x16x32_bf16 v[112:115], a[228:231], a[40:43], 0\n"
"ds_write_b128 v7, v[200:203] offset:49152\n"
"v_mfma_f32_16x16x32_bf16 v[112:115], a[224:227], a[12:15], v[112:115]\n"
"v_accvgpr_read_b32 v211, a135\n"
"v_accvgpr_read_b32 v212, a160\n"
"v_accvgpr_read_b32 v213, a161\n"
"v_mfma_f32_16x16x32_bf16 v[120:123], a[224:227], a[16:19], 0\n"
"ds_read_b64_tr_b16 a[252:253], v12 offset:6144\n"
"v_accvgpr_read_b32 v214, a162\n"
"v_accvgpr_read_b32 v215, a163\n"
"v_mfma_f32_16x16x32_bf16 v[120:123], a[228:231], a[44:47], v[120:123]\n"
"ds_read_b64_tr_b16 a[254:255], v12 offset:7168\n"
"v_mul_f32 v208, v208, v28\n"
"v_mfma_f32_16x16x32_bf16 v[128:131], a[228:231], a[48:51], 0\n"
"ds_read_b64_tr_b16 a[248:249], v12 offset:4096\n"
"v_mul_f32 v209, v209, v28\n"
"v_mul_f32 v210, v210, v28\n"
"v_mfma_f32_16x16x32_bf16 v[128:131], a[224:227], a[20:23], v[128:131]\n"
"s_xor_b32 s74, s48, 98560\n"
"v_mul_f32 v211, v211, v28\n"
"v_mul_f32 v212, v212, v28\n"
"v_mfma_f32_16x16x32_bf16 v[136:139], a[224:227], a[24:27], 0\n"
"ds_read_b64_tr_b16 a[250:251], v12 offset:5120\n"
"v_mul_f32 v213, v213, v28\n"
"v_mul_f32 v214, v214, v28\n"
"v_mfma_f32_16x16x32_bf16 v[136:139], a[228:231], a[52:55], v[136:139]\n"
"v_mul_f32 v215, v215, v28\n"
"v_cvt_pk_bf16_f32 v208, v208, v209\n"
"v_cvt_pk_bf16_f32 v209, v210, v211\n"
"v_mfma_f32_16x16x32_bf16 v[140:143], a[236:239], a[52:55], 0\n"
"v_cvt_pk_bf16_f32 v210, v212, v213\n"
"v_cvt_pk_bf16_f32 v211, v214, v215\n"
"v_mfma_f32_16x16x32_bf16 v[140:143], a[232:235], a[24:27], v[140:143]\n"
"ds_write_b128 v7, v[208:211] offset:53248\n"
"v_mfma_f32_16x16x32_bf16 v[132:135], a[232:235], a[20:23], 0\n"
"v_accvgpr_read_b32 v216, a136\n"
"v_accvgpr_read_b32 v217, a137\n"
"v_mfma_f32_16x16x32_bf16 v[132:135], a[236:239], a[48:51], v[132:135]\n"
"s_add_u32 m0, s46, 4096\n"
"v_accvgpr_read_b32 v218, a138\n"
"v_accvgpr_read_b32 v219, a139\n"
"v_accvgpr_read_b32 v220, a164\n"
"v_mfma_f32_16x16x32_bf16 v[124:127], a[236:239], a[44:47], 0\n"
"s_waitcnt lgkmcnt(1)\n"
"v_accvgpr_read_b32 v221, a165\n"
"v_accvgpr_read_b32 v222, a166\n"
"v_accvgpr_read_b32 v223, a167\n"
"v_mfma_f32_16x16x32_bf16 v[124:127], a[232:235], a[16:19], v[124:127]\n"
"buffer_load_dwordx4 v14, s[24:27], s74 offen lds\n"
"v_mul_f32 v216, v216, v30\n"
"v_mfma_f32_16x16x32_bf16 v[116:119], a[232:235], a[12:15], 0\n"
"v_mul_f32 v217, v217, v30\n"
"v_mul_f32 v218, v218, v30\n"
"v_mul_f32 v219, v219, v30\n"
"v_mfma_f32_16x16x32_bf16 v[116:119], a[236:239], a[40:43], v[116:119]\n"
"v_mul_f32 v220, v220, v30\n"
"v_mul_f32 v221, v221, v30\n"
"v_mfma_f32_16x16x32_bf16 v[108:111], a[236:239], a[36:39], 0\n"
"s_waitcnt vmcnt(20)\n"
"ds_read_b64_tr_b16 a[224:225], v11 offset:32768\n"
"v_mul_f32 v222, v222, v30\n"
"v_mul_f32 v223, v223, v30\n"
"v_mfma_f32_16x16x32_bf16 v[108:111], a[232:235], a[8:11], v[108:111]\n"
"buffer_load_dwordx4 v15, s[24:27], s74 offen offset:1024 lds\n"
"v_cvt_pk_bf16_f32 v216, v216, v217\n"
"v_cvt_pk_bf16_f32 v217, v218, v219\n"
"v_mfma_f32_16x16x32_bf16 v[100:103], a[232:235], a[4:7], 0\n"
"ds_read_b64_tr_b16 a[226:227], v11 offset:33792\n"
"v_cvt_pk_bf16_f32 v218, v220, v221\n"
"v_mfma_f32_16x16x32_bf16 v[100:103], a[236:239], a[32:35], v[100:103]\n"
"v_cvt_pk_bf16_f32 v219, v222, v223\n"
"v_accvgpr_read_b32 v200, a168\n"
"v_accvgpr_read_b32 v201, a169\n"
"v_mfma_f32_16x16x32_bf16 v[92:95], a[236:239], a[28:31], 0\n"
"s_waitcnt vmcnt(19)\n"
"ds_read_b64_tr_b16 a[228:229], v11 offset:34816\n"
"v_accvgpr_read_b32 v202, a170\n"
"v_mfma_f32_16x16x32_bf16 v[92:95], a[232:235], a[0:3], v[92:95]\n"
"buffer_load_dwordx4 v21, s[24:27], s74 offen offset:2048 lds\n"
"v_accvgpr_read_b32 v203, a171\n"
"v_accvgpr_read_b32 v204, a196\n"
"v_mfma_f32_16x16x32_bf16 v[88:91], a[240:243], a[56:59], v[88:91]\n"
"ds_read_b64_tr_b16 a[230:231], v11 offset:35840\n"
"v_accvgpr_read_b32 v205, a197\n"
"v_accvgpr_read_b32 v206, a198\n"
"v_mfma_f32_16x16x32_bf16 v[88:91], a[244:247], a[84:87], v[88:91]\n"
"ds_write_b128 v7, v[216:219] offset:57344\n"
"v_mfma_f32_16x16x32_bf16 v[96:99], a[244:247], a[88:91], v[96:99]\n"
"buffer_load_dwordx4 v23, s[24:27], s74 offen offset:3072 lds\n"
"v_accvgpr_read_b32 v207, a199\n"
"v_mul_f32 v200, v200, v18\n"
"v_mfma_f32_16x16x32_bf16 v[96:99], a[240:243], a[60:63], v[96:99]\n"
"v_mul_f32 v201, v201, v18\n"
"v_mul_f32 v202, v202, v18\n"
"v_mfma_f32_16x16x32_bf16 v[104:107], a[240:243], a[64:67], v[104:107]\n"
"v_mul_f32 v203, v203, v18\n"
"v_mul_f32 v204, v204, v18\n"
"v_mul_f32 v205, v205, v18\n"
"v_mfma_f32_16x16x32_bf16 v[104:107], a[244:247], a[92:95], v[104:107]\n"
"v_mul_f32 v206, v206, v18\n"
"v_mul_f32 v207, v207, v18\n"
"v_cvt_pk_bf16_f32 v200, v200, v201\n"
"v_mfma_f32_16x16x32_bf16 v[112:115], a[244:247], a[96:99], v[112:115]\n"
"v_cvt_pk_bf16_f32 v201, v202, v203\n"
"v_cvt_pk_bf16_f32 v202, v204, v205\n"
"v_mfma_f32_16x16x32_bf16 v[112:115], a[240:243], a[68:71], v[112:115]\n"
"v_cvt_pk_bf16_f32 v203, v206, v207\n"
"v_accvgpr_read_b32 v208, a172\n"
"v_accvgpr_read_b32 v209, a173\n"
"v_mfma_f32_16x16x32_bf16 v[120:123], a[240:243], a[72:75], v[120:123]\n"
"ds_read_b64_tr_b16 a[236:237], v12 offset:34816\n"
"v_accvgpr_read_b32 v210, a174\n"
"v_mfma_f32_16x16x32_bf16 v[120:123], a[244:247], a[100:103], v[120:123]\n"
"ds_read_b64_tr_b16 a[238:239], v12 offset:35840\n"
"v_accvgpr_read_b32 v211, a175\n"
"v_accvgpr_read_b32 v212, a200\n"
"v_mfma_f32_16x16x32_bf16 v[128:131], a[244:247], a[104:107], v[128:131]\n"
"ds_read_b64_tr_b16 a[232:233], v12 offset:32768\n"
"v_accvgpr_read_b32 v213, a201\n"
"v_accvgpr_read_b32 v214, a202\n"
"v_mfma_f32_16x16x32_bf16 v[128:131], a[240:243], a[76:79], v[128:131]\n"
"ds_write_b128 v7, v[200:203] offset:33792\n"
"v_mfma_f32_16x16x32_bf16 v[136:139], a[240:243], a[80:83], v[136:139]\n"
"ds_read_b64_tr_b16 a[234:235], v12 offset:33792\n"
"v_accvgpr_read_b32 v215, a203\n"
"v_mul_f32 v208, v208, v20\n"
"v_mfma_f32_16x16x32_bf16 v[136:139], a[244:247], a[108:111], v[136:139]\n"
"v_mul_f32 v209, v209, v20\n"
"v_mul_f32 v210, v210, v20\n"
"v_mfma_f32_16x16x32_bf16 v[140:143], a[252:255], a[108:111], v[140:143]\n"
"v_mul_f32 v211, v211, v20\n"
"v_mul_f32 v212, v212, v20\n"
"v_mul_f32 v213, v213, v20\n"
"v_mfma_f32_16x16x32_bf16 v[140:143], a[248:251], a[80:83], v[140:143]\n"
"v_mul_f32 v214, v214, v20\n"
"v_mul_f32 v215, v215, v20\n"
"v_cvt_pk_bf16_f32 v208, v208, v209\n"
"v_mfma_f32_16x16x32_bf16 v[132:135], a[248:251], a[76:79], v[132:135]\n"
"s_waitcnt vmcnt(5) lgkmcnt(5)\n"
"s_barrier\n"
"v_cmp_ne_u64 vcc, s[68:69], v[16:17]\n"
"s_cbranch_vccnz 61f\n"
"71:\n"
"v_mfma_f32_16x16x32_bf16 v[132:135], a[252:255], a[104:107], v[132:135]\n"
"s_xor_b32 s76, s51, 0\n"
"buffer_load_dwordx4 v[224:227], v1, s[20:23], s76 offen sc1\n"
"v_cvt_pk_bf16_f32 v209, v210, v211\n"
"v_cvt_pk_bf16_f32 v210, v212, v213\n"
"v_mfma_f32_16x16x32_bf16 v[124:127], a[252:255], a[100:103], v[124:127]\n"
"v_cvt_pk_bf16_f32 v211, v214, v215\n"
"v_accvgpr_read_b32 v216, a176\n"
"v_mfma_f32_16x16x32_bf16 v[124:127], a[248:251], a[72:75], v[124:127]\n"
"ds_write_b128 v7, v[208:211] offset:37888\n"
"v_mfma_f32_16x16x32_bf16 v[116:119], a[248:251], a[68:71], v[116:119]\n"
"v_accvgpr_read_b32 v217, a177\n"
"v_accvgpr_read_b32 v218, a178\n"
"v_accvgpr_read_b32 v219, a179\n"
"v_mfma_f32_16x16x32_bf16 v[116:119], a[252:255], a[96:99], v[116:119]\n"
"buffer_load_dwordx4 v[228:231], v1, s[20:23], s76 offen offset:1024 sc1\n"
"v_accvgpr_read_b32 v220, a204\n"
"v_mfma_f32_16x16x32_bf16 v[108:111], a[252:255], a[92:95], v[108:111]\n"
"ds_read_b64_tr_b16 a[240:241], v11 offset:36864\n"
"v_accvgpr_read_b32 v221, a205\n"
"v_accvgpr_read_b32 v222, a206\n"
"v_mfma_f32_16x16x32_bf16 v[108:111], a[248:251], a[64:67], v[108:111]\n"
"v_accvgpr_read_b32 v223, a207\n"
"v_mul_f32 v216, v216, v22\n"
"v_mfma_f32_16x16x32_bf16 v[100:103], a[248:251], a[60:63], v[100:103]\n"
"ds_read_b64_tr_b16 a[242:243], v11 offset:37888\n"
"v_mul_f32 v217, v217, v22\n"
"v_mul_f32 v218, v218, v22\n"
"v_mfma_f32_16x16x32_bf16 v[100:103], a[252:255], a[88:91], v[100:103]\n"
"ds_read_b128 a[112:115], v6 offset:32768\n"
"v_mul_f32 v219, v219, v22\n"
"v_mul_f32 v220, v220, v22\n"
"v_mfma_f32_16x16x32_bf16 v[92:95], a[252:255], a[84:87], v[92:95]\n"
"ds_read_b64_tr_b16 a[244:245], v11 offset:38912\n"
"v_mul_f32 v221, v221, v22\n"
"v_mfma_f32_16x16x32_bf16 v[92:95], a[248:251], a[56:59], v[92:95]\n"
"buffer_load_dwordx4 v[232:235], v1, s[20:23], s76 offen offset:2048 sc1\n"
"v_mul_f32 v222, v222, v22\n"
"v_mul_f32 v223, v223, v22\n"
"v_mfma_f32_16x16x32_bf16 v[144:147], a[224:227], a[0:3], 0\n"
"ds_read_b64_tr_b16 a[246:247], v11 offset:39936\n"
"v_cvt_pk_bf16_f32 v216, v216, v217\n"
"v_mfma_f32_16x16x32_bf16 v[144:147], a[228:231], a[28:31], v[144:147]\n"
"ds_read_b128 a[140:143], v6 offset:34816\n"
"v_cvt_pk_bf16_f32 v217, v218, v219\n"
"v_cvt_pk_bf16_f32 v218, v220, v221\n"
"v_mfma_f32_16x16x32_bf16 v[152:155], a[228:231], a[32:35], 0\n"
"ds_read_b128 a[144:147], v6 offset:38912\n"
"v_cvt_pk_bf16_f32 v219, v222, v223\n"
"v_accvgpr_read_b32 v200, a180\n"
"v_mfma_f32_16x16x32_bf16 v[152:155], a[224:227], a[4:7], v[152:155]\n"
"buffer_load_dwordx4 v[236:239], v1, s[20:23], s76 offen offset:3072 sc1\n"
"v_accvgpr_read_b32 v201, a181\n"
"v_mfma_f32_16x16x32_bf16 v[160:163], a[224:227], a[8:11], 0\n"
"ds_read_b128 a[116:119], v6 offset:36864\n"
"v_accvgpr_read_b32 v202, a182\n"
"v_accvgpr_read_b32 v203, a183\n"
"v_mfma_f32_16x16x32_bf16 v[160:163], a[228:231], a[36:39], v[160:163]\n"
"ds_read_b128 a[120:123], v6 offset:40960\n"
"v_accvgpr_read_b32 v204, a208\n"
"v_mfma_f32_16x16x32_bf16 v[168:171], a[228:231], a[40:43], 0\n"
"ds_read_b128 a[148:151], v6 offset:43008\n"
"v_accvgpr_read_b32 v205, a209\n"
"v_accvgpr_read_b32 v206, a210\n"
"v_mfma_f32_16x16x32_bf16 v[168:171], a[224:227], a[12:15], v[168:171]\n"
"s_xor_b32 s76, s51, 4096\n"
"buffer_load_dwordx4 v[240:243], v1, s[20:23], s76 offen sc1\n"
"v_accvgpr_read_b32 v207, a211\n"
"v_mul_f32 v200, v200, v24\n"
"v_mfma_f32_16x16x32_bf16 v[176:179], a[224:227], a[16:19], 0\n"
"ds_read_b64_tr_b16 a[252:253], v12 offset:38912\n"
"v_mul_f32 v201, v201, v24\n"
"v_mfma_f32_16x16x32_bf16 v[176:179], a[228:231], a[44:47], v[176:179]\n"
"ds_read_b64_tr_b16 a[254:255], v12 offset:39936\n"
"v_mul_f32 v202, v202, v24\n"
"v_mul_f32 v203, v203, v24\n"
"v_mfma_f32_16x16x32_bf16 v[184:187], a[228:231], a[48:51], 0\n"
"ds_read_b64_tr_b16 a[248:249], v12 offset:36864\n"
"v_mul_f32 v204, v204, v24\n"
"v_mfma_f32_16x16x32_bf16 v[184:187], a[224:227], a[20:23], v[184:187]\n"
"buffer_load_dwordx4 v[244:247], v1, s[20:23], s76 offen offset:1024 sc1\n"
"v_mul_f32 v205, v205, v24\n"
"v_mul_f32 v206, v206, v24\n"
"v_mfma_f32_16x16x32_bf16 v[192:195], a[224:227], a[24:27], 0\n"
"s_waitcnt lgkmcnt(15)\n"
"ds_read_b64_tr_b16 a[250:251], v12 offset:37888\n"
"v_mul_f32 v207, v207, v24\n"
"v_cvt_pk_bf16_f32 v200, v200, v201\n"
"v_mfma_f32_16x16x32_bf16 v[192:195], a[228:231], a[52:55], v[192:195]\n"
"ds_read_b128 a[152:155], v6 offset:47104\n"
"v_cvt_pk_bf16_f32 v201, v202, v203\n"
"v_mfma_f32_16x16x32_bf16 v[196:199], a[236:239], a[52:55], 0\n"
"s_waitcnt lgkmcnt(15)\n"
"ds_read_b128 a[124:127], v6 offset:45056\n"
"v_cvt_pk_bf16_f32 v202, v204, v205\n"
"v_cvt_pk_bf16_f32 v203, v206, v207\n"
"v_mfma_f32_16x16x32_bf16 v[196:199], a[232:235], a[24:27], v[196:199]\n"
"buffer_load_dwordx4 v[248:251], v1, s[20:23], s76 offen offset:2048 sc1\n"
"v_accvgpr_read_b32 v208, a184\n"
"v_mfma_f32_16x16x32_bf16 v[188:191], a[232:235], a[20:23], 0\n"
"ds_read_b128 a[128:131], v6 offset:49152\n"
"v_accvgpr_read_b32 v209, a185\n"
"v_accvgpr_read_b32 v210, a186\n"
"v_mfma_f32_16x16x32_bf16 v[188:191], a[236:239], a[48:51], v[188:191]\n"
"ds_read_b128 a[156:159], v6 offset:51200\n"
"v_accvgpr_read_b32 v211, a187\n"
"v_accvgpr_read_b32 v212, a212\n"
"v_mfma_f32_16x16x32_bf16 v[180:183], a[236:239], a[44:47], 0\n"
"ds_read_b128 a[160:163], v6 offset:55296\n"
"v_accvgpr_read_b32 v213, a213\n"
"v_mfma_f32_16x16x32_bf16 v[180:183], a[232:235], a[16:19], v[180:183]\n"
"ds_write_b128 v7, v[216:219] offset:41984\n"
"v_mfma_f32_16x16x32_bf16 v[172:175], a[232:235], a[12:15], 0\n"
"ds_read_b128 a[132:135], v6 offset:53248\n"
"v_accvgpr_read_b32 v214, a214\n"
"v_mfma_f32_16x16x32_bf16 v[172:175], a[236:239], a[40:43], v[172:175]\n"
"ds_read_b128 a[136:139], v6 offset:57344\n"
"v_accvgpr_read_b32 v215, a215\n"
"v_mul_f32 v208, v208, v26\n"
"v_mfma_f32_16x16x32_bf16 v[164:167], a[236:239], a[36:39], 0\n"
"ds_read_b64_tr_b16 a[224:225], v2 offset:0\n"
"v_mul_f32 v209, v209, v26\n"
"v_mul_f32 v210, v210, v26\n"
"v_mfma_f32_16x16x32_bf16 v[164:167], a[232:235], a[8:11], v[164:167]\n"
"ds_write_b128 v7, v[200:203] offset:46080\n"
"v_mfma_f32_16x16x32_bf16 v[156:159], a[232:235], a[4:7], 0\n"
"ds_read_b64_tr_b16 a[226:227], v2 offset:1024\n"
"v_mul_f32 v211, v211, v26\n"
"v_mul_f32 v212, v212, v26\n"
"v_mfma_f32_16x16x32_bf16 v[156:159], a[236:239], a[32:35], v[156:159]\n"
"ds_read_b128 a[164:167], v6 offset:59392\n"
"v_mul_f32 v213, v213, v26\n"
"v_mfma_f32_16x16x32_bf16 v[148:151], a[236:239], a[28:31], 0\n"
"s_waitcnt lgkmcnt(15)\n"
"ds_read_b64_tr_b16 a[228:229], v2 offset:2048\n"
"v_mul_f32 v214, v214, v26\n"
"v_mul_f32 v215, v215, v26\n"
"v_mfma_f32_16x16x32_bf16 v[148:151], a[232:235], a[0:3], v[148:151]\n"
"v_cvt_pk_bf16_f32 v208, v208, v209\n"
"v_cvt_pk_bf16_f32 v209, v210, v211\n"
"v_cvt_pk_bf16_f32 v210, v212, v213\n"
"v_mfma_f32_16x16x32_bf16 v[144:147], a[240:243], a[56:59], v[144:147]\n"
"s_waitcnt lgkmcnt(15)\n"
"ds_read_b64_tr_b16 a[230:231], v2 offset:3072\n"
"v_cvt_pk_bf16_f32 v211, v214, v215\n"
"v_mfma_f32_16x16x32_bf16 v[144:147], a[244:247], a[84:87], v[144:147]\n"
"ds_write_b128 v7, v[208:211] offset:50176\n"
"v_mfma_f32_16x16x32_bf16 v[152:155], a[244:247], a[88:91], v[152:155]\n"
"v_accvgpr_read_b32 v216, a188\n"
"v_accvgpr_read_b32 v217, a189\n"
"v_mfma_f32_16x16x32_bf16 v[152:155], a[240:243], a[60:63], v[152:155]\n"
"v_accvgpr_read_b32 v218, a190\n"
"v_accvgpr_read_b32 v219, a191\n"
"v_accvgpr_read_b32 v220, a216\n"
"v_mfma_f32_16x16x32_bf16 v[160:163], a[240:243], a[64:67], v[160:163]\n"
"v_accvgpr_read_b32 v221, a217\n"
"v_accvgpr_read_b32 v222, a218\n"
"v_accvgpr_read_b32 v223, a219\n"
"v_mfma_f32_16x16x32_bf16 v[160:163], a[244:247], a[92:95], v[160:163]\n"
"v_mul_f32 v216, v216, v28\n"
"v_mul_f32 v217, v217, v28\n"
"v_mfma_f32_16x16x32_bf16 v[168:171], a[244:247], a[96:99], v[168:171]\n"
"v_mul_f32 v218, v218, v28\n"
"v_mul_f32 v219, v219, v28\n"
"v_mul_f32 v220, v220, v28\n"
"v_mfma_f32_16x16x32_bf16 v[168:171], a[240:243], a[68:71], v[168:171]\n"
"v_mul_f32 v221, v221, v28\n"
"v_mul_f32 v222, v222, v28\n"
"v_mfma_f32_16x16x32_bf16 v[176:179], a[240:243], a[72:75], v[176:179]\n"
"ds_read_b64_tr_b16 a[236:237], v3 offset:2048\n"
"v_mul_f32 v223, v223, v28\n"
"v_cvt_pk_bf16_f32 v216, v216, v217\n"
"v_mfma_f32_16x16x32_bf16 v[176:179], a[244:247], a[100:103], v[176:179]\n"
"ds_read_b64_tr_b16 a[238:239], v3 offset:3072\n"
"v_cvt_pk_bf16_f32 v217, v218, v219\n"
"v_cvt_pk_bf16_f32 v218, v220, v221\n"
"v_mfma_f32_16x16x32_bf16 v[184:187], a[244:247], a[104:107], v[184:187]\n"
"ds_read_b64_tr_b16 a[232:233], v3 offset:0\n"
"v_cvt_pk_bf16_f32 v219, v222, v223\n"
"v_mfma_f32_16x16x32_bf16 v[184:187], a[240:243], a[76:79], v[184:187]\n"
"s_xor_b32 s75, s48, 262144\n"
"ds_write_b128 v7, v[216:219] offset:54272\n"
"v_mfma_f32_16x16x32_bf16 v[192:195], a[240:243], a[80:83], v[192:195]\n"
"s_waitcnt lgkmcnt(15)\n"
"ds_read_b64_tr_b16 a[234:235], v3 offset:1024\n"
"v_accvgpr_read_b32 v200, a192\n"
"v_mfma_f32_16x16x32_bf16 v[192:195], a[244:247], a[108:111], v[192:195]\n"
"v_accvgpr_read_b32 v201, a193\n"
"v_accvgpr_read_b32 v202, a194\n"
"v_accvgpr_read_b32 v203, a195\n"
"v_mfma_f32_16x16x32_bf16 v[196:199], a[252:255], a[108:111], v[196:199]\n"
"s_waitcnt lgkmcnt(15)\n"
"v_accvgpr_read_b32 v204, a220\n"
"v_accvgpr_read_b32 v205, a221\n"
"v_accvgpr_read_b32 v206, a222\n"
"v_mfma_f32_16x16x32_bf16 v[196:199], a[248:251], a[80:83], v[196:199]\n"
"v_accvgpr_read_b32 v207, a223\n"
"v_mul_f32 v200, v200, v30\n"
"v_mfma_f32_16x16x32_bf16 v[188:191], a[248:251], a[76:79], v[188:191]\n"
"v_mul_f32 v201, v201, v30\n"
"v_mul_f32 v202, v202, v30\n"
"v_mul_f32 v203, v203, v30\n"
"v_mfma_f32_16x16x32_bf16 v[188:191], a[252:255], a[104:107], v[188:191]\n"
"s_add_u32 m0, s46, 131072\n"
"v_mul_f32 v204, v204, v30\n"
"v_mul_f32 v205, v205, v30\n"
"v_mfma_f32_16x16x32_bf16 v[180:183], a[252:255], a[100:103], v[180:183]\n"
"s_waitcnt lgkmcnt(0)\n"
"v_mul_f32 v206, v206, v30\n"
"v_mul_f32 v207, v207, v30\n"
"v_cvt_pk_bf16_f32 v200, v200, v201\n"
"v_mfma_f32_16x16x32_bf16 v[180:183], a[248:251], a[72:75], v[180:183]\n"
"buffer_load_dwordx4 v14, s[24:27], s75 offen lds\n"
"v_cvt_pk_bf16_f32 v201, v202, v203\n"
"v_cvt_pk_bf16_f32 v202, v204, v205\n"
"v_mfma_f32_16x16x32_bf16 v[172:175], a[248:251], a[68:71], v[172:175]\n"
"v_cvt_pk_bf16_f32 v203, v206, v207\n"
"v_add_u32 v4, s46, v1\n"
"v_mfma_f32_16x16x32_bf16 v[172:175], a[252:255], a[96:99], v[172:175]\n"
"ds_write_b128 v7, v[200:203] offset:58368\n"
"v_mfma_f32_16x16x32_bf16 v[164:167], a[252:255], a[92:95], v[164:167]\n"
"ds_read_b64_tr_b16 a[240:241], v2 offset:4096\n"
"v_lshlrev_b32 v5, 14, v1\n"
"v_mfma_f32_16x16x32_bf16 v[164:167], a[248:251], a[64:67], v[164:167]\n"
"buffer_load_dwordx4 v15, s[24:27], s75 offen offset:1024 lds\n"
"v_and_b32 v5, 262144, v5\n"
"v_mfma_f32_16x16x32_bf16 v[156:159], a[248:251], a[60:63], v[156:159]\n"
"ds_read_b64_tr_b16 a[242:243], v2 offset:5120\n"
"v_xor_b32 v10, 128, v1\n"
"v_mfma_f32_16x16x32_bf16 v[156:159], a[252:255], a[88:91], v[156:159]\n"
"v_lshrrev_b32 v18, 8, v1\n"
"v_lshrrev_b32 v20, 5, v1\n"
"v_mfma_f32_16x16x32_bf16 v[148:151], a[252:255], a[84:87], v[148:151]\n"
"s_waitcnt lgkmcnt(2)\n"
"s_barrier\n"
"ds_read_b64_tr_b16 a[244:245], v2 offset:6144\n"
"v_mfma_f32_16x16x32_bf16 v[148:151], a[248:251], a[56:59], v[148:151]\n"
"buffer_load_dwordx4 v21, s[24:27], s75 offen offset:2048 lds\n"
"v_and_b32 v20, 2, v20\n"
"v_mfma_f32_16x16x32_bf16 v[32:35], a[224:227], a[112:115], v[32:35]\n"
"ds_read_b128 a[168:171], v6 offset:33792\n"
"v_xor_b32 v18, v18, v20\n"
"v_mfma_f32_16x16x32_bf16 v[32:35], a[228:231], a[140:143], v[32:35]\n"
"ds_read_b128 a[196:199], v6 offset:35840\n"
"v_and_b32 v20, 16, v1\n"
"v_mfma_f32_16x16x32_bf16 v[40:43], a[228:231], a[144:147], v[40:43]\n"
"ds_read_b64_tr_b16 a[246:247], v2 offset:7168\n"
"v_mov_b32 v22, 16256\n"
"v_mfma_f32_16x16x32_bf16 v[40:43], a[224:227], a[116:119], v[40:43]\n"
"buffer_load_dwordx4 v23, s[24:27], s75 offen offset:3072 lds\n"
"v_lshlrev_b32 v20, v20, v22\n"
"v_mfma_f32_16x16x32_bf16 v[48:51], a[224:227], a[120:123], v[48:51]\n"
"ds_read_b128 a[200:203], v6 offset:39936\n"
"v_lshlrev_b32 v18, 2, v18\n"
"v_mfma_f32_16x16x32_bf16 v[48:51], a[228:231], a[148:151], v[48:51]\n"
"ds_read_b128 a[172:175], v6 offset:37888\n"
"v_bfe_u32 v22, v1, 5, 1\n"
"v_mfma_f32_16x16x32_bf16 v[56:59], a[228:231], a[152:155], v[56:59]\n"
"ds_read_b128 a[176:179], v6 offset:41984\n"
"v_or_b32 v18, v18, v22\n"
"v_mfma_f32_16x16x32_bf16 v[56:59], a[224:227], a[124:127], v[56:59]\n"
"v_bfe_u32 v22, v1, 7, 1\n"
"v_lshl_or_b32 v18, v22, 1, v18\n"
"v_mfma_f32_16x16x32_bf16 v[64:67], a[224:227], a[128:131], v[64:67]\n"
"ds_read_b128 a[204:207], v6 offset:44032\n"
"v_cmp_eq_u32 s[70:71], 0, v18\n"
"v_mfma_f32_16x16x32_bf16 v[64:67], a[228:231], a[156:159], v[64:67]\n"
"ds_read_b128 a[208:211], v6 offset:48128\n"
"v_mfma_f32_16x16x32_bf16 v[72:75], a[228:231], a[160:163], v[72:75]\n"
"ds_read_b128 a[180:183], v6 offset:46080\n"
"v_cndmask_b32 v24, 0, v20, s[70:71]\n"
"v_mfma_f32_16x16x32_bf16 v[72:75], a[224:227], a[132:135], v[72:75]\n"
"v_cmp_eq_u32 s[70:71], 1, v18\n"
"v_mfma_f32_16x16x32_bf16 v[80:83], a[224:227], a[136:139], v[80:83]\n"
"ds_read_b128 a[184:187], v6 offset:50176\n"
"v_mfma_f32_16x16x32_bf16 v[80:83], a[228:231], a[164:167], v[80:83]\n"
"ds_read_b128 a[212:215], v6 offset:52224\n"
"v_cndmask_b32 v25, 0, v20, s[70:71]\n"
"v_mfma_f32_16x16x32_bf16 v[84:87], a[236:239], a[164:167], v[84:87]\n"
"ds_read_b128 a[216:219], v6 offset:56320\n"
"v_cmp_eq_u32 s[70:71], 2, v18\n"
"v_mfma_f32_16x16x32_bf16 v[84:87], a[232:235], a[136:139], v[84:87]\n"
"v_mfma_f32_16x16x32_bf16 v[76:79], a[232:235], a[132:135], v[76:79]\n"
"ds_read_b128 a[188:191], v6 offset:54272\n"
"v_cndmask_b32 v26, 0, v20, s[70:71]\n"
"v_mfma_f32_16x16x32_bf16 v[76:79], a[236:239], a[160:163], v[76:79]\n"
"ds_read_b128 a[192:195], v6 offset:58368\n"
"v_cmp_eq_u32 s[70:71], 3, v18\n"
"v_mfma_f32_16x16x32_bf16 v[68:71], a[236:239], a[156:159], v[68:71]\n"
"s_waitcnt vmcnt(10)\n"
"ds_read_b128 a[220:223], v6 offset:60416\n"
"v_mfma_f32_16x16x32_bf16 v[68:71], a[232:235], a[128:131], v[68:71]\n"
"ds_write_b128 v4, v[224:227] offset:32768\n"
"v_mfma_f32_16x16x32_bf16 v[60:63], a[232:235], a[124:127], v[60:63]\n"
"ds_read_b64_tr_b16 a[252:253], v3 offset:6144\n"
"v_cndmask_b32 v27, 0, v20, s[70:71]\n"
"v_mfma_f32_16x16x32_bf16 v[60:63], a[236:239], a[152:155], v[60:63]\n"
"ds_read_b64_tr_b16 a[254:255], v3 offset:7168\n"
"v_cmp_eq_u32 s[70:71], 4, v18\n"
"v_mfma_f32_16x16x32_bf16 v[52:55], a[236:239], a[148:151], v[52:55]\n"
"s_waitcnt vmcnt(9)\n"
"ds_read_b64_tr_b16 a[248:249], v3 offset:4096\n"
"v_mfma_f32_16x16x32_bf16 v[52:55], a[232:235], a[120:123], v[52:55]\n"
"s_xor_b32 s74, s48, 294912\n"
"ds_write_b128 v4, v[228:231] offset:33792\n"
"v_mfma_f32_16x16x32_bf16 v[44:47], a[232:235], a[116:119], v[44:47]\n"
"ds_read_b64_tr_b16 a[250:251], v3 offset:5120\n"
"v_cndmask_b32 v28, 0, v20, s[70:71]\n"
"v_mfma_f32_16x16x32_bf16 v[44:47], a[236:239], a[144:147], v[44:47]\n"
"v_cmp_eq_u32 s[70:71], 5, v18\n"
"v_mfma_f32_16x16x32_bf16 v[36:39], a[236:239], a[140:143], v[36:39]\n"
"s_waitcnt vmcnt(8) lgkmcnt(15)\n"
"ds_read_b64_tr_b16 a[224:225], v11 offset:0\n"
"v_mfma_f32_16x16x32_bf16 v[36:39], a[232:235], a[112:115], v[36:39]\n"
"ds_write_b128 v4, v[232:235] offset:34816\n"
"v_mfma_f32_16x16x32_bf16 v[32:35], a[240:243], a[168:171], v[32:35]\n"
"s_waitcnt lgkmcnt(15)\n"
"ds_read_b64_tr_b16 a[226:227], v11 offset:1024\n"
"v_cndmask_b32 v29, 0, v20, s[70:71]\n"
"v_mfma_f32_16x16x32_bf16 v[32:35], a[244:247], a[196:199], v[32:35]\n"
"s_add_u32 m0, s46, 135168\n"
"v_cmp_eq_u32 s[70:71], 6, v18\n"
"v_mfma_f32_16x16x32_bf16 v[40:43], a[244:247], a[200:203], v[40:43]\n"
"s_waitcnt vmcnt(7) lgkmcnt(15)\n"
"ds_read_b64_tr_b16 a[228:229], v11 offset:2048\n"
"v_mfma_f32_16x16x32_bf16 v[40:43], a[240:243], a[172:175], v[40:43]\n"
"ds_write_b128 v4, v[236:239] offset:35840\n"
"v_mfma_f32_16x16x32_bf16 v[48:51], a[240:243], a[176:179], v[48:51]\n"
"s_waitcnt lgkmcnt(15)\n"
"v_cndmask_b32 v30, 0, v20, s[70:71]\n"
"v_mfma_f32_16x16x32_bf16 v[48:51], a[244:247], a[204:207], v[48:51]\n"
"ds_read_b64_tr_b16 a[230:231], v11 offset:3072\n"
"v_cmp_eq_u32 s[70:71], 7, v18\n"
"s_waitcnt lgkmcnt(15)\n"
"s_branch 5f\n"
"4:\n"
"v_mfma_f32_16x16x32_bf16 a[0:3], a[224:227], v[204:207], a[0:3]\n"
"v_exp_f32 v140, v140\n"
"v_mfma_f32_16x16x32_bf16 a[4:7], a[224:227], v[212:215], a[4:7]\n"
"s_mov_b32 s78, 0x33800000\n"
"v_add_f32 v28, v28, v135\n"
"v_cvt_pk_bf16_f32 v247, v134, v135\n"
"v_mfma_f32_16x16x32_bf16 a[8:11], a[224:227], v[220:223], a[8:11]\n"
"v_exp_f32 v141, v141\n"
"v_mfma_f32_16x16x32_bf16 a[12:15], a[224:227], v[228:231], a[12:15]\n"
"s_mov_b32 s79, 2143289344\n"
"ds_read_b64_tr_b16 a[238:239], v10 offset:37120\n"
"v_add_f32 v30, v30, v140\n"
"v_mfma_f32_16x16x32_bf16 a[16:19], a[224:227], v[236:239], a[16:19]\n"
"s_waitcnt lgkmcnt(4)\n"
"v_exp_f32 v142, v142\n"
"v_mfma_f32_16x16x32_bf16 a[44:47], a[228:231], v[236:239], a[44:47]\n"
"s_xor_b32 s38, s50, 0\n"
"v_add_f32 v30, v30, v141\n"
"v_cvt_pk_bf16_f32 v254, v140, v141\n"
"v_mfma_f32_16x16x32_bf16 a[40:43], a[228:231], v[228:231], a[40:43]\n"
"v_exp_f32 v143, v143\n"
"v_mfma_f32_16x16x32_bf16 a[36:39], a[228:231], v[220:223], a[36:39]\n"
"ds_read_b64_tr_b16 a[240:241], v9 offset:33280\n"
"v_add_f32 v30, v30, v142\n"
"v_mfma_f32_16x16x32_bf16 a[32:35], a[228:231], v[212:215], a[32:35]\n"
"s_waitcnt lgkmcnt(3)\n"
"v_mfma_f32_16x16x32_bf16 a[28:31], a[228:231], v[204:207], a[28:31]\n"
"v_add_f32 v30, v30, v143\n"
"v_cvt_pk_bf16_f32 v255, v142, v143\n"
"v_mfma_f32_16x16x32_bf16 a[56:59], a[232:235], v[204:207], a[56:59]\n"
"ds_read_b64_tr_b16 a[242:243], v9 offset:37376\n"
"v_permlane32_swap_b32 v18, v20\n"
"v_mfma_f32_16x16x32_bf16 a[60:63], a[232:235], v[212:215], a[60:63]\n"
"v_permlane32_swap_b32 v22, v24\n"
"v_permlane32_swap_b32 v26, v28\n"
"v_mfma_f32_16x16x32_bf16 a[64:67], a[232:235], v[220:223], a[64:67]\n"
"ds_read_b64_tr_b16 a[244:245], v10 offset:33280\n"
"v_permlane32_swap_b32 v30, v31\n"
"v_mfma_f32_16x16x32_bf16 a[68:71], a[232:235], v[228:231], a[68:71]\n"
"v_add_f32 v18, v18, v20\n"
"v_add_f32 v22, v22, v24\n"
"v_mfma_f32_16x16x32_bf16 a[72:75], a[232:235], v[236:239], a[72:75]\n"
"s_waitcnt lgkmcnt(3)\n"
"ds_read_b64_tr_b16 a[246:247], v10 offset:37376\n"
"v_add_f32 v26, v26, v28\n"
"v_mfma_f32_16x16x32_bf16 a[100:103], a[236:239], v[236:239], a[100:103]\n"
"v_add_f32 v30, v30, v31\n"
"v_mov_b32 v197, 0x53800000\n"
"v_mfma_f32_16x16x32_bf16 a[96:99], a[236:239], v[228:231], a[96:99]\n"
"ds_read_b64_tr_b16 a[248:249], v9 offset:33536\n"
"v_mov_b32 v196, s79\n"
"v_mfma_f32_16x16x32_bf16 a[92:95], a[236:239], v[220:223], a[92:95]\n"
"v_permlane16_swap_b32 v18, v22\n"
"v_permlane16_swap_b32 v26, v30\n"
"v_mfma_f32_16x16x32_bf16 a[88:91], a[236:239], v[212:215], a[88:91]\n"
"ds_read_b64_tr_b16 a[250:251], v9 offset:37632\n"
"v_add_f32 v18, v18, v22\n"
"v_mfma_f32_16x16x32_bf16 a[84:87], a[236:239], v[204:207], a[84:87]\n"
"v_add_f32 v26, v26, v30\n"
"v_med3_f32 v19, v18, s78, v197\n"
"v_mfma_f32_16x16x32_bf16 a[20:23], a[224:227], v[244:247], a[20:23]\n"
"ds_read_b64_tr_b16 a[252:253], v10 offset:33536\n"
"v_med3_f32 v27, v26, s78, v197\n"
"v_mfma_f32_16x16x32_bf16 a[24:27], a[224:227], v[252:255], a[24:27]\n"
"v_cmp_eq_f32 vcc, v19, v18\n"
"v_cmp_eq_f32 s[70:71], v27, v26\n"
"v_mfma_f32_16x16x32_bf16 a[52:55], a[228:231], v[252:255], a[52:55]\n"
"ds_read_b64_tr_b16 a[254:255], v10 offset:37632\n"
"v_mfma_f32_16x16x32_bf16 a[48:51], a[228:231], v[244:247], a[48:51]\n"
"v_rcp_f32 v18, v18\n"
"v_mfma_f32_16x16x32_bf16 a[76:79], a[232:235], v[244:247], a[76:79]\n"
"v_rcp_f32 v26, v26\n"
"v_mfma_f32_16x16x32_bf16 a[80:83], a[232:235], v[252:255], a[80:83]\n"
"v_cndmask_b32 v18, v196, v18, vcc\n"
"v_cndmask_b32 v26, v196, v26, s[70:71]\n"
"v_mfma_f32_16x16x32_bf16 a[108:111], a[236:239], v[252:255], a[108:111]\n"
"s_waitcnt lgkmcnt(6)\n"
"v_mov_b32 v22, v18\n"
"v_mov_b32 v30, v26\n"
"v_mfma_f32_16x16x32_bf16 a[104:107], a[236:239], v[244:247], a[104:107]\n"
"v_min_u32 v19, 16, v1\n"
"v_lshlrev_b32 v19, 26, v19\n"
"v_mfma_f32_16x16x32_bf16 a[112:115], a[240:243], v[204:207], a[112:115]\n"
"v_permlane16_swap_b32 v18, v22\n"
"v_permlane16_swap_b32 v26, v30\n"
"v_mfma_f32_16x16x32_bf16 a[116:119], a[240:243], v[212:215], a[116:119]\n"
"v_mov_b32 v20, v18\n"
"v_mov_b32 v24, v22\n"
"v_mfma_f32_16x16x32_bf16 a[120:123], a[240:243], v[220:223], a[120:123]\n"
"s_waitcnt vmcnt(22)\n"
"ds_read_b64_tr_b16 a[224:225], v2 offset:0\n"
"v_mov_b32 v28, v26\n"
"v_mfma_f32_16x16x32_bf16 a[124:127], a[240:243], v[228:231], a[124:127]\n"
"v_mov_b32 v31, v30\n"
"v_permlane32_swap_b32 v18, v20\n"
"v_mfma_f32_16x16x32_bf16 a[128:131], a[240:243], v[236:239], a[128:131]\n"
"v_permlane32_swap_b32 v22, v24\n"
"v_permlane32_swap_b32 v26, v28\n"
"v_mfma_f32_16x16x32_bf16 a[132:135], a[240:243], v[244:247], a[132:135]\n"
"ds_read_b64_tr_b16 a[226:227], v2 offset:1024\n"
"v_permlane32_swap_b32 v30, v31\n"
"v_mfma_f32_16x16x32_bf16 a[136:139], a[240:243], v[252:255], a[136:139]\n"
"s_waitcnt lgkmcnt(6)\n"
"v_accvgpr_read_b32 v88, a0\n"
"v_accvgpr_read_b32 v89, a1\n"
"v_mfma_f32_16x16x32_bf16 a[164:167], a[244:247], v[252:255], a[164:167]\n"
"v_accvgpr_read_b32 v90, a2\n"
"v_accvgpr_read_b32 v91, a3\n"
"v_mfma_f32_16x16x32_bf16 a[160:163], a[244:247], v[244:247], a[160:163]\n"
"s_waitcnt vmcnt(20)\n"
"ds_read_b64_tr_b16 a[228:229], v2 offset:2048\n"
"v_accvgpr_read_b32 v92, a28\n"
"v_mfma_f32_16x16x32_bf16 a[156:159], a[244:247], v[236:239], a[156:159]\n"
"v_accvgpr_read_b32 v93, a29\n"
"v_accvgpr_read_b32 v94, a30\n"
"v_mfma_f32_16x16x32_bf16 a[152:155], a[244:247], v[228:231], a[152:155]\n"
"v_accvgpr_read_b32 v95, a31\n"
"v_mul_f32 v88, v88, v18\n"
"v_mfma_f32_16x16x32_bf16 a[148:151], a[244:247], v[220:223], a[148:151]\n"
"ds_read_b64_tr_b16 a[230:231], v2 offset:3072\n"
"v_mul_f32 v89, v89, v18\n"
"v_mfma_f32_16x16x32_bf16 a[144:147], a[244:247], v[212:215], a[144:147]\n"
"s_waitcnt lgkmcnt(6)\n"
"v_mul_f32 v90, v90, v18\n"
"v_mul_f32 v91, v91, v18\n"
"v_mfma_f32_16x16x32_bf16 a[140:143], a[244:247], v[204:207], a[140:143]\n"
"v_mul_f32 v92, v92, v18\n"
"v_mul_f32 v93, v93, v18\n"
"v_mfma_f32_16x16x32_bf16 a[168:171], a[248:251], v[204:207], a[168:171]\n"
"ds_read_b64_tr_b16 a[232:233], v3 offset:0\n"
"v_mul_f32 v94, v94, v18\n"
"v_mfma_f32_16x16x32_bf16 a[172:175], a[248:251], v[212:215], a[172:175]\n"
"v_mul_f32 v95, v95, v18\n"
"v_cvt_pk_bf16_f32 v88, v88, v89\n"
"v_mfma_f32_16x16x32_bf16 a[176:179], a[248:251], v[220:223], a[176:179]\n"
"v_cvt_pk_bf16_f32 v89, v90, v91\n"
"v_cvt_pk_bf16_f32 v90, v92, v93\n"
"v_mfma_f32_16x16x32_bf16 a[180:183], a[248:251], v[228:231], a[180:183]\n"
"ds_read_b64_tr_b16 a[234:235], v3 offset:1024\n"
"v_cvt_pk_bf16_f32 v91, v94, v95\n"
"v_mfma_f32_16x16x32_bf16 a[184:187], a[248:251], v[236:239], a[184:187]\n"
"buffer_store_dwordx4 v[88:91], v1, s[20:23], s38 offen sc1\n"
"v_mfma_f32_16x16x32_bf16 a[188:191], a[248:251], v[244:247], a[188:191]\n"
"v_accvgpr_read_b32 v96, a4\n"
"v_accvgpr_read_b32 v97, a5\n"
"v_mfma_f32_16x16x32_bf16 a[192:195], a[248:251], v[252:255], a[192:195]\n"
"s_waitcnt lgkmcnt(6)\n"
"ds_read_b64_tr_b16 a[236:237], v3 offset:2048\n"
"v_accvgpr_read_b32 v98, a6\n"
"v_mfma_f32_16x16x32_bf16 a[220:223], a[252:255], v[252:255], a[220:223]\n"
"v_accvgpr_read_b32 v99, a7\n"
"v_accvgpr_read_b32 v100, a32\n"
"v_mfma_f32_16x16x32_bf16 a[216:219], a[252:255], v[244:247], a[216:219]\n"
"v_accvgpr_read_b32 v101, a33\n"
"v_accvgpr_read_b32 v102, a34\n"
"v_mfma_f32_16x16x32_bf16 a[212:215], a[252:255], v[236:239], a[212:215]\n"
"ds_read_b64_tr_b16 a[238:239], v3 offset:3072\n"
"v_accvgpr_read_b32 v103, a35\n"
"v_mfma_f32_16x16x32_bf16 a[208:211], a[252:255], v[228:231], a[208:211]\n"
"v_mul_f32 v96, v96, v20\n"
"v_mul_f32 v97, v97, v20\n"
"v_mfma_f32_16x16x32_bf16 a[204:207], a[252:255], v[220:223], a[204:207]\n"
"v_mul_f32 v98, v98, v20\n"
"v_mul_f32 v99, v99, v20\n"
"v_mfma_f32_16x16x32_bf16 a[200:203], a[252:255], v[212:215], a[200:203]\n"
"v_mul_f32 v100, v100, v20\n"
"v_mul_f32 v101, v101, v20\n"
"v_mfma_f32_16x16x32_bf16 a[196:199], a[252:255], v[204:207], a[196:199]\n"
"v_mul_f32 v102, v102, v20\n"
"v_mul_f32 v103, v103, v20\n"
"v_cvt_pk_bf16_f32 v96, v96, v97\n"
"v_cvt_pk_bf16_f32 v97, v98, v99\n"
"v_cvt_pk_bf16_f32 v98, v100, v101\n"
"v_cvt_pk_bf16_f32 v99, v102, v103\n"
"buffer_store_dwordx4 v[96:99], v1, s[20:23], s38 offen offset:1024 sc1\n"
"v_mov_b32 v8, s68\n"
"v_mov_b32 v9, s69\n"
"v_accvgpr_read_b32 v104, a8\n"
"v_accvgpr_read_b32 v105, a9\n"
"v_accvgpr_read_b32 v106, a10\n"
"v_accvgpr_read_b32 v107, a11\n"
"v_accvgpr_read_b32 v108, a36\n"
"v_accvgpr_read_b32 v109, a37\n"
"v_accvgpr_read_b32 v110, a38\n"
"v_accvgpr_read_b32 v111, a39\n"
"v_pk_mul_f32 v[104:105], v[104:105], v[22:23] op_sel_hi:[1,0]\n"
"v_pk_mul_f32 v[106:107], v[106:107], v[22:23] op_sel_hi:[1,0]\n"
"v_pk_mul_f32 v[108:109], v[108:109], v[22:23] op_sel_hi:[1,0]\n"
"v_pk_mul_f32 v[110:111], v[110:111], v[22:23] op_sel_hi:[1,0]\n"
"v_cvt_pk_bf16_f32 v104, v104, v105\n"
"v_cvt_pk_bf16_f32 v105, v106, v107\n"
"v_cvt_pk_bf16_f32 v106, v108, v109\n"
"v_cvt_pk_bf16_f32 v107, v110, v111\n"
"buffer_store_dwordx4 v[104:107], v1, s[20:23], s38 offen offset:2048 sc1\n"
"s_waitcnt vmcnt(21)\n"
"ds_read_b64_tr_b16 a[240:241], v2 offset:4096\n"
"v_accvgpr_read_b32 v112, a12\n"
"v_accvgpr_read_b32 v113, a13\n"
"v_accvgpr_read_b32 v114, a14\n"
"v_accvgpr_read_b32 v115, a15\n"
"v_accvgpr_read_b32 v116, a40\n"
"v_accvgpr_read_b32 v117, a41\n"
"v_accvgpr_read_b32 v118, a42\n"
"v_accvgpr_read_b32 v119, a43\n"
"v_pk_mul_f32 v[112:113], v[112:113], v[24:25] op_sel_hi:[1,0]\n"
"v_pk_mul_f32 v[114:115], v[114:115], v[24:25] op_sel_hi:[1,0]\n"
"v_pk_mul_f32 v[116:117], v[116:117], v[24:25] op_sel_hi:[1,0]\n"
"v_pk_mul_f32 v[118:119], v[118:119], v[24:25] op_sel_hi:[1,0]\n"
"v_cvt_pk_bf16_f32 v112, v112, v113\n"
"v_cvt_pk_bf16_f32 v113, v114, v115\n"
"v_cvt_pk_bf16_f32 v114, v116, v117\n"
"v_cvt_pk_bf16_f32 v115, v118, v119\n"
"buffer_store_dwordx4 v[112:115], v1, s[20:23], s38 offen offset:3072 sc1\n"
"ds_read_b64_tr_b16 a[242:243], v2 offset:5120\n"
"v_accvgpr_read_b32 v120, a16\n"
"v_accvgpr_read_b32 v121, a17\n"
"v_accvgpr_read_b32 v122, a18\n"
"v_accvgpr_read_b32 v123, a19\n"
"v_accvgpr_read_b32 v124, a44\n"
"v_accvgpr_read_b32 v125, a45\n"
"v_accvgpr_read_b32 v126, a46\n"
"v_accvgpr_read_b32 v127, a47\n"
"s_xor_b32 s38, s50, 4096\n"
"v_pk_mul_f32 v[120:121], v[120:121], v[26:27] op_sel_hi:[1,0]\n"
"v_pk_mul_f32 v[122:123], v[122:123], v[26:27] op_sel_hi:[1,0]\n"
"v_pk_mul_f32 v[124:125], v[124:125], v[26:27] op_sel_hi:[1,0]\n"
"v_pk_mul_f32 v[126:127], v[126:127], v[26:27] op_sel_hi:[1,0]\n"
"v_cvt_pk_bf16_f32 v120, v120, v121\n"
"v_cvt_pk_bf16_f32 v121, v122, v123\n"
"v_cvt_pk_bf16_f32 v122, v124, v125\n"
"v_cvt_pk_bf16_f32 v123, v126, v127\n"
"buffer_store_dwordx4 v[120:123], v1, s[20:23], s38 offen sc1\n"
"s_waitcnt vmcnt(21)\n"
"ds_read_b64_tr_b16 a[244:245], v2 offset:6144\n"
"v_accvgpr_read_b32 v128, a20\n"
"v_accvgpr_read_b32 v129, a21\n"
"v_accvgpr_read_b32 v130, a22\n"
"v_accvgpr_read_b32 v131, a23\n"
"v_accvgpr_read_b32 v132, a48\n"
"v_accvgpr_read_b32 v133, a49\n"
"v_accvgpr_read_b32 v134, a50\n"
"v_accvgpr_read_b32 v135, a51\n"
"v_pk_mul_f32 v[128:129], v[128:129], v[28:29] op_sel_hi:[1,0]\n"
"v_pk_mul_f32 v[130:131], v[130:131], v[28:29] op_sel_hi:[1,0]\n"
"v_pk_mul_f32 v[132:133], v[132:133], v[28:29] op_sel_hi:[1,0]\n"
"v_pk_mul_f32 v[134:135], v[134:135], v[28:29] op_sel_hi:[1,0]\n"
"v_cvt_pk_bf16_f32 v128, v128, v129\n"
"v_cvt_pk_bf16_f32 v129, v130, v131\n"
"v_cvt_pk_bf16_f32 v130, v132, v133\n"
"v_cvt_pk_bf16_f32 v131, v134, v135\n"
"buffer_store_dwordx4 v[128:131], v1, s[20:23], s38 offen offset:1024 sc1\n"
"ds_read_b64_tr_b16 a[246:247], v2 offset:7168\n"
"v_accvgpr_read_b32 v136, a24\n"
"v_accvgpr_read_b32 v137, a25\n"
"v_accvgpr_read_b32 v138, a26\n"
"v_accvgpr_read_b32 v139, a27\n"
"v_accvgpr_read_b32 v140, a52\n"
"v_accvgpr_read_b32 v141, a53\n"
"v_accvgpr_read_b32 v142, a54\n"
"v_accvgpr_read_b32 v143, a55\n"
"v_pk_mul_f32 v[136:137], v[136:137], v[30:31] op_sel_hi:[1,0]\n"
"v_pk_mul_f32 v[138:139], v[138:139], v[30:31] op_sel_hi:[1,0]\n"
"v_pk_mul_f32 v[140:141], v[140:141], v[30:31] op_sel_hi:[1,0]\n"
"v_pk_mul_f32 v[142:143], v[142:143], v[30:31] op_sel_hi:[1,0]\n"
"v_cvt_pk_bf16_f32 v136, v136, v137\n"
"v_cvt_pk_bf16_f32 v137, v138, v139\n"
"v_cvt_pk_bf16_f32 v138, v140, v141\n"
"v_cvt_pk_bf16_f32 v139, v142, v143\n"
"buffer_store_dwordx4 v[136:139], v1, s[20:23], s38 offen offset:2048 sc1\n"
"s_xor_b32 s75, s48, 65536\n"
"s_add_u32 m0, s46, 131072\n"
"s_nop 0\n"
"s_waitcnt lgkmcnt(6)\n"
"buffer_load_dwordx4 v14, s[24:27], s75 offen lds\n"
"buffer_load_dwordx4 v15, s[24:27], s75 offen offset:1024 lds\n"
"v_accvgpr_read_b32 v88, a56\n"
"v_accvgpr_read_b32 v89, a57\n"
"v_accvgpr_read_b32 v90, a58\n"
"v_accvgpr_read_b32 v91, a59\n"
"v_accvgpr_read_b32 v92, a84\n"
"v_accvgpr_read_b32 v93, a85\n"
"v_accvgpr_read_b32 v94, a86\n"
"v_accvgpr_read_b32 v95, a87\n"
"s_xor_b32 s38, s50, 16384\n"
"v_pk_mul_f32 v[88:89], v[88:89], v[18:19] op_sel_hi:[1,0]\n"
"v_pk_mul_f32 v[90:91], v[90:91], v[18:19] op_sel_hi:[1,0]\n"
"v_pk_mul_f32 v[92:93], v[92:93], v[18:19] op_sel_hi:[1,0]\n"
"v_pk_mul_f32 v[94:95], v[94:95], v[18:19] op_sel_hi:[1,0]\n"
"v_cvt_pk_bf16_f32 v88, v88, v89\n"
"v_cvt_pk_bf16_f32 v89, v90, v91\n"
"v_cvt_pk_bf16_f32 v90, v92, v93\n"
"v_cvt_pk_bf16_f32 v91, v94, v95\n"
"buffer_store_dwordx4 v[88:91], v1, s[20:23], s38 offen sc1\n"
"s_waitcnt lgkmcnt(4)\n"
"buffer_load_dwordx4 v21, s[24:27], s75 offen offset:2048 lds\n"
"buffer_load_dwordx4 v23, s[24:27], s75 offen offset:3072 lds\n"
"v_accvgpr_read_b32 v96, a60\n"
"v_accvgpr_read_b32 v97, a61\n"
"v_accvgpr_read_b32 v98, a62\n"
"v_accvgpr_read_b32 v99, a63\n"
"v_accvgpr_read_b32 v100, a88\n"
"v_accvgpr_read_b32 v101, a89\n"
"v_accvgpr_read_b32 v102, a90\n"
"v_accvgpr_read_b32 v103, a91\n"
"v_pk_mul_f32 v[96:97], v[96:97], v[20:21] op_sel_hi:[1,0]\n"
"v_pk_mul_f32 v[98:99], v[98:99], v[20:21] op_sel_hi:[1,0]\n"
"v_pk_mul_f32 v[100:101], v[100:101], v[20:21] op_sel_hi:[1,0]\n"
"v_pk_mul_f32 v[102:103], v[102:103], v[20:21] op_sel_hi:[1,0]\n"
"v_cvt_pk_bf16_f32 v96, v96, v97\n"
"v_cvt_pk_bf16_f32 v97, v98, v99\n"
"v_cvt_pk_bf16_f32 v98, v100, v101\n"
"v_cvt_pk_bf16_f32 v99, v102, v103\n"
"buffer_store_dwordx4 v[96:99], v1, s[20:23], s38 offen offset:1024 sc1\n"
"v_accvgpr_read_b32 v104, a64\n"
"v_accvgpr_read_b32 v105, a65\n"
"v_accvgpr_read_b32 v106, a66\n"
"v_accvgpr_read_b32 v107, a67\n"
"v_accvgpr_read_b32 v108, a92\n"
"v_accvgpr_read_b32 v109, a93\n"
"v_accvgpr_read_b32 v110, a94\n"
"v_accvgpr_read_b32 v111, a95\n"
"v_pk_mul_f32 v[104:105], v[104:105], v[22:23] op_sel_hi:[1,0]\n"
"v_pk_mul_f32 v[106:107], v[106:107], v[22:23] op_sel_hi:[1,0]\n"
"v_pk_mul_f32 v[108:109], v[108:109], v[22:23] op_sel_hi:[1,0]\n"
"v_pk_mul_f32 v[110:111], v[110:111], v[22:23] op_sel_hi:[1,0]\n"
"v_cvt_pk_bf16_f32 v104, v104, v105\n"
"v_cvt_pk_bf16_f32 v105, v106, v107\n"
"v_cvt_pk_bf16_f32 v106, v108, v109\n"
"v_cvt_pk_bf16_f32 v107, v110, v111\n"
"buffer_store_dwordx4 v[104:107], v1, s[20:23], s38 offen offset:2048 sc1\n"
"v_accvgpr_read_b32 v112, a68\n"
"v_accvgpr_read_b32 v113, a69\n"
"v_accvgpr_read_b32 v114, a70\n"
"v_accvgpr_read_b32 v115, a71\n"
"v_accvgpr_read_b32 v116, a96\n"
"v_accvgpr_read_b32 v117, a97\n"
"v_accvgpr_read_b32 v118, a98\n"
"v_accvgpr_read_b32 v119, a99\n"
"v_pk_mul_f32 v[112:113], v[112:113], v[24:25] op_sel_hi:[1,0]\n"
"v_pk_mul_f32 v[114:115], v[114:115], v[24:25] op_sel_hi:[1,0]\n"
"v_pk_mul_f32 v[116:117], v[116:117], v[24:25] op_sel_hi:[1,0]\n"
"v_pk_mul_f32 v[118:119], v[118:119], v[24:25] op_sel_hi:[1,0]\n"
"v_cvt_pk_bf16_f32 v112, v112, v113\n"
"v_cvt_pk_bf16_f32 v113, v114, v115\n"
"v_cvt_pk_bf16_f32 v114, v116, v117\n"
"v_cvt_pk_bf16_f32 v115, v118, v119\n"
"buffer_store_dwordx4 v[112:115], v1, s[20:23], s38 offen offset:3072 sc1\n"
"v_accvgpr_read_b32 v120, a72\n"
"v_accvgpr_read_b32 v121, a73\n"
"v_accvgpr_read_b32 v122, a74\n"
"v_accvgpr_read_b32 v123, a75\n"
"v_accvgpr_read_b32 v124, a100\n"
"v_accvgpr_read_b32 v125, a101\n"
"v_accvgpr_read_b32 v126, a102\n"
"v_accvgpr_read_b32 v127, a103\n"
"s_xor_b32 s38, s50, 20480\n"
"v_pk_mul_f32 v[120:121], v[120:121], v[26:27] op_sel_hi:[1,0]\n"
"v_pk_mul_f32 v[122:123], v[122:123], v[26:27] op_sel_hi:[1,0]\n"
"v_pk_mul_f32 v[124:125], v[124:125], v[26:27] op_sel_hi:[1,0]\n"
"v_pk_mul_f32 v[126:127], v[126:127], v[26:27] op_sel_hi:[1,0]\n"
"v_cvt_pk_bf16_f32 v120, v120, v121\n"
"v_cvt_pk_bf16_f32 v121, v122, v123\n"
"v_cvt_pk_bf16_f32 v122, v124, v125\n"
"v_cvt_pk_bf16_f32 v123, v126, v127\n"
"buffer_store_dwordx4 v[120:123], v1, s[20:23], s38 offen sc1\n"
"v_accvgpr_read_b32 v128, a76\n"
"v_accvgpr_read_b32 v129, a77\n"
"v_accvgpr_read_b32 v130, a78\n"
"v_accvgpr_read_b32 v131, a79\n"
"v_accvgpr_read_b32 v132, a104\n"
"v_accvgpr_read_b32 v133, a105\n"
"v_accvgpr_read_b32 v134, a106\n"
"v_accvgpr_read_b32 v135, a107\n"
"v_pk_mul_f32 v[128:129], v[128:129], v[28:29] op_sel_hi:[1,0]\n"
"v_pk_mul_f32 v[130:131], v[130:131], v[28:29] op_sel_hi:[1,0]\n"
"v_pk_mul_f32 v[132:133], v[132:133], v[28:29] op_sel_hi:[1,0]\n"
"v_pk_mul_f32 v[134:135], v[134:135], v[28:29] op_sel_hi:[1,0]\n"
"v_cvt_pk_bf16_f32 v128, v128, v129\n"
"v_cvt_pk_bf16_f32 v129, v130, v131\n"
"v_cvt_pk_bf16_f32 v130, v132, v133\n"
"v_cvt_pk_bf16_f32 v131, v134, v135\n"
"buffer_store_dwordx4 v[128:131], v1, s[20:23], s38 offen offset:1024 sc1\n"
"v_accvgpr_read_b32 v136, a80\n"
"v_accvgpr_read_b32 v137, a81\n"
"v_accvgpr_read_b32 v138, a82\n"
"v_accvgpr_read_b32 v139, a83\n"
"v_accvgpr_read_b32 v140, a108\n"
"v_accvgpr_read_b32 v141, a109\n"
"v_accvgpr_read_b32 v142, a110\n"
"v_accvgpr_read_b32 v143, a111\n"
"v_pk_mul_f32 v[136:137], v[136:137], v[30:31] op_sel_hi:[1,0]\n"
"v_pk_mul_f32 v[138:139], v[138:139], v[30:31] op_sel_hi:[1,0]\n"
"v_pk_mul_f32 v[140:141], v[140:141], v[30:31] op_sel_hi:[1,0]\n"
"v_pk_mul_f32 v[142:143], v[142:143], v[30:31] op_sel_hi:[1,0]\n"
"v_cvt_pk_bf16_f32 v136, v136, v137\n"
"v_cvt_pk_bf16_f32 v137, v138, v139\n"
"v_cvt_pk_bf16_f32 v138, v140, v141\n"
"v_cvt_pk_bf16_f32 v139, v142, v143\n"
"buffer_store_dwordx4 v[136:139], v1, s[20:23], s38 offen offset:2048 sc1\n"
"s_waitcnt lgkmcnt(0)\n"
"s_barrier\n"
"ds_read_b128 a[0:3], v6 offset:0\n"
"ds_read_b128 a[28:31], v6 offset:2048\n"
"ds_read_b128 a[32:35], v6 offset:6144\n"
"ds_read_b128 a[4:7], v6 offset:4096\n"
"ds_read_b128 a[8:11], v6 offset:8192\n"
"s_waitcnt lgkmcnt(4)\n"
"ds_read_b128 a[36:39], v6 offset:10240\n"
"v_mfma_f32_16x16x32_bf16 v[32:35], a[224:227], a[0:3], 0\n"
"s_waitcnt lgkmcnt(3)\n"
"ds_read_b128 a[40:43], v6 offset:14336\n"
"v_accvgpr_read_b32 v216, a112\n"
"v_mfma_f32_16x16x32_bf16 v[32:35], a[228:231], a[28:31], v[32:35]\n"
"s_xor_b32 s38, s50, 32768\n"
"ds_read_b128 a[12:15], v6 offset:12288\n"
"v_accvgpr_read_b32 v217, a113\n"
"v_accvgpr_read_b32 v218, a114\n"
"v_mfma_f32_16x16x32_bf16 v[40:43], a[228:231], a[32:35], 0\n"
"s_waitcnt lgkmcnt(3)\n"
"ds_read_b128 a[16:19], v6 offset:16384\n"
"v_accvgpr_read_b32 v219, a115\n"
"v_mfma_f32_16x16x32_bf16 v[40:43], a[224:227], a[4:7], v[40:43]\n"
"ds_read_b128 a[44:47], v6 offset:18432\n"
"v_accvgpr_read_b32 v220, a140\n"
"v_accvgpr_read_b32 v221, a141\n"
"v_mfma_f32_16x16x32_bf16 v[48:51], a[224:227], a[8:11], 0\n"
"s_waitcnt lgkmcnt(3)\n"
"ds_read_b128 a[48:51], v6 offset:22528\n"
"v_accvgpr_read_b32 v222, a142\n"
"v_accvgpr_read_b32 v223, a143\n"
"v_mfma_f32_16x16x32_bf16 v[48:51], a[228:231], a[36:39], v[48:51]\n"
"ds_read_b128 a[20:23], v6 offset:20480\n"
"v_mul_f32 v216, v216, v18\n"
"v_mfma_f32_16x16x32_bf16 v[56:59], a[228:231], a[40:43], 0\n"
"s_waitcnt lgkmcnt(3)\n"
"ds_read_b128 a[24:27], v6 offset:24576\n"
"v_mul_f32 v217, v217, v18\n"
"v_mul_f32 v218, v218, v18\n"
"v_mfma_f32_16x16x32_bf16 v[56:59], a[224:227], a[12:15], v[56:59]\n"
"ds_read_b128 a[52:55], v6 offset:26624\n"
"v_mul_f32 v219, v219, v18\n"
"v_mfma_f32_16x16x32_bf16 v[64:67], a[224:227], a[16:19], 0\n"
"s_waitcnt lgkmcnt(3)\n"
"ds_read_b128 a[56:59], v6 offset:1024\n"
"v_mul_f32 v220, v220, v18\n"
"v_mul_f32 v221, v221, v18\n"
"v_mfma_f32_16x16x32_bf16 v[64:67], a[228:231], a[44:47], v[64:67]\n"
"ds_read_b128 a[84:87], v6 offset:3072\n"
"v_mul_f32 v222, v222, v18\n"
"v_mul_f32 v223, v223, v18\n"
"v_mfma_f32_16x16x32_bf16 v[72:75], a[228:231], a[48:51], 0\n"
"s_waitcnt vmcnt(0) lgkmcnt(3)\n"
"buffer_store_dwordx2 v[8:9], v19, s[20:23], s60 offen offset:128 sc1\n"
"v_cvt_pk_bf16_f32 v216, v216, v217\n"
"v_mfma_f32_16x16x32_bf16 v[72:75], a[224:227], a[20:23], v[72:75]\n"
"v_cvt_pk_bf16_f32 v217, v218, v219\n"
"v_cvt_pk_bf16_f32 v218, v220, v221\n"
"v_cvt_pk_bf16_f32 v219, v222, v223\n"
"v_mfma_f32_16x16x32_bf16 v[80:83], a[224:227], a[24:27], 0\n"
"s_waitcnt lgkmcnt(2)\n"
"ds_read_b128 a[88:91], v6 offset:7168\n"
"v_accvgpr_read_b32 v200, a116\n"
"v_mfma_f32_16x16x32_bf16 v[80:83], a[228:231], a[52:55], v[80:83]\n"
"ds_read_b128 a[60:63], v6 offset:5120\n"
"v_accvgpr_read_b32 v201, a117\n"
"v_accvgpr_read_b32 v202, a118\n"
"v_mfma_f32_16x16x32_bf16 v[84:87], a[236:239], a[52:55], 0\n"
"ds_read_b128 a[64:67], v6 offset:9216\n"
"v_accvgpr_read_b32 v203, a119\n"
"v_accvgpr_read_b32 v204, a144\n"
"v_mfma_f32_16x16x32_bf16 v[84:87], a[232:235], a[24:27], v[84:87]\n"
"buffer_store_dwordx4 v[216:219], v1, s[20:23], s38 offen sc1\n"
"v_mfma_f32_16x16x32_bf16 v[76:79], a[232:235], a[20:23], 0\n"
"ds_read_b128 a[92:95], v6 offset:11264\n"
"v_accvgpr_read_b32 v205, a145\n"
"v_accvgpr_read_b32 v206, a146\n"
"v_mfma_f32_16x16x32_bf16 v[76:79], a[236:239], a[48:51], v[76:79]\n"
"ds_read_b128 a[96:99], v6 offset:15360\n"
"v_accvgpr_read_b32 v207, a147\n"
"v_mfma_f32_16x16x32_bf16 v[68:71], a[236:239], a[44:47], 0\n"
"ds_read_b128 a[68:71], v6 offset:13312\n"
"v_mul_f32 v200, v200, v20\n"
"v_mul_f32 v201, v201, v20\n"
"v_mfma_f32_16x16x32_bf16 v[68:71], a[232:235], a[16:19], v[68:71]\n"
"v_mul_f32 v202, v202, v20\n"
"v_mul_f32 v203, v203, v20\n"
"v_mul_f32 v204, v204, v20\n"
"v_mfma_f32_16x16x32_bf16 v[60:63], a[232:235], a[12:15], 0\n"
"ds_read_b128 a[72:75], v6 offset:17408\n"
"v_mul_f32 v205, v205, v20\n"
"v_mfma_f32_16x16x32_bf16 v[60:63], a[236:239], a[40:43], v[60:63]\n"
"ds_read_b128 a[100:103], v6 offset:19456\n"
"v_mul_f32 v206, v206, v20\n"
"v_mul_f32 v207, v207, v20\n"
"v_mfma_f32_16x16x32_bf16 v[52:55], a[236:239], a[36:39], 0\n"
"ds_read_b128 a[104:107], v6 offset:23552\n"
"v_cvt_pk_bf16_f32 v200, v200, v201\n"
"v_mfma_f32_16x16x32_bf16 v[52:55], a[232:235], a[8:11], v[52:55]\n"
"v_cvt_pk_bf16_f32 v201, v202, v203\n"
"v_cvt_pk_bf16_f32 v202, v204, v205\n"
"v_cvt_pk_bf16_f32 v203, v206, v207\n"
"v_mfma_f32_16x16x32_bf16 v[44:47], a[232:235], a[4:7], 0\n"
"ds_read_b128 a[76:79], v6 offset:21504\n"
"v_accvgpr_read_b32 v208, a120\n"
"v_accvgpr_read_b32 v209, a121\n"
"v_mfma_f32_16x16x32_bf16 v[44:47], a[236:239], a[32:35], v[44:47]\n"
"ds_read_b128 a[80:83], v6 offset:25600\n"
"v_accvgpr_read_b32 v210, a122\n"
"v_mfma_f32_16x16x32_bf16 v[36:39], a[236:239], a[28:31], 0\n"
"s_waitcnt lgkmcnt(12)\n"
"ds_read_b128 a[108:111], v6 offset:27648\n"
"v_accvgpr_read_b32 v211, a123\n"
"v_accvgpr_read_b32 v212, a148\n"
"v_mfma_f32_16x16x32_bf16 v[36:39], a[232:235], a[0:3], v[36:39]\n"
"buffer_store_dwordx4 v[200:203], v1, s[20:23], s38 offen offset:1024 sc1\n"
"v_mfma_f32_16x16x32_bf16 v[32:35], a[240:243], a[56:59], v[32:35]\n"
"s_waitcnt lgkmcnt(11)\n"
"ds_read_b64_tr_b16 a[252:253], v3 offset:6144\n"
"v_accvgpr_read_b32 v213, a149\n"
"v_accvgpr_read_b32 v214, a150\n"
"v_mfma_f32_16x16x32_bf16 v[32:35], a[244:247], a[84:87], v[32:35]\n"
"ds_read_b64_tr_b16 a[254:255], v3 offset:7168\n"
"v_accvgpr_read_b32 v215, a151\n"
"v_mul_f32 v208, v208, v22\n"
"v_mfma_f32_16x16x32_bf16 v[40:43], a[244:247], a[88:91], v[40:43]\n"
"s_waitcnt lgkmcnt(11)\n"
"ds_read_b64_tr_b16 a[248:249], v3 offset:4096\n"
"v_mul_f32 v209, v209, v22\n"
"v_mfma_f32_16x16x32_bf16 v[40:43], a[240:243], a[60:63], v[40:43]\n"
"s_xor_b32 s74, s48, 98304\n"
"ds_read_b64_tr_b16 a[250:251], v3 offset:5120\n"
"v_mul_f32 v210, v210, v22\n"
"v_mul_f32 v211, v211, v22\n"
"v_mfma_f32_16x16x32_bf16 v[48:51], a[240:243], a[64:67], v[48:51]\n"
"s_waitcnt lgkmcnt(11)\n"
"ds_read_b64_tr_b16 a[224:225], v11 offset:0\n"
"v_mul_f32 v212, v212, v22\n"
"v_mfma_f32_16x16x32_bf16 v[48:51], a[244:247], a[92:95], v[48:51]\n"
"ds_read_b64_tr_b16 a[226:227], v11 offset:1024\n"
"v_mul_f32 v213, v213, v22\n"
"v_mul_f32 v214, v214, v22\n"
"v_mfma_f32_16x16x32_bf16 v[56:59], a[244:247], a[96:99], v[56:59]\n"
"s_waitcnt lgkmcnt(11)\n"
"ds_read_b64_tr_b16 a[228:229], v11 offset:2048\n"
"v_mul_f32 v215, v215, v22\n"
"v_cvt_pk_bf16_f32 v208, v208, v209\n"
"v_mfma_f32_16x16x32_bf16 v[56:59], a[240:243], a[68:71], v[56:59]\n"
"s_add_u32 m0, s46, 135168\n"
"v_cvt_pk_bf16_f32 v209, v210, v211\n"
"v_cvt_pk_bf16_f32 v210, v212, v213\n"
"v_mfma_f32_16x16x32_bf16 v[64:67], a[240:243], a[72:75], v[64:67]\n"
"s_waitcnt lgkmcnt(10)\n"
"ds_read_b64_tr_b16 a[230:231], v11 offset:3072\n"
"v_cvt_pk_bf16_f32 v211, v214, v215\n"
"v_accvgpr_read_b32 v216, a124\n"
"v_mfma_f32_16x16x32_bf16 v[64:67], a[244:247], a[100:103], v[64:67]\n"
"ds_read_b64_tr_b16 a[236:237], v12 offset:2048\n"
"v_accvgpr_read_b32 v217, a125\n"
"v_mfma_f32_16x16x32_bf16 v[72:75], a[244:247], a[104:107], v[72:75]\n"
"s_waitcnt lgkmcnt(5)\n"
"ds_read_b64_tr_b16 a[238:239], v12 offset:3072\n"
"v_accvgpr_read_b32 v218, a126\n"
"v_accvgpr_read_b32 v219, a127\n"
"v_mfma_f32_16x16x32_bf16 v[72:75], a[240:243], a[76:79], v[72:75]\n"
"buffer_load_dwordx4 v14, s[24:27], s74 offen lds\n"
"v_accvgpr_read_b32 v220, a152\n"
"v_accvgpr_read_b32 v221, a153\n"
"v_mfma_f32_16x16x32_bf16 v[80:83], a[240:243], a[80:83], v[80:83]\n"
"ds_read_b64_tr_b16 a[232:233], v12 offset:0\n"
"v_accvgpr_read_b32 v222, a154\n"
"v_mfma_f32_16x16x32_bf16 v[80:83], a[244:247], a[108:111], v[80:83]\n"
"ds_read_b64_tr_b16 a[234:235], v12 offset:1024\n"
"v_accvgpr_read_b32 v223, a155\n"
"v_mul_f32 v216, v216, v24\n"
"v_mfma_f32_16x16x32_bf16 v[84:87], a[252:255], a[108:111], v[84:87]\n"
"buffer_load_dwordx4 v15, s[24:27], s74 offen offset:1024 lds\n"
"v_mul_f32 v217, v217, v24\n"
"v_mfma_f32_16x16x32_bf16 v[84:87], a[248:251], a[80:83], v[84:87]\n"
"s_xor_b32 s75, s48, 65792\n"
"buffer_store_dwordx4 v[208:211], v1, s[20:23], s38 offen offset:2048 sc1\n"
"v_mfma_f32_16x16x32_bf16 v[76:79], a[248:251], a[76:79], v[76:79]\n"
"v_mul_f32 v218, v218, v24\n"
"v_mul_f32 v219, v219, v24\n"
"v_mul_f32 v220, v220, v24\n"
"v_mfma_f32_16x16x32_bf16 v[76:79], a[252:255], a[104:107], v[76:79]\n"
"buffer_load_dwordx4 v21, s[24:27], s74 offen offset:2048 lds\n"
"v_mul_f32 v221, v221, v24\n"
"v_mfma_f32_16x16x32_bf16 v[68:71], a[252:255], a[100:103], v[68:71]\n"
"v_mul_f32 v222, v222, v24\n"
"v_mul_f32 v223, v223, v24\n"
"v_cvt_pk_bf16_f32 v216, v216, v217\n"
"v_mfma_f32_16x16x32_bf16 v[68:71], a[248:251], a[72:75], v[68:71]\n"
"v_cvt_pk_bf16_f32 v217, v218, v219\n"
"v_cvt_pk_bf16_f32 v218, v220, v221\n"
"v_mfma_f32_16x16x32_bf16 v[60:63], a[248:251], a[68:71], v[60:63]\n"
"buffer_load_dwordx4 v23, s[24:27], s74 offen offset:3072 lds\n"
"v_cvt_pk_bf16_f32 v219, v222, v223\n"
"v_accvgpr_read_b32 v200, a128\n"
"v_mfma_f32_16x16x32_bf16 v[60:63], a[252:255], a[96:99], v[60:63]\n"
"s_add_u32 m0, s46, 0\n"
"buffer_store_dwordx4 v[216:219], v1, s[20:23], s38 offen offset:3072 sc1\n"
"v_mfma_f32_16x16x32_bf16 v[52:55], a[252:255], a[92:95], v[52:55]\n"
"s_waitcnt lgkmcnt(0)\n"
"ds_read_b64_tr_b16 a[240:241], v11 offset:4096\n"
"v_accvgpr_read_b32 v201, a129\n"
"v_mfma_f32_16x16x32_bf16 v[52:55], a[248:251], a[64:67], v[52:55]\n"
"s_xor_b32 s38, s50, 36864\n"
"buffer_load_dwordx4 v14, s[24:27], s75 offen lds\n"
"v_accvgpr_read_b32 v202, a130\n"
"v_accvgpr_read_b32 v203, a131\n"
"v_mfma_f32_16x16x32_bf16 v[44:47], a[248:251], a[60:63], v[44:47]\n"
"ds_read_b64_tr_b16 a[242:243], v11 offset:5120\n"
"v_accvgpr_read_b32 v204, a156\n"
"v_mfma_f32_16x16x32_bf16 v[44:47], a[252:255], a[88:91], v[44:47]\n"
"v_accvgpr_read_b32 v205, a157\n"
"v_accvgpr_read_b32 v206, a158\n"
"v_accvgpr_read_b32 v207, a159\n"
"v_mfma_f32_16x16x32_bf16 v[36:39], a[252:255], a[84:87], v[36:39]\n"
"ds_read_b64_tr_b16 a[244:245], v11 offset:6144\n"
"v_mul_f32 v200, v200, v26\n"
"v_mul_f32 v201, v201, v26\n"
"v_mfma_f32_16x16x32_bf16 v[36:39], a[248:251], a[56:59], v[36:39]\n"
"buffer_load_dwordx4 v15, s[24:27], s75 offen offset:1024 lds\n"
"v_mul_f32 v202, v202, v26\n"
"v_mfma_f32_16x16x32_bf16 v[88:91], a[224:227], a[0:3], 0\n"
"ds_read_b64_tr_b16 a[246:247], v11 offset:7168\n"
"v_mul_f32 v203, v203, v26\n"
"v_mul_f32 v204, v204, v26\n"
"v_mfma_f32_16x16x32_bf16 v[88:91], a[228:231], a[28:31], v[88:91]\n"
"v_mul_f32 v205, v205, v26\n"
"v_mul_f32 v206, v206, v26\n"
"v_mfma_f32_16x16x32_bf16 v[96:99], a[228:231], a[32:35], 0\n"
"buffer_load_dwordx4 v21, s[24:27], s75 offen offset:2048 lds\n"
"v_mul_f32 v207, v207, v26\n"
"v_cvt_pk_bf16_f32 v200, v200, v201\n"
"v_mfma_f32_16x16x32_bf16 v[96:99], a[224:227], a[4:7], v[96:99]\n"
"v_cvt_pk_bf16_f32 v201, v202, v203\n"
"v_cvt_pk_bf16_f32 v202, v204, v205\n"
"v_cvt_pk_bf16_f32 v203, v206, v207\n"
"v_mfma_f32_16x16x32_bf16 v[104:107], a[224:227], a[8:11], 0\n"
"buffer_load_dwordx2 v[16:17], off, s[20:23], s61 offset:128 sc1\n"
"v_accvgpr_read_b32 v208, a132\n"
"v_mfma_f32_16x16x32_bf16 v[104:107], a[228:231], a[36:39], v[104:107]\n"
"buffer_load_dwordx4 v23, s[24:27], s75 offen offset:3072 lds\n"
"v_accvgpr_read_b32 v209, a133\n"
"v_accvgpr_read_b32 v210, a134\n"
"v_mfma_f32_16x16x32_bf16 v[112:115], a[228:231], a[40:43], 0\n"
"buffer_store_dwordx4 v[200:203], v1, s[20:23], s38 offen sc1\n"
"v_mfma_f32_16x16x32_bf16 v[112:115], a[224:227], a[12:15], v[112:115]\n"
"v_accvgpr_read_b32 v211, a135\n"
"v_accvgpr_read_b32 v212, a160\n"
"v_accvgpr_read_b32 v213, a161\n"
"v_mfma_f32_16x16x32_bf16 v[120:123], a[224:227], a[16:19], 0\n"
"ds_read_b64_tr_b16 a[252:253], v12 offset:6144\n"
"v_accvgpr_read_b32 v214, a162\n"
"v_accvgpr_read_b32 v215, a163\n"
"v_mfma_f32_16x16x32_bf16 v[120:123], a[228:231], a[44:47], v[120:123]\n"
"ds_read_b64_tr_b16 a[254:255], v12 offset:7168\n"
"v_mul_f32 v208, v208, v28\n"
"v_mfma_f32_16x16x32_bf16 v[128:131], a[228:231], a[48:51], 0\n"
"ds_read_b64_tr_b16 a[248:249], v12 offset:4096\n"
"v_mul_f32 v209, v209, v28\n"
"v_mul_f32 v210, v210, v28\n"
"v_mfma_f32_16x16x32_bf16 v[128:131], a[224:227], a[20:23], v[128:131]\n"
"s_xor_b32 s74, s48, 98560\n"
"v_mul_f32 v211, v211, v28\n"
"v_mul_f32 v212, v212, v28\n"
"v_mfma_f32_16x16x32_bf16 v[136:139], a[224:227], a[24:27], 0\n"
"ds_read_b64_tr_b16 a[250:251], v12 offset:5120\n"
"v_mul_f32 v213, v213, v28\n"
"v_mul_f32 v214, v214, v28\n"
"v_mfma_f32_16x16x32_bf16 v[136:139], a[228:231], a[52:55], v[136:139]\n"
"v_mul_f32 v215, v215, v28\n"
"v_cvt_pk_bf16_f32 v208, v208, v209\n"
"v_cvt_pk_bf16_f32 v209, v210, v211\n"
"v_mfma_f32_16x16x32_bf16 v[140:143], a[236:239], a[52:55], 0\n"
"v_cvt_pk_bf16_f32 v210, v212, v213\n"
"v_cvt_pk_bf16_f32 v211, v214, v215\n"
"v_mfma_f32_16x16x32_bf16 v[140:143], a[232:235], a[24:27], v[140:143]\n"
"buffer_store_dwordx4 v[208:211], v1, s[20:23], s38 offen offset:1024 sc1\n"
"v_mfma_f32_16x16x32_bf16 v[132:135], a[232:235], a[20:23], 0\n"
"v_accvgpr_read_b32 v216, a136\n"
"v_accvgpr_read_b32 v217, a137\n"
"v_mfma_f32_16x16x32_bf16 v[132:135], a[236:239], a[48:51], v[132:135]\n"
"s_add_u32 m0, s46, 4096\n"
"v_accvgpr_read_b32 v218, a138\n"
"v_accvgpr_read_b32 v219, a139\n"
"v_accvgpr_read_b32 v220, a164\n"
"v_mfma_f32_16x16x32_bf16 v[124:127], a[236:239], a[44:47], 0\n"
"s_waitcnt lgkmcnt(0)\n"
"v_accvgpr_read_b32 v221, a165\n"
"v_accvgpr_read_b32 v222, a166\n"
"v_accvgpr_read_b32 v223, a167\n"
"v_mfma_f32_16x16x32_bf16 v[124:127], a[232:235], a[16:19], v[124:127]\n"
"buffer_load_dwordx4 v14, s[24:27], s74 offen lds\n"
"v_mul_f32 v216, v216, v30\n"
"v_mfma_f32_16x16x32_bf16 v[116:119], a[232:235], a[12:15], 0\n"
"v_mul_f32 v217, v217, v30\n"
"v_mul_f32 v218, v218, v30\n"
"v_mul_f32 v219, v219, v30\n"
"v_mfma_f32_16x16x32_bf16 v[116:119], a[236:239], a[40:43], v[116:119]\n"
"v_mul_f32 v220, v220, v30\n"
"v_mul_f32 v221, v221, v30\n"
"v_mfma_f32_16x16x32_bf16 v[108:111], a[236:239], a[36:39], 0\n"
"ds_read_b64_tr_b16 a[224:225], v11 offset:32768\n"
"v_mul_f32 v222, v222, v30\n"
"v_mul_f32 v223, v223, v30\n"
"v_mfma_f32_16x16x32_bf16 v[108:111], a[232:235], a[8:11], v[108:111]\n"
"buffer_load_dwordx4 v15, s[24:27], s74 offen offset:1024 lds\n"
"v_cvt_pk_bf16_f32 v216, v216, v217\n"
"v_cvt_pk_bf16_f32 v217, v218, v219\n"
"v_mfma_f32_16x16x32_bf16 v[100:103], a[232:235], a[4:7], 0\n"
"ds_read_b64_tr_b16 a[226:227], v11 offset:33792\n"
"v_cvt_pk_bf16_f32 v218, v220, v221\n"
"v_mfma_f32_16x16x32_bf16 v[100:103], a[236:239], a[32:35], v[100:103]\n"
"v_cvt_pk_bf16_f32 v219, v222, v223\n"
"v_accvgpr_read_b32 v200, a168\n"
"v_accvgpr_read_b32 v201, a169\n"
"v_mfma_f32_16x16x32_bf16 v[92:95], a[236:239], a[28:31], 0\n"
"ds_read_b64_tr_b16 a[228:229], v11 offset:34816\n"
"v_accvgpr_read_b32 v202, a170\n"
"v_mfma_f32_16x16x32_bf16 v[92:95], a[232:235], a[0:3], v[92:95]\n"
"buffer_load_dwordx4 v21, s[24:27], s74 offen offset:2048 lds\n"
"v_accvgpr_read_b32 v203, a171\n"
"v_accvgpr_read_b32 v204, a196\n"
"v_mfma_f32_16x16x32_bf16 v[88:91], a[240:243], a[56:59], v[88:91]\n"
"ds_read_b64_tr_b16 a[230:231], v11 offset:35840\n"
"v_accvgpr_read_b32 v205, a197\n"
"v_accvgpr_read_b32 v206, a198\n"
"v_mfma_f32_16x16x32_bf16 v[88:91], a[244:247], a[84:87], v[88:91]\n"
"buffer_store_dwordx4 v[216:219], v1, s[20:23], s38 offen offset:2048 sc1\n"
"v_mfma_f32_16x16x32_bf16 v[96:99], a[244:247], a[88:91], v[96:99]\n"
"buffer_load_dwordx4 v23, s[24:27], s74 offen offset:3072 lds\n"
"v_accvgpr_read_b32 v207, a199\n"
"v_mul_f32 v200, v200, v18\n"
"v_mfma_f32_16x16x32_bf16 v[96:99], a[240:243], a[60:63], v[96:99]\n"
"s_xor_b32 s38, s50, 49152\n"
"v_mul_f32 v201, v201, v18\n"
"v_mul_f32 v202, v202, v18\n"
"v_mfma_f32_16x16x32_bf16 v[104:107], a[240:243], a[64:67], v[104:107]\n"
"v_mul_f32 v203, v203, v18\n"
"v_mul_f32 v204, v204, v18\n"
"v_mul_f32 v205, v205, v18\n"
"v_mfma_f32_16x16x32_bf16 v[104:107], a[244:247], a[92:95], v[104:107]\n"
"v_mul_f32 v206, v206, v18\n"
"v_mul_f32 v207, v207, v18\n"
"v_cvt_pk_bf16_f32 v200, v200, v201\n"
"v_mfma_f32_16x16x32_bf16 v[112:115], a[244:247], a[96:99], v[112:115]\n"
"v_cvt_pk_bf16_f32 v201, v202, v203\n"
"v_cvt_pk_bf16_f32 v202, v204, v205\n"
"v_mfma_f32_16x16x32_bf16 v[112:115], a[240:243], a[68:71], v[112:115]\n"
"v_cvt_pk_bf16_f32 v203, v206, v207\n"
"v_accvgpr_read_b32 v208, a172\n"
"v_accvgpr_read_b32 v209, a173\n"
"v_mfma_f32_16x16x32_bf16 v[120:123], a[240:243], a[72:75], v[120:123]\n"
"ds_read_b64_tr_b16 a[236:237], v12 offset:34816\n"
"v_accvgpr_read_b32 v210, a174\n"
"v_mfma_f32_16x16x32_bf16 v[120:123], a[244:247], a[100:103], v[120:123]\n"
"ds_read_b64_tr_b16 a[238:239], v12 offset:35840\n"
"v_accvgpr_read_b32 v211, a175\n"
"v_accvgpr_read_b32 v212, a200\n"
"v_mfma_f32_16x16x32_bf16 v[128:131], a[244:247], a[104:107], v[128:131]\n"
"ds_read_b64_tr_b16 a[232:233], v12 offset:32768\n"
"v_accvgpr_read_b32 v213, a201\n"
"v_accvgpr_read_b32 v214, a202\n"
"v_mfma_f32_16x16x32_bf16 v[128:131], a[240:243], a[76:79], v[128:131]\n"
"buffer_store_dwordx4 v[200:203], v1, s[20:23], s38 offen sc1\n"
"v_mfma_f32_16x16x32_bf16 v[136:139], a[240:243], a[80:83], v[136:139]\n"
"ds_read_b64_tr_b16 a[234:235], v12 offset:33792\n"
"v_accvgpr_read_b32 v215, a203\n"
"v_mul_f32 v208, v208, v20\n"
"v_mfma_f32_16x16x32_bf16 v[136:139], a[244:247], a[108:111], v[136:139]\n"
"v_mul_f32 v209, v209, v20\n"
"v_mul_f32 v210, v210, v20\n"
"v_mfma_f32_16x16x32_bf16 v[140:143], a[252:255], a[108:111], v[140:143]\n"
"v_mul_f32 v211, v211, v20\n"
"v_mul_f32 v212, v212, v20\n"
"v_mul_f32 v213, v213, v20\n"
"v_mfma_f32_16x16x32_bf16 v[140:143], a[248:251], a[80:83], v[140:143]\n"
"v_mul_f32 v214, v214, v20\n"
"v_mul_f32 v215, v215, v20\n"
"v_cvt_pk_bf16_f32 v208, v208, v209\n"
"v_mfma_f32_16x16x32_bf16 v[132:135], a[248:251], a[76:79], v[132:135]\n"
"s_waitcnt vmcnt(9)\n"
"s_barrier\n"
"v_cmp_ne_u64 vcc, s[68:69], v[16:17]\n"
"s_cbranch_vccnz 81f\n"
"91:\n"
"v_mfma_f32_16x16x32_bf16 v[132:135], a[252:255], a[104:107], v[132:135]\n"
"s_xor_b32 s76, s51, 0\n"
"buffer_load_dwordx4 v[224:227], v1, s[20:23], s76 offen sc1\n"
"v_cvt_pk_bf16_f32 v209, v210, v211\n"
"v_cvt_pk_bf16_f32 v210, v212, v213\n"
"v_mfma_f32_16x16x32_bf16 v[124:127], a[252:255], a[100:103], v[124:127]\n"
"v_cvt_pk_bf16_f32 v211, v214, v215\n"
"v_accvgpr_read_b32 v216, a176\n"
"v_mfma_f32_16x16x32_bf16 v[124:127], a[248:251], a[72:75], v[124:127]\n"
"buffer_store_dwordx4 v[208:211], v1, s[20:23], s38 offen offset:1024 sc1\n"
"v_mfma_f32_16x16x32_bf16 v[116:119], a[248:251], a[68:71], v[116:119]\n"
"v_accvgpr_read_b32 v217, a177\n"
"v_accvgpr_read_b32 v218, a178\n"
"v_accvgpr_read_b32 v219, a179\n"
"v_mfma_f32_16x16x32_bf16 v[116:119], a[252:255], a[96:99], v[116:119]\n"
"buffer_load_dwordx4 v[228:231], v1, s[20:23], s76 offen offset:1024 sc1\n"
"v_accvgpr_read_b32 v220, a204\n"
"v_mfma_f32_16x16x32_bf16 v[108:111], a[252:255], a[92:95], v[108:111]\n"
"ds_read_b64_tr_b16 a[240:241], v11 offset:36864\n"
"v_accvgpr_read_b32 v221, a205\n"
"v_accvgpr_read_b32 v222, a206\n"
"v_mfma_f32_16x16x32_bf16 v[108:111], a[248:251], a[64:67], v[108:111]\n"
"v_accvgpr_read_b32 v223, a207\n"
"v_mul_f32 v216, v216, v22\n"
"v_mfma_f32_16x16x32_bf16 v[100:103], a[248:251], a[60:63], v[100:103]\n"
"ds_read_b64_tr_b16 a[242:243], v11 offset:37888\n"
"v_mul_f32 v217, v217, v22\n"
"v_mul_f32 v218, v218, v22\n"
"v_mfma_f32_16x16x32_bf16 v[100:103], a[252:255], a[88:91], v[100:103]\n"
"ds_read_b128 a[112:115], v6 offset:32768\n"
"v_mul_f32 v219, v219, v22\n"
"v_mul_f32 v220, v220, v22\n"
"v_mfma_f32_16x16x32_bf16 v[92:95], a[252:255], a[84:87], v[92:95]\n"
"s_waitcnt lgkmcnt(9)\n"
"ds_read_b64_tr_b16 a[244:245], v11 offset:38912\n"
"v_mul_f32 v221, v221, v22\n"
"v_mfma_f32_16x16x32_bf16 v[92:95], a[248:251], a[56:59], v[92:95]\n"
"buffer_load_dwordx4 v[232:235], v1, s[20:23], s76 offen offset:2048 sc1\n"
"v_mul_f32 v222, v222, v22\n"
"v_mul_f32 v223, v223, v22\n"
"v_mfma_f32_16x16x32_bf16 v[144:147], a[224:227], a[0:3], 0\n"
"s_waitcnt lgkmcnt(8)\n"
"ds_read_b64_tr_b16 a[246:247], v11 offset:39936\n"
"v_cvt_pk_bf16_f32 v216, v216, v217\n"
"v_mfma_f32_16x16x32_bf16 v[144:147], a[228:231], a[28:31], v[144:147]\n"
"ds_read_b128 a[140:143], v6 offset:34816\n"
"v_cvt_pk_bf16_f32 v217, v218, v219\n"
"v_cvt_pk_bf16_f32 v218, v220, v221\n"
"v_mfma_f32_16x16x32_bf16 v[152:155], a[228:231], a[32:35], 0\n"
"ds_read_b128 a[144:147], v6 offset:38912\n"
"v_cvt_pk_bf16_f32 v219, v222, v223\n"
"v_accvgpr_read_b32 v200, a180\n"
"v_mfma_f32_16x16x32_bf16 v[152:155], a[224:227], a[4:7], v[152:155]\n"
"buffer_load_dwordx4 v[236:239], v1, s[20:23], s76 offen offset:3072 sc1\n"
"v_accvgpr_read_b32 v201, a181\n"
"v_mfma_f32_16x16x32_bf16 v[160:163], a[224:227], a[8:11], 0\n"
"ds_read_b128 a[116:119], v6 offset:36864\n"
"v_accvgpr_read_b32 v202, a182\n"
"v_accvgpr_read_b32 v203, a183\n"
"v_mfma_f32_16x16x32_bf16 v[160:163], a[228:231], a[36:39], v[160:163]\n"
"ds_read_b128 a[120:123], v6 offset:40960\n"
"v_accvgpr_read_b32 v204, a208\n"
"v_mfma_f32_16x16x32_bf16 v[168:171], a[228:231], a[40:43], 0\n"
"ds_read_b128 a[148:151], v6 offset:43008\n"
"v_accvgpr_read_b32 v205, a209\n"
"v_accvgpr_read_b32 v206, a210\n"
"v_mfma_f32_16x16x32_bf16 v[168:171], a[224:227], a[12:15], v[168:171]\n"
"s_xor_b32 s76, s51, 4096\n"
"buffer_load_dwordx4 v[240:243], v1, s[20:23], s76 offen sc1\n"
"v_accvgpr_read_b32 v207, a211\n"
"v_mul_f32 v200, v200, v24\n"
"v_mfma_f32_16x16x32_bf16 v[176:179], a[224:227], a[16:19], 0\n"
"ds_read_b64_tr_b16 a[252:253], v12 offset:38912\n"
"v_mul_f32 v201, v201, v24\n"
"v_mfma_f32_16x16x32_bf16 v[176:179], a[228:231], a[44:47], v[176:179]\n"
"ds_read_b64_tr_b16 a[254:255], v12 offset:39936\n"
"v_mul_f32 v202, v202, v24\n"
"v_mul_f32 v203, v203, v24\n"
"v_mfma_f32_16x16x32_bf16 v[184:187], a[228:231], a[48:51], 0\n"
"ds_read_b64_tr_b16 a[248:249], v12 offset:36864\n"
"v_mul_f32 v204, v204, v24\n"
"v_mfma_f32_16x16x32_bf16 v[184:187], a[224:227], a[20:23], v[184:187]\n"
"buffer_load_dwordx4 v[244:247], v1, s[20:23], s76 offen offset:1024 sc1\n"
"v_mul_f32 v205, v205, v24\n"
"v_mul_f32 v206, v206, v24\n"
"v_mfma_f32_16x16x32_bf16 v[192:195], a[224:227], a[24:27], 0\n"
"s_waitcnt lgkmcnt(15)\n"
"ds_read_b64_tr_b16 a[250:251], v12 offset:37888\n"
"v_mul_f32 v207, v207, v24\n"
"v_cvt_pk_bf16_f32 v200, v200, v201\n"
"v_mfma_f32_16x16x32_bf16 v[192:195], a[228:231], a[52:55], v[192:195]\n"
"ds_read_b128 a[152:155], v6 offset:47104\n"
"v_cvt_pk_bf16_f32 v201, v202, v203\n"
"v_mfma_f32_16x16x32_bf16 v[196:199], a[236:239], a[52:55], 0\n"
"s_waitcnt lgkmcnt(15)\n"
"ds_read_b128 a[124:127], v6 offset:45056\n"
"v_cvt_pk_bf16_f32 v202, v204, v205\n"
"v_cvt_pk_bf16_f32 v203, v206, v207\n"
"v_mfma_f32_16x16x32_bf16 v[196:199], a[232:235], a[24:27], v[196:199]\n"
"buffer_load_dwordx4 v[248:251], v1, s[20:23], s76 offen offset:2048 sc1\n"
"v_accvgpr_read_b32 v208, a184\n"
"v_mfma_f32_16x16x32_bf16 v[188:191], a[232:235], a[20:23], 0\n"
"ds_read_b128 a[128:131], v6 offset:49152\n"
"v_accvgpr_read_b32 v209, a185\n"
"v_accvgpr_read_b32 v210, a186\n"
"v_mfma_f32_16x16x32_bf16 v[188:191], a[236:239], a[48:51], v[188:191]\n"
"ds_read_b128 a[156:159], v6 offset:51200\n"
"v_accvgpr_read_b32 v211, a187\n"
"v_accvgpr_read_b32 v212, a212\n"
"v_mfma_f32_16x16x32_bf16 v[180:183], a[236:239], a[44:47], 0\n"
"ds_read_b128 a[160:163], v6 offset:55296\n"
"v_accvgpr_read_b32 v213, a213\n"
"v_mfma_f32_16x16x32_bf16 v[180:183], a[232:235], a[16:19], v[180:183]\n"
"buffer_store_dwordx4 v[216:219], v1, s[20:23], s38 offen offset:2048 sc1\n"
"v_mfma_f32_16x16x32_bf16 v[172:175], a[232:235], a[12:15], 0\n"
"ds_read_b128 a[132:135], v6 offset:53248\n"
"v_accvgpr_read_b32 v214, a214\n"
"v_mfma_f32_16x16x32_bf16 v[172:175], a[236:239], a[40:43], v[172:175]\n"
"ds_read_b128 a[136:139], v6 offset:57344\n"
"v_accvgpr_read_b32 v215, a215\n"
"v_mul_f32 v208, v208, v26\n"
"v_mfma_f32_16x16x32_bf16 v[164:167], a[236:239], a[36:39], 0\n"
"ds_read_b64_tr_b16 a[224:225], v2 offset:0\n"
"v_mul_f32 v209, v209, v26\n"
"v_mul_f32 v210, v210, v26\n"
"v_mfma_f32_16x16x32_bf16 v[164:167], a[232:235], a[8:11], v[164:167]\n"
"buffer_store_dwordx4 v[200:203], v1, s[20:23], s38 offen offset:3072 sc1\n"
"v_mfma_f32_16x16x32_bf16 v[156:159], a[232:235], a[4:7], 0\n"
"ds_read_b64_tr_b16 a[226:227], v2 offset:1024\n"
"v_mul_f32 v211, v211, v26\n"
"v_mul_f32 v212, v212, v26\n"
"v_mfma_f32_16x16x32_bf16 v[156:159], a[236:239], a[32:35], v[156:159]\n"
"s_xor_b32 s38, s50, 53248\n"
"ds_read_b128 a[164:167], v6 offset:59392\n"
"v_mul_f32 v213, v213, v26\n"
"v_mfma_f32_16x16x32_bf16 v[148:151], a[236:239], a[28:31], 0\n"
"s_waitcnt lgkmcnt(15)\n"
"ds_read_b64_tr_b16 a[228:229], v2 offset:2048\n"
"v_mul_f32 v214, v214, v26\n"
"v_mul_f32 v215, v215, v26\n"
"v_mfma_f32_16x16x32_bf16 v[148:151], a[232:235], a[0:3], v[148:151]\n"
"v_cvt_pk_bf16_f32 v208, v208, v209\n"
"v_cvt_pk_bf16_f32 v209, v210, v211\n"
"v_cvt_pk_bf16_f32 v210, v212, v213\n"
"v_mfma_f32_16x16x32_bf16 v[144:147], a[240:243], a[56:59], v[144:147]\n"
"s_waitcnt lgkmcnt(15)\n"
"ds_read_b64_tr_b16 a[230:231], v2 offset:3072\n"
"v_cvt_pk_bf16_f32 v211, v214, v215\n"
"v_mfma_f32_16x16x32_bf16 v[144:147], a[244:247], a[84:87], v[144:147]\n"
"buffer_store_dwordx4 v[208:211], v1, s[20:23], s38 offen sc1\n"
"v_mfma_f32_16x16x32_bf16 v[152:155], a[244:247], a[88:91], v[152:155]\n"
"v_accvgpr_read_b32 v216, a188\n"
"v_accvgpr_read_b32 v217, a189\n"
"v_mfma_f32_16x16x32_bf16 v[152:155], a[240:243], a[60:63], v[152:155]\n"
"v_accvgpr_read_b32 v218, a190\n"
"v_accvgpr_read_b32 v219, a191\n"
"v_accvgpr_read_b32 v220, a216\n"
"v_mfma_f32_16x16x32_bf16 v[160:163], a[240:243], a[64:67], v[160:163]\n"
"v_accvgpr_read_b32 v221, a217\n"
"v_accvgpr_read_b32 v222, a218\n"
"v_accvgpr_read_b32 v223, a219\n"
"v_mfma_f32_16x16x32_bf16 v[160:163], a[244:247], a[92:95], v[160:163]\n"
"v_mul_f32 v216, v216, v28\n"
"v_mul_f32 v217, v217, v28\n"
"v_mfma_f32_16x16x32_bf16 v[168:171], a[244:247], a[96:99], v[168:171]\n"
"v_mul_f32 v218, v218, v28\n"
"v_mul_f32 v219, v219, v28\n"
"v_mul_f32 v220, v220, v28\n"
"v_mfma_f32_16x16x32_bf16 v[168:171], a[240:243], a[68:71], v[168:171]\n"
"v_mul_f32 v221, v221, v28\n"
"v_mul_f32 v222, v222, v28\n"
"v_mfma_f32_16x16x32_bf16 v[176:179], a[240:243], a[72:75], v[176:179]\n"
"ds_read_b64_tr_b16 a[236:237], v3 offset:2048\n"
"v_mul_f32 v223, v223, v28\n"
"v_cvt_pk_bf16_f32 v216, v216, v217\n"
"v_mfma_f32_16x16x32_bf16 v[176:179], a[244:247], a[100:103], v[176:179]\n"
"ds_read_b64_tr_b16 a[238:239], v3 offset:3072\n"
"v_cvt_pk_bf16_f32 v217, v218, v219\n"
"v_cvt_pk_bf16_f32 v218, v220, v221\n"
"v_mfma_f32_16x16x32_bf16 v[184:187], a[244:247], a[104:107], v[184:187]\n"
"ds_read_b64_tr_b16 a[232:233], v3 offset:0\n"
"v_cvt_pk_bf16_f32 v219, v222, v223\n"
"v_mfma_f32_16x16x32_bf16 v[184:187], a[240:243], a[76:79], v[184:187]\n"
"s_xor_b32 s75, s48, 262144\n"
"buffer_store_dwordx4 v[216:219], v1, s[20:23], s38 offen offset:1024 sc1\n"
"v_mfma_f32_16x16x32_bf16 v[192:195], a[240:243], a[80:83], v[192:195]\n"
"s_waitcnt lgkmcnt(15)\n"
"ds_read_b64_tr_b16 a[234:235], v3 offset:1024\n"
"v_accvgpr_read_b32 v200, a192\n"
"v_mfma_f32_16x16x32_bf16 v[192:195], a[244:247], a[108:111], v[192:195]\n"
"v_accvgpr_read_b32 v201, a193\n"
"v_accvgpr_read_b32 v202, a194\n"
"v_accvgpr_read_b32 v203, a195\n"
"v_mfma_f32_16x16x32_bf16 v[196:199], a[252:255], a[108:111], v[196:199]\n"
"s_waitcnt lgkmcnt(15)\n"
"v_accvgpr_read_b32 v204, a220\n"
"v_accvgpr_read_b32 v205, a221\n"
"v_accvgpr_read_b32 v206, a222\n"
"v_mfma_f32_16x16x32_bf16 v[196:199], a[248:251], a[80:83], v[196:199]\n"
"v_accvgpr_read_b32 v207, a223\n"
"v_mul_f32 v200, v200, v30\n"
"v_mfma_f32_16x16x32_bf16 v[188:191], a[248:251], a[76:79], v[188:191]\n"
"v_mul_f32 v201, v201, v30\n"
"v_mul_f32 v202, v202, v30\n"
"v_mul_f32 v203, v203, v30\n"
"v_mfma_f32_16x16x32_bf16 v[188:191], a[252:255], a[104:107], v[188:191]\n"
"s_add_u32 m0, s46, 131072\n"
"v_mul_f32 v204, v204, v30\n"
"v_mul_f32 v205, v205, v30\n"
"v_mfma_f32_16x16x32_bf16 v[180:183], a[252:255], a[100:103], v[180:183]\n"
"s_waitcnt lgkmcnt(0)\n"
"v_mul_f32 v206, v206, v30\n"
"v_mul_f32 v207, v207, v30\n"
"v_cvt_pk_bf16_f32 v200, v200, v201\n"
"v_mfma_f32_16x16x32_bf16 v[180:183], a[248:251], a[72:75], v[180:183]\n"
"buffer_load_dwordx4 v14, s[24:27], s75 offen lds\n"
"v_cvt_pk_bf16_f32 v201, v202, v203\n"
"v_cvt_pk_bf16_f32 v202, v204, v205\n"
"v_mfma_f32_16x16x32_bf16 v[172:175], a[248:251], a[68:71], v[172:175]\n"
"v_cvt_pk_bf16_f32 v203, v206, v207\n"
"v_add_u32 v4, s46, v1\n"
"v_mfma_f32_16x16x32_bf16 v[172:175], a[252:255], a[96:99], v[172:175]\n"
"buffer_store_dwordx4 v[200:203], v1, s[20:23], s38 offen offset:2048 sc1\n"
"v_mfma_f32_16x16x32_bf16 v[164:167], a[252:255], a[92:95], v[164:167]\n"
"ds_read_b64_tr_b16 a[240:241], v2 offset:4096\n"
"v_lshlrev_b32 v5, 14, v1\n"
"v_mfma_f32_16x16x32_bf16 v[164:167], a[248:251], a[64:67], v[164:167]\n"
"buffer_load_dwordx4 v15, s[24:27], s75 offen offset:1024 lds\n"
"v_and_b32 v5, 262144, v5\n"
"v_mfma_f32_16x16x32_bf16 v[156:159], a[248:251], a[60:63], v[156:159]\n"
"ds_read_b64_tr_b16 a[242:243], v2 offset:5120\n"
"v_xor_b32 v10, 128, v1\n"
"v_mfma_f32_16x16x32_bf16 v[156:159], a[252:255], a[88:91], v[156:159]\n"
"v_lshrrev_b32 v18, 8, v1\n"
"v_lshrrev_b32 v20, 5, v1\n"
"v_mfma_f32_16x16x32_bf16 v[148:151], a[252:255], a[84:87], v[148:151]\n"
"s_barrier\n"
"ds_read_b64_tr_b16 a[244:245], v2 offset:6144\n"
"v_mfma_f32_16x16x32_bf16 v[148:151], a[248:251], a[56:59], v[148:151]\n"
"buffer_load_dwordx4 v21, s[24:27], s75 offen offset:2048 lds\n"
"v_and_b32 v20, 2, v20\n"
"v_mfma_f32_16x16x32_bf16 v[32:35], a[224:227], a[112:115], v[32:35]\n"
"ds_read_b128 a[168:171], v6 offset:33792\n"
"v_xor_b32 v18, v18, v20\n"
"v_mfma_f32_16x16x32_bf16 v[32:35], a[228:231], a[140:143], v[32:35]\n"
"ds_read_b128 a[196:199], v6 offset:35840\n"
"v_and_b32 v20, 16, v1\n"
"v_mfma_f32_16x16x32_bf16 v[40:43], a[228:231], a[144:147], v[40:43]\n"
"ds_read_b64_tr_b16 a[246:247], v2 offset:7168\n"
"v_mov_b32 v22, 16256\n"
"v_mfma_f32_16x16x32_bf16 v[40:43], a[224:227], a[116:119], v[40:43]\n"
"buffer_load_dwordx4 v23, s[24:27], s75 offen offset:3072 lds\n"
"v_lshlrev_b32 v20, v20, v22\n"
"v_mfma_f32_16x16x32_bf16 v[48:51], a[224:227], a[120:123], v[48:51]\n"
"ds_read_b128 a[200:203], v6 offset:39936\n"
"v_lshlrev_b32 v18, 2, v18\n"
"v_mfma_f32_16x16x32_bf16 v[48:51], a[228:231], a[148:151], v[48:51]\n"
"ds_read_b128 a[172:175], v6 offset:37888\n"
"v_bfe_u32 v22, v1, 5, 1\n"
"v_mfma_f32_16x16x32_bf16 v[56:59], a[228:231], a[152:155], v[56:59]\n"
"ds_read_b128 a[176:179], v6 offset:41984\n"
"v_or_b32 v18, v18, v22\n"
"v_mfma_f32_16x16x32_bf16 v[56:59], a[224:227], a[124:127], v[56:59]\n"
"v_bfe_u32 v22, v1, 7, 1\n"
"v_lshl_or_b32 v18, v22, 1, v18\n"
"v_mfma_f32_16x16x32_bf16 v[64:67], a[224:227], a[128:131], v[64:67]\n"
"ds_read_b128 a[204:207], v6 offset:44032\n"
"v_cmp_eq_u32 s[70:71], 0, v18\n"
"v_mfma_f32_16x16x32_bf16 v[64:67], a[228:231], a[156:159], v[64:67]\n"
"ds_read_b128 a[208:211], v6 offset:48128\n"
"v_mfma_f32_16x16x32_bf16 v[72:75], a[228:231], a[160:163], v[72:75]\n"
"ds_read_b128 a[180:183], v6 offset:46080\n"
"v_cndmask_b32 v24, 0, v20, s[70:71]\n"
"v_mfma_f32_16x16x32_bf16 v[72:75], a[224:227], a[132:135], v[72:75]\n"
"v_cmp_eq_u32 s[70:71], 1, v18\n"
"v_mfma_f32_16x16x32_bf16 v[80:83], a[224:227], a[136:139], v[80:83]\n"
"ds_read_b128 a[184:187], v6 offset:50176\n"
"v_mfma_f32_16x16x32_bf16 v[80:83], a[228:231], a[164:167], v[80:83]\n"
"ds_read_b128 a[212:215], v6 offset:52224\n"
"v_cndmask_b32 v25, 0, v20, s[70:71]\n"
"v_mfma_f32_16x16x32_bf16 v[84:87], a[236:239], a[164:167], v[84:87]\n"
"ds_read_b128 a[216:219], v6 offset:56320\n"
"v_cmp_eq_u32 s[70:71], 2, v18\n"
"v_mfma_f32_16x16x32_bf16 v[84:87], a[232:235], a[136:139], v[84:87]\n"
"v_mfma_f32_16x16x32_bf16 v[76:79], a[232:235], a[132:135], v[76:79]\n"
"ds_read_b128 a[188:191], v6 offset:54272\n"
"v_cndmask_b32 v26, 0, v20, s[70:71]\n"
"v_mfma_f32_16x16x32_bf16 v[76:79], a[236:239], a[160:163], v[76:79]\n"
"ds_read_b128 a[192:195], v6 offset:58368\n"
"v_cmp_eq_u32 s[70:71], 3, v18\n"
"v_mfma_f32_16x16x32_bf16 v[68:71], a[236:239], a[156:159], v[68:71]\n"
"s_waitcnt vmcnt(16)\n"
"ds_read_b128 a[220:223], v6 offset:60416\n"
"v_mfma_f32_16x16x32_bf16 v[68:71], a[232:235], a[128:131], v[68:71]\n"
"ds_write_b128 v4, v[224:227] offset:32768\n"
"v_mfma_f32_16x16x32_bf16 v[60:63], a[232:235], a[124:127], v[60:63]\n"
"ds_read_b64_tr_b16 a[252:253], v3 offset:6144\n"
"v_cndmask_b32 v27, 0, v20, s[70:71]\n"
"v_mfma_f32_16x16x32_bf16 v[60:63], a[236:239], a[152:155], v[60:63]\n"
"ds_read_b64_tr_b16 a[254:255], v3 offset:7168\n"
"v_cmp_eq_u32 s[70:71], 4, v18\n"
"v_mfma_f32_16x16x32_bf16 v[52:55], a[236:239], a[148:151], v[52:55]\n"
"s_waitcnt vmcnt(14)\n"
"ds_read_b64_tr_b16 a[248:249], v3 offset:4096\n"
"v_mfma_f32_16x16x32_bf16 v[52:55], a[232:235], a[120:123], v[52:55]\n"
"s_xor_b32 s74, s48, 294912\n"
"ds_write_b128 v4, v[228:231] offset:33792\n"
"v_mfma_f32_16x16x32_bf16 v[44:47], a[232:235], a[116:119], v[44:47]\n"
"ds_read_b64_tr_b16 a[250:251], v3 offset:5120\n"
"v_cndmask_b32 v28, 0, v20, s[70:71]\n"
"v_mfma_f32_16x16x32_bf16 v[44:47], a[236:239], a[144:147], v[44:47]\n"
"v_cmp_eq_u32 s[70:71], 5, v18\n"
"v_mfma_f32_16x16x32_bf16 v[36:39], a[236:239], a[140:143], v[36:39]\n"
"s_waitcnt vmcnt(13) lgkmcnt(15)\n"
"ds_read_b64_tr_b16 a[224:225], v11 offset:0\n"
"v_mfma_f32_16x16x32_bf16 v[36:39], a[232:235], a[112:115], v[36:39]\n"
"ds_write_b128 v4, v[232:235] offset:34816\n"
"v_mfma_f32_16x16x32_bf16 v[32:35], a[240:243], a[168:171], v[32:35]\n"
"s_waitcnt lgkmcnt(15)\n"
"ds_read_b64_tr_b16 a[226:227], v11 offset:1024\n"
"v_cndmask_b32 v29, 0, v20, s[70:71]\n"
"v_mfma_f32_16x16x32_bf16 v[32:35], a[244:247], a[196:199], v[32:35]\n"
"s_add_u32 m0, s46, 135168\n"
"v_cmp_eq_u32 s[70:71], 6, v18\n"
"v_mfma_f32_16x16x32_bf16 v[40:43], a[244:247], a[200:203], v[40:43]\n"
"s_waitcnt vmcnt(12) lgkmcnt(15)\n"
"ds_read_b64_tr_b16 a[228:229], v11 offset:2048\n"
"v_mfma_f32_16x16x32_bf16 v[40:43], a[240:243], a[172:175], v[40:43]\n"
"ds_write_b128 v4, v[236:239] offset:35840\n"
"v_mfma_f32_16x16x32_bf16 v[48:51], a[240:243], a[176:179], v[48:51]\n"
"s_waitcnt vmcnt(3) lgkmcnt(15)\n"
"buffer_store_dwordx2 v[8:9], v19, s[20:23], s60 offen offset:256 sc1\n"
"v_cndmask_b32 v30, 0, v20, s[70:71]\n"
"v_mfma_f32_16x16x32_bf16 v[48:51], a[244:247], a[204:207], v[48:51]\n"
"ds_read_b64_tr_b16 a[230:231], v11 offset:3072\n"
"v_cmp_eq_u32 s[70:71], 7, v18\n"
"5:\n"
"v_mfma_f32_16x16x32_bf16 v[56:59], a[244:247], a[208:211], v[56:59]\n"
"s_waitcnt vmcnt(6) lgkmcnt(6)\n"
"buffer_load_dwordx4 v14, s[24:27], s74 offen lds\n"
"v_mfma_f32_16x16x32_bf16 v[56:59], a[240:243], a[180:183], v[56:59]\n"
"ds_write_b128 v4, v[240:243] offset:36864\n"
"v_mfma_f32_16x16x32_bf16 v[64:67], a[240:243], a[184:187], v[64:67]\n"
"ds_read_b64_tr_b16 a[236:237], v12 offset:2048\n"
"v_cndmask_b32 v31, 0, v20, s[70:71]\n"
"v_mfma_f32_16x16x32_bf16 v[64:67], a[244:247], a[212:215], v[64:67]\n"
"ds_read_b64_tr_b16 a[238:239], v12 offset:3072\n"
"v_mfma_f32_16x16x32_bf16 v[72:75], a[244:247], a[216:219], v[72:75]\n"
"s_waitcnt vmcnt(6)\n"
"ds_read_b64_tr_b16 a[232:233], v12 offset:0\n"
"v_mfma_f32_16x16x32_bf16 v[72:75], a[240:243], a[188:191], v[72:75]\n"
"ds_write_b128 v4, v[244:247] offset:37888\n"
"v_mfma_f32_16x16x32_bf16 v[80:83], a[240:243], a[192:195], v[80:83]\n"
"ds_read_b64_tr_b16 a[234:235], v12 offset:1024\n"
"v_mfma_f32_16x16x32_bf16 v[80:83], a[244:247], a[220:223], v[80:83]\n"
"buffer_load_dwordx4 v15, s[24:27], s74 offen offset:1024 lds\n"
"v_mfma_f32_16x16x32_bf16 v[84:87], a[252:255], a[220:223], v[84:87]\n"
"v_mfma_f32_16x16x32_bf16 v[84:87], a[248:251], a[192:195], v[84:87]\n"
"s_xor_b32 s75, s48, 327680\n"
"v_mfma_f32_16x16x32_bf16 v[76:79], a[248:251], a[188:191], v[76:79]\n"
"s_waitcnt vmcnt(6)\n"
"ds_write_b128 v4, v[248:251] offset:38912\n"
"v_mfma_f32_16x16x32_bf16 v[76:79], a[252:255], a[216:219], v[76:79]\n"
"buffer_load_dwordx4 v21, s[24:27], s74 offen offset:2048 lds\n"
"v_mfma_f32_16x16x32_bf16 v[68:71], a[252:255], a[212:215], v[68:71]\n"
"v_mfma_f32_16x16x32_bf16 v[68:71], a[248:251], a[184:187], v[68:71]\n"
"v_mfma_f32_16x16x32_bf16 v[60:63], a[248:251], a[180:183], v[60:63]\n"
"s_waitcnt lgkmcnt(0)\n"
"s_barrier\n"
"buffer_load_dwordx4 v23, s[24:27], s74 offen offset:3072 lds\n"
"v_mfma_f32_16x16x32_bf16 v[60:63], a[252:255], a[208:211], v[60:63]\n"
"s_add_u32 m0, s46, 0\n"
"ds_read_b128 a[0:3], v1 offset:32768\n"
"v_mfma_f32_16x16x32_bf16 v[52:55], a[252:255], a[204:207], v[52:55]\n"
"ds_read_b64_tr_b16 a[240:241], v11 offset:4096\n"
"v_mfma_f32_16x16x32_bf16 v[52:55], a[248:251], a[176:179], v[52:55]\n"
"buffer_load_dwordx4 v14, s[24:27], s75 offen lds\n"
"v_mfma_f32_16x16x32_bf16 v[44:47], a[248:251], a[172:175], v[44:47]\n"
"ds_read_b64_tr_b16 a[242:243], v11 offset:5120\n"
"v_mfma_f32_16x16x32_bf16 v[44:47], a[252:255], a[200:203], v[44:47]\n"
"ds_read_b128 a[28:31], v1 offset:49152\n"
"v_mfma_f32_16x16x32_bf16 v[36:39], a[252:255], a[196:199], v[36:39]\n"
"ds_read_b64_tr_b16 a[244:245], v11 offset:6144\n"
"v_mfma_f32_16x16x32_bf16 v[36:39], a[248:251], a[168:171], v[36:39]\n"
"buffer_load_dwordx4 v15, s[24:27], s75 offen offset:1024 lds\n"
"v_mfma_f32_16x16x32_bf16 v[88:91], a[224:227], a[112:115], v[88:91]\n"
"ds_read_b64_tr_b16 a[246:247], v11 offset:7168\n"
"v_mfma_f32_16x16x32_bf16 v[88:91], a[228:231], a[140:143], v[88:91]\n"
"ds_read_b128 a[32:35], v1 offset:50176\n"
"v_mfma_f32_16x16x32_bf16 v[96:99], a[228:231], a[144:147], v[96:99]\n"
"ds_read_b128 a[4:7], v1 offset:33792\n"
"v_mfma_f32_16x16x32_bf16 v[96:99], a[224:227], a[116:119], v[96:99]\n"
"buffer_load_dwordx4 v21, s[24:27], s75 offen offset:2048 lds\n"
"v_mfma_f32_16x16x32_bf16 v[104:107], a[224:227], a[120:123], v[104:107]\n"
"ds_read_b128 a[8:11], v1 offset:34816\n"
"v_mfma_f32_16x16x32_bf16 v[104:107], a[228:231], a[148:151], v[104:107]\n"
"ds_read_b128 a[36:39], v1 offset:51200\n"
"v_mfma_f32_16x16x32_bf16 v[112:115], a[228:231], a[152:155], v[112:115]\n"
"buffer_load_dwordx2 v[16:17], off, s[20:23], s61 offset:256 sc1\n"
"v_mfma_f32_16x16x32_bf16 v[112:115], a[224:227], a[124:127], v[112:115]\n"
"buffer_load_dwordx4 v23, s[24:27], s75 offen offset:3072 lds\n"
"v_mfma_f32_16x16x32_bf16 v[120:123], a[224:227], a[128:131], v[120:123]\n"
"ds_read_b64_tr_b16 a[252:253], v12 offset:6144\n"
"v_mfma_f32_16x16x32_bf16 v[120:123], a[228:231], a[156:159], v[120:123]\n"
"ds_read_b64_tr_b16 a[254:255], v12 offset:7168\n"
"v_mfma_f32_16x16x32_bf16 v[128:131], a[228:231], a[160:163], v[128:131]\n"
"ds_read_b64_tr_b16 a[248:249], v12 offset:4096\n"
"v_mfma_f32_16x16x32_bf16 v[128:131], a[224:227], a[132:135], v[128:131]\n"
"s_xor_b32 s74, s48, 360448\n"
"v_mfma_f32_16x16x32_bf16 v[136:139], a[224:227], a[136:139], v[136:139]\n"
"ds_read_b64_tr_b16 a[250:251], v12 offset:5120\n"
"v_mfma_f32_16x16x32_bf16 v[136:139], a[228:231], a[164:167], v[136:139]\n"
"ds_read_b128 a[40:43], v1 offset:52224\n"
"v_mfma_f32_16x16x32_bf16 v[140:143], a[236:239], a[164:167], v[140:143]\n"
"ds_read_b128 a[12:15], v1 offset:35840\n"
"v_mfma_f32_16x16x32_bf16 v[140:143], a[232:235], a[136:139], v[140:143]\n"
"v_mfma_f32_16x16x32_bf16 v[132:135], a[232:235], a[132:135], v[132:135]\n"
"ds_read_b128 a[16:19], v1 offset:36864\n"
"v_mfma_f32_16x16x32_bf16 v[132:135], a[236:239], a[160:163], v[132:135]\n"
"s_add_u32 m0, s46, 4096\n"
"ds_read_b128 a[44:47], v1 offset:53248\n"
"v_mfma_f32_16x16x32_bf16 v[124:127], a[236:239], a[156:159], v[124:127]\n"
"s_waitcnt lgkmcnt(4)\n"
"ds_read_b128 a[48:51], v1 offset:54272\n"
"v_mfma_f32_16x16x32_bf16 v[124:127], a[232:235], a[128:131], v[124:127]\n"
"buffer_load_dwordx4 v14, s[24:27], s74 offen lds\n"
"v_mfma_f32_16x16x32_bf16 v[116:119], a[232:235], a[124:127], v[116:119]\n"
"ds_read_b128 a[20:23], v1 offset:37888\n"
"v_mfma_f32_16x16x32_bf16 v[116:119], a[236:239], a[152:155], v[116:119]\n"
"ds_read_b128 a[24:27], v1 offset:38912\n"
"v_mfma_f32_16x16x32_bf16 v[108:111], a[236:239], a[148:151], v[108:111]\n"
"s_waitcnt vmcnt(12)\n"
"ds_read_b64_tr_b16 a[224:225], v2 offset:0\n"
"v_mfma_f32_16x16x32_bf16 v[108:111], a[232:235], a[120:123], v[108:111]\n"
"buffer_load_dwordx4 v15, s[24:27], s74 offen offset:1024 lds\n"
"v_mfma_f32_16x16x32_bf16 v[100:103], a[232:235], a[116:119], v[100:103]\n"
"ds_read_b64_tr_b16 a[226:227], v2 offset:1024\n"
"v_mfma_f32_16x16x32_bf16 v[100:103], a[236:239], a[144:147], v[100:103]\n"
"ds_read_b128 a[52:55], v1 offset:55296\n"
"v_mfma_f32_16x16x32_bf16 v[92:95], a[236:239], a[140:143], v[92:95]\n"
"s_waitcnt vmcnt(11)\n"
"ds_read_b64_tr_b16 a[228:229], v2 offset:2048\n"
"v_mfma_f32_16x16x32_bf16 v[92:95], a[232:235], a[112:115], v[92:95]\n"
"buffer_load_dwordx4 v21, s[24:27], s74 offen offset:2048 lds\n"
"v_mfma_f32_16x16x32_bf16 v[88:91], a[240:243], a[168:171], v[88:91]\n"
"ds_read_b64_tr_b16 a[230:231], v2 offset:3072\n"
"v_mfma_f32_16x16x32_bf16 v[88:91], a[244:247], a[196:199], v[88:91]\n"
"ds_read_b128 a[56:59], v1 offset:40960\n"
"v_mfma_f32_16x16x32_bf16 v[96:99], a[244:247], a[200:203], v[96:99]\n"
"ds_read_b128 a[84:87], v1 offset:57344\n"
"v_mfma_f32_16x16x32_bf16 v[96:99], a[240:243], a[172:175], v[96:99]\n"
"buffer_load_dwordx4 v23, s[24:27], s74 offen offset:3072 lds\n"
"v_mfma_f32_16x16x32_bf16 v[104:107], a[240:243], a[176:179], v[104:107]\n"
"ds_read_b128 a[88:91], v1 offset:58368\n"
"v_mfma_f32_16x16x32_bf16 v[104:107], a[244:247], a[204:207], v[104:107]\n"
"ds_read_b128 a[60:63], v1 offset:41984\n"
"v_mfma_f32_16x16x32_bf16 v[112:115], a[244:247], a[208:211], v[112:115]\n"
"ds_read_b128 a[64:67], v1 offset:43008\n"
"v_mfma_f32_16x16x32_bf16 v[112:115], a[240:243], a[180:183], v[112:115]\n"
"v_mfma_f32_16x16x32_bf16 v[120:123], a[240:243], a[184:187], v[120:123]\n"
"s_waitcnt vmcnt(5)\n"
"v_cmp_ne_u64 vcc, s[68:69], v[16:17]\n"
"s_cbranch_vccnz 42f\n"
"52:\n"
"v_mfma_f32_16x16x32_bf16 v[120:123], a[244:247], a[212:215], v[120:123]\n"
"s_xor_b32 s76, s51, 32768\n"
"ds_read_b64_tr_b16 a[236:237], v3 offset:2048\n"
"v_mfma_f32_16x16x32_bf16 v[128:131], a[244:247], a[216:219], v[128:131]\n"
"ds_read_b64_tr_b16 a[238:239], v3 offset:3072\n"
"v_mfma_f32_16x16x32_bf16 v[128:131], a[240:243], a[188:191], v[128:131]\n"
"buffer_load_dwordx4 v[200:203], v1, s[20:23], s76 offen sc1\n"
"v_mfma_f32_16x16x32_bf16 v[136:139], a[240:243], a[192:195], v[136:139]\n"
"ds_read_b64_tr_b16 a[232:233], v3 offset:0\n"
"v_mfma_f32_16x16x32_bf16 v[136:139], a[244:247], a[220:223], v[136:139]\n"
"s_xor_b32 s75, s48, 262400\n"
"ds_read_b64_tr_b16 a[234:235], v3 offset:1024\n"
"v_mfma_f32_16x16x32_bf16 v[140:143], a[252:255], a[220:223], v[140:143]\n"
"ds_read_b128 a[92:95], v1 offset:59392\n"
"v_mfma_f32_16x16x32_bf16 v[140:143], a[248:251], a[192:195], v[140:143]\n"
"buffer_load_dwordx4 v[204:207], v1, s[20:23], s76 offen offset:1024 sc1\n"
"v_mfma_f32_16x16x32_bf16 v[132:135], a[248:251], a[188:191], v[132:135]\n"
"ds_read_b128 a[96:99], v1 offset:60416\n"
"v_mfma_f32_16x16x32_bf16 v[132:135], a[252:255], a[216:219], v[132:135]\n"
"s_add_u32 m0, s46, 131072\n"
"ds_read_b128 a[68:71], v1 offset:44032\n"
"v_mfma_f32_16x16x32_bf16 v[124:127], a[252:255], a[212:215], v[124:127]\n"
"ds_read_b128 a[72:75], v1 offset:45056\n"
"v_mfma_f32_16x16x32_bf16 v[124:127], a[248:251], a[184:187], v[124:127]\n"
"buffer_load_dwordx4 v[208:211], v1, s[20:23], s76 offen offset:2048 sc1\n"
"v_mfma_f32_16x16x32_bf16 v[116:119], a[248:251], a[180:183], v[116:119]\n"
"ds_read_b128 a[100:103], v1 offset:61440\n"
"v_mfma_f32_16x16x32_bf16 v[116:119], a[252:255], a[208:211], v[116:119]\n"
"ds_read_b128 a[104:107], v1 offset:62464\n"
"v_mfma_f32_16x16x32_bf16 v[108:111], a[252:255], a[204:207], v[108:111]\n"
"ds_read_b64_tr_b16 a[240:241], v2 offset:4096\n"
"v_mfma_f32_16x16x32_bf16 v[108:111], a[248:251], a[176:179], v[108:111]\n"
"buffer_load_dwordx4 v[212:215], v1, s[20:23], s76 offen offset:3072 sc1\n"
"v_mfma_f32_16x16x32_bf16 v[100:103], a[248:251], a[172:175], v[100:103]\n"
"ds_read_b64_tr_b16 a[242:243], v2 offset:5120\n"
"v_mfma_f32_16x16x32_bf16 v[100:103], a[252:255], a[200:203], v[100:103]\n"
"ds_read_b128 a[76:79], v1 offset:46080\n"
"v_mfma_f32_16x16x32_bf16 v[92:95], a[252:255], a[196:199], v[92:95]\n"
"s_waitcnt lgkmcnt(15)\n"
"ds_read_b64_tr_b16 a[244:245], v2 offset:6144\n"
"v_mfma_f32_16x16x32_bf16 v[92:95], a[248:251], a[168:171], v[92:95]\n"
"s_xor_b32 s76, s51, 36864\n"
"buffer_load_dwordx4 v[216:219], v1, s[20:23], s76 offen sc1\n"
"v_mfma_f32_16x16x32_bf16 v[32:35], a[224:227], a[0:3], v[32:35]\n"
"s_waitcnt lgkmcnt(15)\n"
"ds_read_b64_tr_b16 a[246:247], v2 offset:7168\n"
"v_mfma_f32_16x16x32_bf16 v[32:35], a[228:231], a[28:31], v[32:35]\n"
"ds_read_b128 a[80:83], v1 offset:47104\n"
"v_mfma_f32_16x16x32_bf16 v[40:43], a[228:231], a[32:35], v[40:43]\n"
"ds_read_b128 a[108:111], v1 offset:63488\n"
"v_mfma_f32_16x16x32_bf16 v[40:43], a[224:227], a[4:7], v[40:43]\n"
"buffer_load_dwordx4 v[220:223], v1, s[20:23], s76 offen offset:1024 sc1\n"
"v_mfma_f32_16x16x32_bf16 v[48:51], a[224:227], a[8:11], v[48:51]\n"
"s_waitcnt lgkmcnt(13)\n"
"buffer_load_dwordx4 v14, s[24:27], s75 offen lds\n"
"v_mfma_f32_16x16x32_bf16 v[48:51], a[228:231], a[36:39], v[48:51]\n"
"v_mfma_f32_16x16x32_bf16 v[56:59], a[228:231], a[40:43], v[56:59]\n"
"v_mfma_f32_16x16x32_bf16 v[56:59], a[224:227], a[12:15], v[56:59]\n"
"buffer_load_dwordx4 v[252:255], v1, s[20:23], s76 offen offset:2048 sc1\n"
"v_mfma_f32_16x16x32_bf16 v[64:67], a[224:227], a[16:19], v[64:67]\n"
"s_waitcnt lgkmcnt(0)\n"
"s_barrier\n"
"ds_read_b64_tr_b16 a[252:253], v3 offset:6144\n"
"v_mfma_f32_16x16x32_bf16 v[64:67], a[228:231], a[44:47], v[64:67]\n"
"ds_read_b64_tr_b16 a[254:255], v3 offset:7168\n"
"v_mfma_f32_16x16x32_bf16 v[72:75], a[228:231], a[48:51], v[72:75]\n"
"s_waitcnt vmcnt(7)\n"
"ds_read_b64_tr_b16 a[248:249], v3 offset:4096\n"
"v_mfma_f32_16x16x32_bf16 v[72:75], a[224:227], a[20:23], v[72:75]\n"
"ds_write_b128 v4, v[200:203] offset:32768\n"
"v_mfma_f32_16x16x32_bf16 v[80:83], a[224:227], a[24:27], v[80:83]\n"
"ds_read_b64_tr_b16 a[250:251], v3 offset:5120\n"
"v_mfma_f32_16x16x32_bf16 v[80:83], a[228:231], a[52:55], v[80:83]\n"
"buffer_load_dwordx4 v15, s[24:27], s75 offen offset:1024 lds\n"
"v_mfma_f32_16x16x32_bf16 v[84:87], a[236:239], a[52:55], v[84:87]\n"
"s_waitcnt vmcnt(7)\n"
"v_mfma_f32_16x16x32_bf16 v[84:87], a[232:235], a[24:27], v[84:87]\n"
"s_xor_b32 s74, s48, 295168\n"
"ds_write_b128 v4, v[204:207] offset:33792\n"
"v_mfma_f32_16x16x32_bf16 v[76:79], a[232:235], a[20:23], v[76:79]\n"
"buffer_load_dwordx4 v21, s[24:27], s75 offen offset:2048 lds\n"
"v_mfma_f32_16x16x32_bf16 v[76:79], a[236:239], a[48:51], v[76:79]\n"
"v_mfma_f32_16x16x32_bf16 v[68:71], a[236:239], a[44:47], v[68:71]\n"
"s_waitcnt vmcnt(7)\n"
"v_mfma_f32_16x16x32_bf16 v[68:71], a[232:235], a[16:19], v[68:71]\n"
"ds_write_b128 v4, v[208:211] offset:34816\n"
"v_mfma_f32_16x16x32_bf16 v[60:63], a[232:235], a[12:15], v[60:63]\n"
"buffer_load_dwordx4 v23, s[24:27], s75 offen offset:3072 lds\n"
"v_mfma_f32_16x16x32_bf16 v[60:63], a[236:239], a[40:43], v[60:63]\n"
"s_add_u32 m0, s46, 135168\n"
"v_mfma_f32_16x16x32_bf16 v[52:55], a[236:239], a[36:39], v[52:55]\n"
"s_waitcnt vmcnt(7)\n"
"ds_read_b64_tr_b16 a[224:225], v11 offset:0\n"
"v_mfma_f32_16x16x32_bf16 v[52:55], a[232:235], a[8:11], v[52:55]\n"
"ds_write_b128 v4, v[212:215] offset:35840\n"
"v_mfma_f32_16x16x32_bf16 v[44:47], a[232:235], a[4:7], v[44:47]\n"
"s_waitcnt lgkmcnt(4)\n"
"ds_read_b64_tr_b16 a[226:227], v11 offset:1024\n"
"v_mfma_f32_16x16x32_bf16 v[44:47], a[236:239], a[32:35], v[44:47]\n"
"buffer_load_dwordx4 v14, s[24:27], s74 offen lds\n"
"v_mfma_f32_16x16x32_bf16 v[36:39], a[236:239], a[28:31], v[36:39]\n"
"s_waitcnt vmcnt(7)\n"
"ds_read_b64_tr_b16 a[228:229], v11 offset:2048\n"
"v_mfma_f32_16x16x32_bf16 v[36:39], a[232:235], a[0:3], v[36:39]\n"
"ds_write_b128 v4, v[216:219] offset:36864\n"
"v_mfma_f32_16x16x32_bf16 v[32:35], a[240:243], a[56:59], v[32:35]\n"
"ds_read_b64_tr_b16 a[230:231], v11 offset:3072\n"
"v_mfma_f32_16x16x32_bf16 v[32:35], a[244:247], a[84:87], v[32:35]\n"
"buffer_load_dwordx4 v15, s[24:27], s74 offen offset:1024 lds\n"
"v_mfma_f32_16x16x32_bf16 v[40:43], a[244:247], a[88:91], v[40:43]\n"
"s_waitcnt vmcnt(7)\n"
"v_mfma_f32_16x16x32_bf16 v[40:43], a[240:243], a[60:63], v[40:43]\n"
"ds_write_b128 v4, v[220:223] offset:37888\n"
"v_mfma_f32_16x16x32_bf16 v[48:51], a[240:243], a[64:67], v[48:51]\n"
"buffer_load_dwordx4 v21, s[24:27], s74 offen offset:2048 lds\n"
"v_mfma_f32_16x16x32_bf16 v[48:51], a[244:247], a[92:95], v[48:51]\n"
"v_mfma_f32_16x16x32_bf16 v[56:59], a[244:247], a[96:99], v[56:59]\n"
"s_waitcnt vmcnt(6)\n"
"v_mfma_f32_16x16x32_bf16 v[56:59], a[240:243], a[68:71], v[56:59]\n"
"ds_write_b128 v4, v[252:255] offset:38912\n"
"v_mfma_f32_16x16x32_bf16 v[64:67], a[240:243], a[72:75], v[64:67]\n"
"ds_read_b64_tr_b16 a[236:237], v12 offset:2048\n"
"v_mfma_f32_16x16x32_bf16 v[64:67], a[244:247], a[100:103], v[64:67]\n"
"ds_read_b64_tr_b16 a[238:239], v12 offset:3072\n"
"v_mfma_f32_16x16x32_bf16 v[72:75], a[244:247], a[104:107], v[72:75]\n"
"ds_read_b64_tr_b16 a[232:233], v12 offset:0\n"
"v_mfma_f32_16x16x32_bf16 v[72:75], a[240:243], a[76:79], v[72:75]\n"
"s_xor_b32 s75, s48, 327936\n"
"buffer_load_dwordx4 v23, s[24:27], s74 offen offset:3072 lds\n"
"v_mfma_f32_16x16x32_bf16 v[80:83], a[240:243], a[80:83], v[80:83]\n"
"ds_read_b64_tr_b16 a[234:235], v12 offset:1024\n"
"v_mfma_f32_16x16x32_bf16 v[80:83], a[244:247], a[108:111], v[80:83]\n"
"v_mfma_f32_16x16x32_bf16 v[84:87], a[252:255], a[108:111], v[84:87]\n"
"s_waitcnt lgkmcnt(4)\n"
"s_barrier\n"
"v_mfma_f32_16x16x32_bf16 v[84:87], a[248:251], a[80:83], v[84:87]\n"
"v_mfma_f32_16x16x32_bf16 v[76:79], a[248:251], a[76:79], v[76:79]\n"
"ds_read_b128 a[112:115], v1 offset:32768\n"
"v_mfma_f32_16x16x32_bf16 v[76:79], a[252:255], a[104:107], v[76:79]\n"
"s_add_u32 m0, s46, 0\n"
"ds_read_b128 a[140:143], v1 offset:49152\n"
"v_mfma_f32_16x16x32_bf16 v[68:71], a[252:255], a[100:103], v[68:71]\n"
"ds_read_b128 a[144:147], v1 offset:50176\n"
"v_mfma_f32_16x16x32_bf16 v[68:71], a[248:251], a[72:75], v[68:71]\n"
"ds_read_b128 a[116:119], v1 offset:33792\n"
"v_mfma_f32_16x16x32_bf16 v[60:63], a[248:251], a[68:71], v[60:63]\n"
"ds_read_b128 a[120:123], v1 offset:34816\n"
"v_mfma_f32_16x16x32_bf16 v[60:63], a[252:255], a[96:99], v[60:63]\n"
"ds_read_b128 a[148:151], v1 offset:51200\n"
"v_mfma_f32_16x16x32_bf16 v[52:55], a[252:255], a[92:95], v[52:55]\n"
"ds_read_b128 a[152:155], v1 offset:52224\n"
"v_mfma_f32_16x16x32_bf16 v[52:55], a[248:251], a[64:67], v[52:55]\n"
"ds_read_b128 a[124:127], v1 offset:35840\n"
"v_mfma_f32_16x16x32_bf16 v[44:47], a[248:251], a[60:63], v[44:47]\n"
"ds_read_b128 a[128:131], v1 offset:36864\n"
"v_mfma_f32_16x16x32_bf16 v[44:47], a[252:255], a[88:91], v[44:47]\n"
"ds_read_b128 a[156:159], v1 offset:53248\n"
"v_mfma_f32_16x16x32_bf16 v[36:39], a[252:255], a[84:87], v[36:39]\n"
"s_waitcnt lgkmcnt(9)\n"
"ds_read_b128 a[160:163], v1 offset:54272\n"
"v_mfma_f32_16x16x32_bf16 v[36:39], a[248:251], a[56:59], v[36:39]\n"
"ds_read_b128 a[132:135], v1 offset:37888\n"
"v_mfma_f32_16x16x32_bf16 v[32:35], a[224:227], a[112:115], v[32:35]\n"
"s_waitcnt lgkmcnt(9)\n"
"ds_read_b128 a[136:139], v1 offset:38912\n"
"v_mfma_f32_16x16x32_bf16 v[32:35], a[228:231], a[140:143], v[32:35]\n"
"ds_read_b128 a[164:167], v1 offset:55296\n"
"v_mfma_f32_16x16x32_bf16 v[40:43], a[228:231], a[144:147], v[40:43]\n"
"s_waitcnt lgkmcnt(9)\n"
"ds_read_b128 a[168:171], v1 offset:40960\n"
"v_mfma_f32_16x16x32_bf16 v[40:43], a[224:227], a[116:119], v[40:43]\n"
"buffer_load_dwordx4 v14, s[24:27], s75 offen lds\n"
"v_mfma_f32_16x16x32_bf16 v[48:51], a[224:227], a[120:123], v[48:51]\n"
"s_waitcnt lgkmcnt(8)\n"
"ds_read_b64_tr_b16 a[240:241], v11 offset:4096\n"
"v_mfma_f32_16x16x32_bf16 v[48:51], a[228:231], a[148:151], v[48:51]\n"
"ds_read_b64_tr_b16 a[242:243], v11 offset:5120\n"
"v_mfma_f32_16x16x32_bf16 v[56:59], a[228:231], a[152:155], v[56:59]\n"
"s_waitcnt lgkmcnt(8)\n"
"ds_read_b128 a[196:199], v1 offset:57344\n"
"v_mfma_f32_16x16x32_bf16 v[56:59], a[224:227], a[124:127], v[56:59]\n"
"buffer_load_dwordx4 v15, s[24:27], s75 offen offset:1024 lds\n"
"v_mfma_f32_16x16x32_bf16 v[64:67], a[224:227], a[128:131], v[64:67]\n"
"s_waitcnt lgkmcnt(7)\n"
"ds_read_b64_tr_b16 a[244:245], v11 offset:6144\n"
"v_mfma_f32_16x16x32_bf16 v[64:67], a[228:231], a[156:159], v[64:67]\n"
"ds_read_b64_tr_b16 a[246:247], v11 offset:7168\n"
"v_mfma_f32_16x16x32_bf16 v[72:75], a[228:231], a[160:163], v[72:75]\n"
"s_waitcnt lgkmcnt(7)\n"
"ds_read_b128 a[200:203], v1 offset:58368\n"
"v_mfma_f32_16x16x32_bf16 v[72:75], a[224:227], a[132:135], v[72:75]\n"
"buffer_load_dwordx4 v21, s[24:27], s75 offen offset:2048 lds\n"
"v_mfma_f32_16x16x32_bf16 v[80:83], a[224:227], a[136:139], v[80:83]\n"
"s_waitcnt lgkmcnt(7)\n"
"ds_read_b128 a[172:175], v1 offset:41984\n"
"v_mfma_f32_16x16x32_bf16 v[80:83], a[228:231], a[164:167], v[80:83]\n"
"ds_read_b128 a[176:179], v1 offset:43008\n"
"v_mfma_f32_16x16x32_bf16 v[84:87], a[236:239], a[164:167], v[84:87]\n"
"ds_read_b128 a[204:207], v1 offset:59392\n"
"v_mfma_f32_16x16x32_bf16 v[84:87], a[232:235], a[136:139], v[84:87]\n"
"buffer_load_dwordx4 v23, s[24:27], s75 offen offset:3072 lds\n"
"v_mfma_f32_16x16x32_bf16 v[76:79], a[232:235], a[132:135], v[76:79]\n"
"ds_read_b128 a[208:211], v1 offset:60416\n"
"v_mfma_f32_16x16x32_bf16 v[76:79], a[236:239], a[160:163], v[76:79]\n"
"ds_read_b128 a[180:183], v1 offset:44032\n"
"v_mfma_f32_16x16x32_bf16 v[68:71], a[236:239], a[156:159], v[68:71]\n"
"ds_read_b128 a[184:187], v1 offset:45056\n"
"v_mfma_f32_16x16x32_bf16 v[68:71], a[232:235], a[128:131], v[68:71]\n"
"v_mfma_f32_16x16x32_bf16 v[60:63], a[232:235], a[124:127], v[60:63]\n"
"ds_read_b128 a[212:215], v1 offset:61440\n"
"v_mfma_f32_16x16x32_bf16 v[60:63], a[236:239], a[152:155], v[60:63]\n"
"ds_read_b128 a[216:219], v1 offset:62464\n"
"v_mfma_f32_16x16x32_bf16 v[52:55], a[236:239], a[148:151], v[52:55]\n"
"ds_read_b128 a[188:191], v1 offset:46080\n"
"v_mfma_f32_16x16x32_bf16 v[52:55], a[232:235], a[120:123], v[52:55]\n"
"s_xor_b32 s74, s48, 360704\n"
"v_mfma_f32_16x16x32_bf16 v[44:47], a[232:235], a[116:119], v[44:47]\n"
"ds_read_b128 a[192:195], v1 offset:47104\n"
"v_mfma_f32_16x16x32_bf16 v[44:47], a[236:239], a[144:147], v[44:47]\n"
"ds_read_b128 a[220:223], v1 offset:63488\n"
"v_mfma_f32_16x16x32_bf16 v[36:39], a[236:239], a[140:143], v[36:39]\n"
"s_waitcnt lgkmcnt(15)\n"
"ds_read_b64_tr_b16 a[252:253], v12 offset:6144\n"
"v_mfma_f32_16x16x32_bf16 v[36:39], a[232:235], a[112:115], v[36:39]\n"
"v_mfma_f32_16x16x32_bf16 v[32:35], a[240:243], a[168:171], v[32:35]\n"
"s_waitcnt lgkmcnt(12)\n"
"ds_read_b64_tr_b16 a[254:255], v12 offset:7168\n"
"v_mfma_f32_16x16x32_bf16 v[32:35], a[244:247], a[196:199], v[32:35]\n"
"s_add_u32 m0, s46, 32768\n"
"ds_read_b64_tr_b16 a[248:249], v12 offset:4096\n"
"v_mfma_f32_16x16x32_bf16 v[40:43], a[244:247], a[200:203], v[40:43]\n"
"s_waitcnt lgkmcnt(3)\n"
"s_barrier\n"
"ds_read_b64_tr_b16 a[250:251], v12 offset:5120\n"
"v_mfma_f32_16x16x32_bf16 v[40:43], a[240:243], a[172:175], v[40:43]\n"
"buffer_load_dwordx4 v14, s[24:27], s74 offen lds\n"
"v_mfma_f32_16x16x32_bf16 v[48:51], a[240:243], a[176:179], v[48:51]\n"
"s_waitcnt vmcnt(11)\n"
"ds_read_b64_tr_b16 a[224:225], v2 offset:0\n"
"v_mfma_f32_16x16x32_bf16 v[48:51], a[244:247], a[204:207], v[48:51]\n"
"ds_read_b64_tr_b16 a[226:227], v2 offset:1024\n"
"v_mfma_f32_16x16x32_bf16 v[56:59], a[244:247], a[208:211], v[56:59]\n"
"s_waitcnt vmcnt(9)\n"
"ds_read_b64_tr_b16 a[228:229], v2 offset:2048\n"
"v_mfma_f32_16x16x32_bf16 v[56:59], a[240:243], a[180:183], v[56:59]\n"
"buffer_load_dwordx4 v15, s[24:27], s74 offen offset:1024 lds\n"
"v_mfma_f32_16x16x32_bf16 v[64:67], a[240:243], a[184:187], v[64:67]\n"
"ds_read_b64_tr_b16 a[230:231], v2 offset:3072\n"
"v_mfma_f32_16x16x32_bf16 v[64:67], a[244:247], a[212:215], v[64:67]\n"
"s_xor_b32 s75, s48, 328192\n"
"ds_read_b64_tr_b16 a[236:237], v3 offset:2048\n"
"v_mfma_f32_16x16x32_bf16 v[72:75], a[244:247], a[216:219], v[72:75]\n"
"ds_read_b64_tr_b16 a[238:239], v3 offset:3072\n"
"v_mfma_f32_16x16x32_bf16 v[72:75], a[240:243], a[188:191], v[72:75]\n"
"buffer_load_dwordx4 v21, s[24:27], s74 offen offset:2048 lds\n"
"v_mfma_f32_16x16x32_bf16 v[80:83], a[240:243], a[192:195], v[80:83]\n"
"s_waitcnt lgkmcnt(8)\n"
"ds_read_b64_tr_b16 a[232:233], v3 offset:0\n"
"v_mfma_f32_16x16x32_bf16 v[80:83], a[244:247], a[220:223], v[80:83]\n"
"ds_read_b64_tr_b16 a[234:235], v3 offset:1024\n"
"v_mfma_f32_16x16x32_bf16 v[84:87], a[252:255], a[220:223], v[84:87]\n"
"s_waitcnt lgkmcnt(8)\n"
"buffer_load_dwordx4 v23, s[24:27], s74 offen offset:3072 lds\n"
"v_mfma_f32_16x16x32_bf16 v[84:87], a[248:251], a[192:195], v[84:87]\n"
"s_add_u32 m0, s46, 36864\n"
"v_mfma_f32_16x16x32_bf16 v[76:79], a[248:251], a[188:191], v[76:79]\n"
"v_mfma_f32_16x16x32_bf16 v[76:79], a[252:255], a[216:219], v[76:79]\n"
"s_xor_b32 s38, s52, 786432\n"
"buffer_load_dwordx4 v14, s[24:27], s75 offen lds\n"
"v_mfma_f32_16x16x32_bf16 v[68:71], a[252:255], a[212:215], v[68:71]\n"
"v_mfma_f32_16x16x32_bf16 v[68:71], a[248:251], a[184:187], v[68:71]\n"
"v_cvt_pk_bf16_f32 v80, v80, v81\n"
"v_cvt_pk_bf16_f32 v81, v82, v83\n"
"v_mfma_f32_16x16x32_bf16 v[60:63], a[248:251], a[180:183], v[60:63]\n"
"buffer_load_dwordx4 v15, s[24:27], s75 offen offset:1024 lds\n"
"v_cvt_pk_bf16_f32 v82, v84, v85\n"
"v_mfma_f32_16x16x32_bf16 v[60:63], a[252:255], a[208:211], v[60:63]\n"
"v_cvt_pk_bf16_f32 v83, v86, v87\n"
"v_cvt_pk_bf16_f32 v72, v72, v73\n"
"v_mfma_f32_16x16x32_bf16 v[52:55], a[252:255], a[204:207], v[52:55]\n"
"s_waitcnt vmcnt(12)\n"
"ds_read_b64_tr_b16 a[240:241], v2 offset:4096\n"
"v_cvt_pk_bf16_f32 v73, v74, v75\n"
"v_mfma_f32_16x16x32_bf16 v[52:55], a[248:251], a[176:179], v[52:55]\n"
"s_xor_b32 s74, s48, 360960\n"
"buffer_load_dwordx4 v21, s[24:27], s75 offen offset:2048 lds\n"
"v_cvt_pk_bf16_f32 v74, v76, v77\n"
"v_mfma_f32_16x16x32_bf16 v[44:47], a[248:251], a[172:175], v[44:47]\n"
"ds_read_b64_tr_b16 a[242:243], v2 offset:5120\n"
"v_cvt_pk_bf16_f32 v75, v78, v79\n"
"v_mfma_f32_16x16x32_bf16 v[44:47], a[252:255], a[200:203], v[44:47]\n"
"buffer_store_dwordx4 v[80:83], v1, s[20:23], s38 offen offset:3072 sc1\n"
"v_mfma_f32_16x16x32_bf16 v[36:39], a[252:255], a[196:199], v[36:39]\n"
"s_waitcnt vmcnt(12) lgkmcnt(8)\n"
"ds_read_b64_tr_b16 a[244:245], v2 offset:6144\n"
"v_cvt_pk_bf16_f32 v64, v64, v65\n"
"v_mfma_f32_16x16x32_bf16 v[36:39], a[248:251], a[168:171], v[36:39]\n"
"buffer_load_dwordx4 v23, s[24:27], s75 offen offset:3072 lds\n"
"v_cvt_pk_bf16_f32 v65, v66, v67\n"
"v_mfma_f32_16x16x32_bf16 v[88:91], a[224:227], a[0:3], v[88:91]\n"
"s_waitcnt lgkmcnt(7)\n"
"ds_read_b64_tr_b16 a[246:247], v2 offset:7168\n"
"v_cvt_pk_bf16_f32 v66, v68, v69\n"
"v_mfma_f32_16x16x32_bf16 v[88:91], a[228:231], a[28:31], v[88:91]\n"
"s_add_u32 m0, s46, 4096\n"
"v_cvt_pk_bf16_f32 v67, v70, v71\n"
"v_cvt_pk_bf16_f32 v56, v56, v57\n"
"v_mfma_f32_16x16x32_bf16 v[96:99], a[228:231], a[32:35], v[96:99]\n"
"buffer_load_dwordx4 v14, s[24:27], s74 offen lds\n"
"v_cvt_pk_bf16_f32 v57, v58, v59\n"
"v_mfma_f32_16x16x32_bf16 v[96:99], a[224:227], a[4:7], v[96:99]\n"
"v_cvt_pk_bf16_f32 v58, v60, v61\n"
"v_cvt_pk_bf16_f32 v59, v62, v63\n"
"v_mfma_f32_16x16x32_bf16 v[104:107], a[224:227], a[8:11], v[104:107]\n"
"v_cvt_pk_bf16_f32 v48, v48, v49\n"
"v_cvt_pk_bf16_f32 v49, v50, v51\n"
"v_mfma_f32_16x16x32_bf16 v[104:107], a[228:231], a[36:39], v[104:107]\n"
"buffer_load_dwordx4 v15, s[24:27], s74 offen offset:1024 lds\n"
"v_cvt_pk_bf16_f32 v50, v52, v53\n"
"v_mfma_f32_16x16x32_bf16 v[112:115], a[228:231], a[40:43], v[112:115]\n"
"v_cvt_pk_bf16_f32 v51, v54, v55\n"
"v_cvt_pk_bf16_f32 v40, v40, v41\n"
"v_mfma_f32_16x16x32_bf16 v[112:115], a[224:227], a[12:15], v[112:115]\n"
"v_cvt_pk_bf16_f32 v41, v42, v43\n"
"v_cvt_pk_bf16_f32 v42, v44, v45\n"
"v_mfma_f32_16x16x32_bf16 v[120:123], a[224:227], a[16:19], v[120:123]\n"
"ds_read_b64_tr_b16 a[252:253], v3 offset:6144\n"
"v_cvt_pk_bf16_f32 v43, v46, v47\n"
"v_mfma_f32_16x16x32_bf16 v[120:123], a[228:231], a[44:47], v[120:123]\n"
"s_xor_b32 s75, s48, 328448\n"
"ds_read_b64_tr_b16 a[254:255], v3 offset:7168\n"
"v_cvt_pk_bf16_f32 v32, v32, v33\n"
"v_mfma_f32_16x16x32_bf16 v[128:131], a[228:231], a[48:51], v[128:131]\n"
"ds_read_b64_tr_b16 a[248:249], v3 offset:4096\n"
"v_cvt_pk_bf16_f32 v33, v34, v35\n"
"v_mfma_f32_16x16x32_bf16 v[128:131], a[224:227], a[20:23], v[128:131]\n"
"buffer_load_dwordx4 v21, s[24:27], s74 offen offset:2048 lds\n"
"v_cvt_pk_bf16_f32 v34, v36, v37\n"
"v_mfma_f32_16x16x32_bf16 v[136:139], a[224:227], a[24:27], v[136:139]\n"
"s_waitcnt lgkmcnt(9)\n"
"ds_read_b64_tr_b16 a[250:251], v3 offset:5120\n"
"v_cvt_pk_bf16_f32 v35, v38, v39\n"
"v_mfma_f32_16x16x32_bf16 v[136:139], a[228:231], a[52:55], v[136:139]\n"
"buffer_store_dwordx4 v[72:75], v1, s[20:23], s38 offen offset:2048 sc1\n"
"v_mfma_f32_16x16x32_bf16 v[140:143], a[236:239], a[52:55], v[140:143]\n"
"s_waitcnt lgkmcnt(8)\n"
"buffer_load_dwordx4 v23, s[24:27], s74 offen offset:3072 lds\n"
"v_mfma_f32_16x16x32_bf16 v[140:143], a[232:235], a[24:27], v[140:143]\n"
"s_add_u32 m0, s46, 131072\n"
"v_mfma_f32_16x16x32_bf16 v[132:135], a[232:235], a[20:23], v[132:135]\n"
"v_mfma_f32_16x16x32_bf16 v[132:135], a[236:239], a[48:51], v[132:135]\n"
"buffer_load_dwordx4 v14, s[24:27], s75 offen lds\n"
"v_mfma_f32_16x16x32_bf16 v[124:127], a[236:239], a[44:47], v[124:127]\n"
"v_mfma_f32_16x16x32_bf16 v[124:127], a[232:235], a[16:19], v[124:127]\n"
"v_mfma_f32_16x16x32_bf16 v[116:119], a[232:235], a[12:15], v[116:119]\n"
"buffer_load_dwordx4 v15, s[24:27], s75 offen offset:1024 lds\n"
"v_mfma_f32_16x16x32_bf16 v[116:119], a[236:239], a[40:43], v[116:119]\n"
"v_mfma_f32_16x16x32_bf16 v[108:111], a[236:239], a[36:39], v[108:111]\n"
"s_waitcnt vmcnt(18)\n"
"ds_read_b64_tr_b16 a[224:225], v11 offset:0\n"
"v_mfma_f32_16x16x32_bf16 v[108:111], a[232:235], a[8:11], v[108:111]\n"
"s_xor_b32 s74, s48, 361216\n"
"buffer_load_dwordx4 v21, s[24:27], s75 offen offset:2048 lds\n"
"v_mfma_f32_16x16x32_bf16 v[100:103], a[232:235], a[4:7], v[100:103]\n"
"ds_read_b64_tr_b16 a[226:227], v11 offset:1024\n"
"v_mfma_f32_16x16x32_bf16 v[100:103], a[236:239], a[32:35], v[100:103]\n"
"v_mfma_f32_16x16x32_bf16 v[92:95], a[236:239], a[28:31], v[92:95]\n"
"s_waitcnt vmcnt(17) lgkmcnt(8)\n"
"ds_read_b64_tr_b16 a[228:229], v11 offset:2048\n"
"v_mfma_f32_16x16x32_bf16 v[92:95], a[232:235], a[0:3], v[92:95]\n"
"buffer_load_dwordx4 v23, s[24:27], s75 offen offset:3072 lds\n"
"v_mfma_f32_16x16x32_bf16 v[88:91], a[240:243], a[56:59], v[88:91]\n"
"s_waitcnt lgkmcnt(7)\n"
"ds_read_b64_tr_b16 a[230:231], v11 offset:3072\n"
"v_mfma_f32_16x16x32_bf16 v[88:91], a[244:247], a[84:87], v[88:91]\n"
"s_add_u32 m0, s46, 135168\n"
"buffer_store_dwordx4 v[64:67], v1, s[20:23], s38 offen offset:1024 sc1\n"
"v_mfma_f32_16x16x32_bf16 v[96:99], a[244:247], a[88:91], v[96:99]\n"
"s_waitcnt lgkmcnt(4)\n"
"buffer_load_dwordx4 v14, s[24:27], s74 offen lds\n"
"v_mfma_f32_16x16x32_bf16 v[96:99], a[240:243], a[60:63], v[96:99]\n"
"s_xor_b32 s38, s53, 786432\n"
"v_mfma_f32_16x16x32_bf16 v[104:107], a[240:243], a[64:67], v[104:107]\n"
"v_mfma_f32_16x16x32_bf16 v[104:107], a[244:247], a[92:95], v[104:107]\n"
"buffer_load_dwordx4 v15, s[24:27], s74 offen offset:1024 lds\n"
"v_mfma_f32_16x16x32_bf16 v[112:115], a[244:247], a[96:99], v[112:115]\n"
"v_mfma_f32_16x16x32_bf16 v[112:115], a[240:243], a[68:71], v[112:115]\n"
"v_mfma_f32_16x16x32_bf16 v[120:123], a[240:243], a[72:75], v[120:123]\n"
"ds_read_b64_tr_b16 a[236:237], v12 offset:2048\n"
"v_mfma_f32_16x16x32_bf16 v[120:123], a[244:247], a[100:103], v[120:123]\n"
"ds_read_b64_tr_b16 a[238:239], v12 offset:3072\n"
"v_mfma_f32_16x16x32_bf16 v[128:131], a[244:247], a[104:107], v[128:131]\n"
"ds_read_b64_tr_b16 a[232:233], v12 offset:0\n"
"v_mfma_f32_16x16x32_bf16 v[128:131], a[240:243], a[76:79], v[128:131]\n"
"s_xor_b32 s75, s48, 262656\n"
"buffer_load_dwordx4 v21, s[24:27], s74 offen offset:2048 lds\n"
"v_mfma_f32_16x16x32_bf16 v[136:139], a[240:243], a[80:83], v[136:139]\n"
"ds_read_b64_tr_b16 a[234:235], v12 offset:1024\n"
"v_mfma_f32_16x16x32_bf16 v[136:139], a[244:247], a[108:111], v[136:139]\n"
"v_mfma_f32_16x16x32_bf16 v[140:143], a[252:255], a[108:111], v[140:143]\n"
"buffer_load_dwordx4 v23, s[24:27], s74 offen offset:3072 lds\n"
"v_mfma_f32_16x16x32_bf16 v[140:143], a[248:251], a[80:83], v[140:143]\n"
"buffer_store_dwordx4 v[56:59], v1, s[20:23], s38 offen sc1\n"
"v_mfma_f32_16x16x32_bf16 v[132:135], a[248:251], a[76:79], v[132:135]\n"
"v_mfma_f32_16x16x32_bf16 v[132:135], a[252:255], a[104:107], v[132:135]\n"
"s_add_u32 m0, s46, 0\n"
"v_mfma_f32_16x16x32_bf16 v[124:127], a[252:255], a[100:103], v[124:127]\n"
"s_waitcnt lgkmcnt(0)\n"
"v_mfma_f32_16x16x32_bf16 v[124:127], a[248:251], a[72:75], v[124:127]\n"
"s_xor_b32 s38, s52, 917504\n"
"buffer_load_dwordx4 v14, s[24:27], s75 offen lds\n"
"v_mfma_f32_16x16x32_bf16 v[116:119], a[248:251], a[68:71], v[116:119]\n"
"v_mfma_f32_16x16x32_bf16 v[116:119], a[252:255], a[96:99], v[116:119]\n"
"v_mfma_f32_16x16x32_bf16 v[108:111], a[252:255], a[92:95], v[108:111]\n"
"s_waitcnt vmcnt(23)\n"
"ds_read_b64_tr_b16 a[240:241], v11 offset:32768\n"
"v_mfma_f32_16x16x32_bf16 v[108:111], a[248:251], a[64:67], v[108:111]\n"
"buffer_load_dwordx4 v15, s[24:27], s75 offen offset:1024 lds\n"
"v_mfma_f32_16x16x32_bf16 v[100:103], a[248:251], a[60:63], v[100:103]\n"
"ds_read_b64_tr_b16 a[242:243], v11 offset:33792\n"
"v_mfma_f32_16x16x32_bf16 v[100:103], a[252:255], a[88:91], v[100:103]\n"
"v_mfma_f32_16x16x32_bf16 v[92:95], a[252:255], a[84:87], v[92:95]\n"
"s_waitcnt vmcnt(22)\n"
"ds_read_b64_tr_b16 a[244:245], v11 offset:34816\n"
"v_mfma_f32_16x16x32_bf16 v[92:95], a[248:251], a[56:59], v[92:95]\n"
"buffer_load_dwordx4 v21, s[24:27], s75 offen offset:2048 lds\n"
"v_mfma_f32_16x16x32_bf16 v[88:91], a[224:227], a[112:115], v[88:91]\n"
"ds_read_b64_tr_b16 a[246:247], v11 offset:35840\n"
"v_mfma_f32_16x16x32_bf16 v[88:91], a[228:231], a[140:143], v[88:91]\n"
"buffer_store_dwordx4 v[48:51], v1, s[20:23], s38 offen offset:1024 sc1\n"
"v_mfma_f32_16x16x32_bf16 v[96:99], a[228:231], a[144:147], v[96:99]\n"
"buffer_load_dwordx4 v23, s[24:27], s75 offen offset:3072 lds\n"
"v_mfma_f32_16x16x32_bf16 v[96:99], a[224:227], a[116:119], v[96:99]\n"
"v_mfma_f32_16x16x32_bf16 v[104:107], a[224:227], a[120:123], v[104:107]\n"
"v_mfma_f32_16x16x32_bf16 v[104:107], a[228:231], a[148:151], v[104:107]\n"
"v_mfma_f32_16x16x32_bf16 v[112:115], a[228:231], a[152:155], v[112:115]\n"
"v_mfma_f32_16x16x32_bf16 v[112:115], a[224:227], a[124:127], v[112:115]\n"
"v_mfma_f32_16x16x32_bf16 v[120:123], a[224:227], a[128:131], v[120:123]\n"
"ds_read_b64_tr_b16 a[252:253], v12 offset:34816\n"
"v_mfma_f32_16x16x32_bf16 v[120:123], a[228:231], a[156:159], v[120:123]\n"
"ds_read_b64_tr_b16 a[254:255], v12 offset:35840\n"
"v_mfma_f32_16x16x32_bf16 v[128:131], a[228:231], a[160:163], v[128:131]\n"
"ds_read_b64_tr_b16 a[248:249], v12 offset:32768\n"
"v_mfma_f32_16x16x32_bf16 v[128:131], a[224:227], a[132:135], v[128:131]\n"
"s_xor_b32 s74, s48, 295424\n"
"v_mfma_f32_16x16x32_bf16 v[136:139], a[224:227], a[136:139], v[136:139]\n"
"ds_read_b64_tr_b16 a[250:251], v12 offset:33792\n"
"v_mfma_f32_16x16x32_bf16 v[136:139], a[228:231], a[164:167], v[136:139]\n"
"v_mfma_f32_16x16x32_bf16 v[140:143], a[236:239], a[164:167], v[140:143]\n"
"v_mfma_f32_16x16x32_bf16 v[140:143], a[232:235], a[136:139], v[140:143]\n"
"buffer_store_dwordx4 v[40:43], v1, s[20:23], s38 offen offset:2048 sc1\n"
"v_mfma_f32_16x16x32_bf16 v[132:135], a[232:235], a[132:135], v[132:135]\n"
"v_mfma_f32_16x16x32_bf16 v[132:135], a[236:239], a[160:163], v[132:135]\n"
"s_add_u32 m0, s46, 32768\n"
"v_mfma_f32_16x16x32_bf16 v[124:127], a[236:239], a[156:159], v[124:127]\n"
"s_waitcnt lgkmcnt(0)\n"
"v_mfma_f32_16x16x32_bf16 v[124:127], a[232:235], a[128:131], v[124:127]\n"
"buffer_load_dwordx4 v14, s[24:27], s74 offen lds\n"
"v_mfma_f32_16x16x32_bf16 v[116:119], a[232:235], a[124:127], v[116:119]\n"
"v_mfma_f32_16x16x32_bf16 v[116:119], a[236:239], a[152:155], v[116:119]\n"
"v_mfma_f32_16x16x32_bf16 v[108:111], a[236:239], a[148:151], v[108:111]\n"
"s_waitcnt vmcnt(25)\n"
"ds_read_b64_tr_b16 a[224:225], v11 offset:36864\n"
"v_mfma_f32_16x16x32_bf16 v[108:111], a[232:235], a[120:123], v[108:111]\n"
"buffer_load_dwordx4 v15, s[24:27], s74 offen offset:1024 lds\n"
"v_mfma_f32_16x16x32_bf16 v[100:103], a[232:235], a[116:119], v[100:103]\n"
"ds_read_b64_tr_b16 a[226:227], v11 offset:37888\n"
"v_mfma_f32_16x16x32_bf16 v[100:103], a[236:239], a[144:147], v[100:103]\n"
"v_mfma_f32_16x16x32_bf16 v[92:95], a[236:239], a[140:143], v[92:95]\n"
"s_waitcnt vmcnt(23)\n"
"ds_read_b64_tr_b16 a[228:229], v11 offset:38912\n"
"v_mfma_f32_16x16x32_bf16 v[92:95], a[232:235], a[112:115], v[92:95]\n"
"buffer_load_dwordx4 v21, s[24:27], s74 offen offset:2048 lds\n"
"v_mfma_f32_16x16x32_bf16 v[88:91], a[240:243], a[168:171], v[88:91]\n"
"ds_read_b64_tr_b16 a[230:231], v11 offset:39936\n"
"v_mfma_f32_16x16x32_bf16 v[88:91], a[244:247], a[196:199], v[88:91]\n"
"buffer_store_dwordx4 v[32:35], v1, s[20:23], s38 offen offset:3072 sc1\n"
"v_mfma_f32_16x16x32_bf16 v[96:99], a[244:247], a[200:203], v[96:99]\n"
"buffer_load_dwordx4 v23, s[24:27], s74 offen offset:3072 lds\n"
"v_mfma_f32_16x16x32_bf16 v[96:99], a[240:243], a[172:175], v[96:99]\n"
"v_mfma_f32_16x16x32_bf16 v[104:107], a[240:243], a[176:179], v[104:107]\n"
"v_mfma_f32_16x16x32_bf16 v[104:107], a[244:247], a[204:207], v[104:107]\n"
"v_mfma_f32_16x16x32_bf16 v[112:115], a[244:247], a[208:211], v[112:115]\n"
"v_mfma_f32_16x16x32_bf16 v[112:115], a[240:243], a[180:183], v[112:115]\n"
"v_mfma_f32_16x16x32_bf16 v[120:123], a[240:243], a[184:187], v[120:123]\n"
"ds_read_b64_tr_b16 a[236:237], v12 offset:38912\n"
"v_mfma_f32_16x16x32_bf16 v[120:123], a[244:247], a[212:215], v[120:123]\n"
"ds_read_b64_tr_b16 a[238:239], v12 offset:39936\n"
"v_mfma_f32_16x16x32_bf16 v[128:131], a[244:247], a[216:219], v[128:131]\n"
"ds_read_b64_tr_b16 a[232:233], v12 offset:36864\n"
"v_mfma_f32_16x16x32_bf16 v[128:131], a[240:243], a[188:191], v[128:131]\n"
"s_xor_b32 s75, s48, 66048\n"
"v_mfma_f32_16x16x32_bf16 v[136:139], a[240:243], a[192:195], v[136:139]\n"
"ds_read_b64_tr_b16 a[234:235], v12 offset:37888\n"
"v_mfma_f32_16x16x32_bf16 v[136:139], a[244:247], a[220:223], v[136:139]\n"
"v_mfma_f32_16x16x32_bf16 v[140:143], a[252:255], a[220:223], v[140:143]\n"
"v_mfma_f32_16x16x32_bf16 v[140:143], a[248:251], a[192:195], v[140:143]\n"
"s_xor_b32 s38, s52, 589824\n"
"v_mfma_f32_16x16x32_bf16 v[132:135], a[248:251], a[188:191], v[132:135]\n"
"v_mfma_f32_16x16x32_bf16 v[132:135], a[252:255], a[216:219], v[132:135]\n"
"s_add_u32 m0, s46, 36864\n"
"v_mfma_f32_16x16x32_bf16 v[124:127], a[252:255], a[212:215], v[124:127]\n"
"s_waitcnt lgkmcnt(0)\n"
"v_mfma_f32_16x16x32_bf16 v[124:127], a[248:251], a[184:187], v[124:127]\n"
"buffer_load_dwordx4 v14, s[24:27], s75 offen lds\n"
"v_cvt_pk_bf16_f32 v136, v136, v137\n"
"v_mfma_f32_16x16x32_bf16 v[116:119], a[248:251], a[180:183], v[116:119]\n"
"v_cvt_pk_bf16_f32 v137, v138, v139\n"
"v_cvt_pk_bf16_f32 v138, v140, v141\n"
"v_mfma_f32_16x16x32_bf16 v[116:119], a[252:255], a[208:211], v[116:119]\n"
"v_cvt_pk_bf16_f32 v139, v142, v143\n"
"v_cvt_pk_bf16_f32 v128, v128, v129\n"
"v_mfma_f32_16x16x32_bf16 v[108:111], a[252:255], a[204:207], v[108:111]\n"
"s_waitcnt vmcnt(25)\n"
"ds_read_b64_tr_b16 a[240:241], v11 offset:4096\n"
"v_cvt_pk_bf16_f32 v129, v130, v131\n"
"v_mfma_f32_16x16x32_bf16 v[108:111], a[248:251], a[176:179], v[108:111]\n"
"buffer_load_dwordx4 v15, s[24:27], s75 offen offset:1024 lds\n"
"v_cvt_pk_bf16_f32 v130, v132, v133\n"
"v_mfma_f32_16x16x32_bf16 v[100:103], a[248:251], a[172:175], v[100:103]\n"
"ds_read_b64_tr_b16 a[242:243], v11 offset:5120\n"
"v_cvt_pk_bf16_f32 v131, v134, v135\n"
"v_mfma_f32_16x16x32_bf16 v[100:103], a[252:255], a[200:203], v[100:103]\n"
"buffer_store_dwordx4 v[136:139], v1, s[20:23], s38 offen offset:3072 sc1\n"
"v_mfma_f32_16x16x32_bf16 v[92:95], a[252:255], a[196:199], v[92:95]\n"
"s_waitcnt vmcnt(24)\n"
"ds_read_b64_tr_b16 a[244:245], v11 offset:6144\n"
"v_cvt_pk_bf16_f32 v120, v120, v121\n"
"v_mfma_f32_16x16x32_bf16 v[92:95], a[248:251], a[168:171], v[92:95]\n"
"buffer_load_dwordx4 v21, s[24:27], s75 offen offset:2048 lds\n"
"v_cvt_pk_bf16_f32 v121, v122, v123\n"
"v_mfma_f32_16x16x32_bf16 v[144:147], a[224:227], a[112:115], v[144:147]\n"
"ds_read_b64_tr_b16 a[246:247], v11 offset:7168\n"
"v_cvt_pk_bf16_f32 v122, v124, v125\n"
"v_mfma_f32_16x16x32_bf16 v[144:147], a[228:231], a[140:143], v[144:147]\n"
"v_cvt_pk_bf16_f32 v123, v126, v127\n"
"v_cvt_pk_bf16_f32 v112, v112, v113\n"
"v_mfma_f32_16x16x32_bf16 v[152:155], a[228:231], a[144:147], v[152:155]\n"
"buffer_load_dwordx4 v23, s[24:27], s75 offen offset:3072 lds\n"
"v_cvt_pk_bf16_f32 v113, v114, v115\n"
"v_mfma_f32_16x16x32_bf16 v[152:155], a[224:227], a[116:119], v[152:155]\n"
"v_cvt_pk_bf16_f32 v114, v116, v117\n"
"v_cvt_pk_bf16_f32 v115, v118, v119\n"
"v_mfma_f32_16x16x32_bf16 v[160:163], a[224:227], a[120:123], v[160:163]\n"
"v_cvt_pk_bf16_f32 v104, v104, v105\n"
"v_cvt_pk_bf16_f32 v105, v106, v107\n"
"v_mfma_f32_16x16x32_bf16 v[160:163], a[228:231], a[148:151], v[160:163]\n"
"v_cvt_pk_bf16_f32 v106, v108, v109\n"
"v_cvt_pk_bf16_f32 v107, v110, v111\n"
"v_mfma_f32_16x16x32_bf16 v[168:171], a[228:231], a[152:155], v[168:171]\n"
"v_cvt_pk_bf16_f32 v96, v96, v97\n"
"v_cvt_pk_bf16_f32 v97, v98, v99\n"
"v_mfma_f32_16x16x32_bf16 v[168:171], a[224:227], a[124:127], v[168:171]\n"
"v_cvt_pk_bf16_f32 v98, v100, v101\n"
"v_cvt_pk_bf16_f32 v99, v102, v103\n"
"v_mfma_f32_16x16x32_bf16 v[176:179], a[224:227], a[128:131], v[176:179]\n"
"ds_read_b64_tr_b16 a[252:253], v12 offset:6144\n"
"v_cvt_pk_bf16_f32 v88, v88, v89\n"
"v_mfma_f32_16x16x32_bf16 v[176:179], a[228:231], a[156:159], v[176:179]\n"
"ds_read_b64_tr_b16 a[254:255], v12 offset:7168\n"
"v_cvt_pk_bf16_f32 v89, v90, v91\n"
"v_mfma_f32_16x16x32_bf16 v[184:187], a[228:231], a[160:163], v[184:187]\n"
"ds_read_b64_tr_b16 a[248:249], v12 offset:4096\n"
"v_cvt_pk_bf16_f32 v90, v92, v93\n"
"v_mfma_f32_16x16x32_bf16 v[184:187], a[224:227], a[132:135], v[184:187]\n"
"s_xor_b32 s74, s48, 98816\n"
"buffer_store_dwordx4 v[128:131], v1, s[20:23], s38 offen offset:2048 sc1\n"
"v_mfma_f32_16x16x32_bf16 v[192:195], a[224:227], a[136:139], v[192:195]\n"
"ds_read_b64_tr_b16 a[250:251], v12 offset:5120\n"
"v_cvt_pk_bf16_f32 v91, v94, v95\n"
"v_mfma_f32_16x16x32_bf16 v[192:195], a[228:231], a[164:167], v[192:195]\n"
"v_mfma_f32_16x16x32_bf16 v[196:199], a[236:239], a[164:167], v[196:199]\n"
"v_mfma_f32_16x16x32_bf16 v[196:199], a[232:235], a[136:139], v[196:199]\n"
"v_mfma_f32_16x16x32_bf16 v[188:191], a[232:235], a[132:135], v[188:191]\n"
"v_mfma_f32_16x16x32_bf16 v[188:191], a[236:239], a[160:163], v[188:191]\n"
"s_add_u32 m0, s46, 4096\n"
"v_mfma_f32_16x16x32_bf16 v[180:183], a[236:239], a[156:159], v[180:183]\n"
"s_waitcnt lgkmcnt(0)\n"
"v_mfma_f32_16x16x32_bf16 v[180:183], a[232:235], a[128:131], v[180:183]\n"
"buffer_load_dwordx4 v14, s[24:27], s74 offen lds\n"
"v_mfma_f32_16x16x32_bf16 v[172:175], a[232:235], a[124:127], v[172:175]\n"
"v_mfma_f32_16x16x32_bf16 v[172:175], a[236:239], a[152:155], v[172:175]\n"
"v_mfma_f32_16x16x32_bf16 v[164:167], a[236:239], a[148:151], v[164:167]\n"
"s_waitcnt vmcnt(26)\n"
"ds_read_b64_tr_b16 a[224:225], v2 offset:0\n"
"v_mfma_f32_16x16x32_bf16 v[164:167], a[232:235], a[120:123], v[164:167]\n"
"buffer_load_dwordx4 v15, s[24:27], s74 offen offset:1024 lds\n"
"v_mfma_f32_16x16x32_bf16 v[156:159], a[232:235], a[116:119], v[156:159]\n"
"ds_read_b64_tr_b16 a[226:227], v2 offset:1024\n"
"v_mfma_f32_16x16x32_bf16 v[156:159], a[236:239], a[144:147], v[156:159]\n"
"buffer_store_dwordx4 v[120:123], v1, s[20:23], s38 offen offset:1024 sc1\n"
"v_mfma_f32_16x16x32_bf16 v[148:151], a[236:239], a[140:143], v[148:151]\n"
"s_waitcnt vmcnt(10)\n"
"buffer_store_dwordx2 v[8:9], v19, s[20:23], s60 offen offset:384 sc1\n"
"v_mfma_f32_16x16x32_bf16 v[148:151], a[232:235], a[112:115], v[148:151]\n"
"s_xor_b32 s38, s53, 589824\n"
"buffer_load_dwordx4 v21, s[24:27], s74 offen offset:2048 lds\n"
"v_mfma_f32_16x16x32_bf16 v[144:147], a[240:243], a[168:171], v[144:147]\n"
"ds_read_b64_tr_b16 a[228:229], v2 offset:2048\n"
"v_mfma_f32_16x16x32_bf16 v[144:147], a[244:247], a[196:199], v[144:147]\n"
"ds_read_b64_tr_b16 a[230:231], v2 offset:3072\n"
"v_mfma_f32_16x16x32_bf16 v[152:155], a[244:247], a[200:203], v[152:155]\n"
"buffer_load_dwordx4 v23, s[24:27], s74 offen offset:3072 lds\n"
"v_mfma_f32_16x16x32_bf16 v[152:155], a[240:243], a[172:175], v[152:155]\n"
"v_mfma_f32_16x16x32_bf16 v[160:163], a[240:243], a[176:179], v[160:163]\n"
"v_mfma_f32_16x16x32_bf16 v[160:163], a[244:247], a[204:207], v[160:163]\n"
"v_mfma_f32_16x16x32_bf16 v[168:171], a[244:247], a[208:211], v[168:171]\n"
"v_mfma_f32_16x16x32_bf16 v[168:171], a[240:243], a[180:183], v[168:171]\n"
"v_mfma_f32_16x16x32_bf16 v[176:179], a[240:243], a[184:187], v[176:179]\n"
"ds_read_b64_tr_b16 a[236:237], v3 offset:2048\n"
"v_mfma_f32_16x16x32_bf16 v[176:179], a[244:247], a[212:215], v[176:179]\n"
"ds_read_b64_tr_b16 a[238:239], v3 offset:3072\n"
"v_mfma_f32_16x16x32_bf16 v[184:187], a[244:247], a[216:219], v[184:187]\n"
"ds_read_b64_tr_b16 a[232:233], v3 offset:0\n"
"v_mfma_f32_16x16x32_bf16 v[184:187], a[240:243], a[188:191], v[184:187]\n"
"s_xor_b32 s75, s48, 262912\n"
"buffer_store_dwordx4 v[112:115], v1, s[20:23], s38 offen sc1\n"
"v_mfma_f32_16x16x32_bf16 v[192:195], a[240:243], a[192:195], v[192:195]\n"
"ds_read_b64_tr_b16 a[234:235], v3 offset:1024\n"
"v_mfma_f32_16x16x32_bf16 v[192:195], a[244:247], a[220:223], v[192:195]\n"
"s_xor_b32 s38, s52, 720896\n"
"v_mfma_f32_16x16x32_bf16 v[196:199], a[252:255], a[220:223], v[196:199]\n"
"v_mfma_f32_16x16x32_bf16 v[196:199], a[248:251], a[192:195], v[196:199]\n"
"v_mfma_f32_16x16x32_bf16 v[188:191], a[248:251], a[188:191], v[188:191]\n"
"v_mfma_f32_16x16x32_bf16 v[188:191], a[252:255], a[216:219], v[188:191]\n"
"s_add_u32 m0, s46, 131072\n"
"v_mfma_f32_16x16x32_bf16 v[180:183], a[252:255], a[212:215], v[180:183]\n"
"s_waitcnt lgkmcnt(0)\n"
"v_mfma_f32_16x16x32_bf16 v[180:183], a[248:251], a[184:187], v[180:183]\n"
"buffer_load_dwordx4 v14, s[24:27], s75 offen lds\n"
"v_mfma_f32_16x16x32_bf16 v[172:175], a[248:251], a[180:183], v[172:175]\n"
"v_mfma_f32_16x16x32_bf16 v[172:175], a[252:255], a[208:211], v[172:175]\n"
"v_mfma_f32_16x16x32_bf16 v[164:167], a[252:255], a[204:207], v[164:167]\n"
"ds_read_b64_tr_b16 a[240:241], v2 offset:4096\n"
"v_mfma_f32_16x16x32_bf16 v[164:167], a[248:251], a[176:179], v[164:167]\n"
"buffer_load_dwordx4 v15, s[24:27], s75 offen offset:1024 lds\n"
"v_mfma_f32_16x16x32_bf16 v[156:159], a[248:251], a[172:175], v[156:159]\n"
"ds_read_b64_tr_b16 a[242:243], v2 offset:5120\n"
"v_mfma_f32_16x16x32_bf16 v[156:159], a[252:255], a[200:203], v[156:159]\n"
"buffer_store_dwordx4 v[104:107], v1, s[20:23], s38 offen offset:1024 sc1\n"
"v_mfma_f32_16x16x32_bf16 v[148:151], a[252:255], a[196:199], v[148:151]\n"
"ds_read_b64_tr_b16 a[244:245], v2 offset:6144\n"
"v_mfma_f32_16x16x32_bf16 v[148:151], a[248:251], a[168:171], v[148:151]\n"
"buffer_load_dwordx4 v21, s[24:27], s75 offen offset:2048 lds\n"
"v_mfma_f32_16x16x32_bf16 v[200:203], a[224:227], a[112:115], 0\n"
"ds_read_b64_tr_b16 a[246:247], v2 offset:7168\n"
"v_mfma_f32_16x16x32_bf16 v[200:203], a[228:231], a[140:143], v[200:203]\n"
"v_mfma_f32_16x16x32_bf16 v[208:211], a[228:231], a[144:147], 0\n"
"buffer_load_dwordx4 v23, s[24:27], s75 offen offset:3072 lds\n"
"v_mfma_f32_16x16x32_bf16 v[208:211], a[224:227], a[116:119], v[208:211]\n"
"v_mfma_f32_16x16x32_bf16 v[216:219], a[224:227], a[120:123], 0\n"
"v_mfma_f32_16x16x32_bf16 v[216:219], a[228:231], a[148:151], v[216:219]\n"
"v_mfma_f32_16x16x32_bf16 v[224:227], a[228:231], a[152:155], 0\n"
"v_mfma_f32_16x16x32_bf16 v[224:227], a[224:227], a[124:127], v[224:227]\n"
"v_mfma_f32_16x16x32_bf16 v[232:235], a[224:227], a[128:131], 0\n"
"ds_read_b64_tr_b16 a[252:253], v3 offset:6144\n"
"v_mfma_f32_16x16x32_bf16 v[232:235], a[228:231], a[156:159], v[232:235]\n"
"ds_read_b64_tr_b16 a[254:255], v3 offset:7168\n"
"v_mfma_f32_16x16x32_bf16 v[240:243], a[228:231], a[160:163], 0\n"
"ds_read_b64_tr_b16 a[248:249], v3 offset:4096\n"
"v_mfma_f32_16x16x32_bf16 v[240:243], a[224:227], a[132:135], v[240:243]\n"
"s_xor_b32 s74, s48, 295680\n"
"buffer_store_dwordx4 v[96:99], v1, s[20:23], s38 offen offset:2048 sc1\n"
"v_mfma_f32_16x16x32_bf16 v[248:251], a[224:227], a[136:139], 0\n"
"ds_read_b64_tr_b16 a[250:251], v3 offset:5120\n"
"v_mfma_f32_16x16x32_bf16 v[248:251], a[228:231], a[164:167], v[248:251]\n"
"v_mfma_f32_16x16x32_bf16 v[252:255], a[236:239], a[164:167], 0\n"
"v_mfma_f32_16x16x32_bf16 v[252:255], a[232:235], a[136:139], v[252:255]\n"
"v_mfma_f32_16x16x32_bf16 v[244:247], a[232:235], a[132:135], 0\n"
"v_mfma_f32_16x16x32_bf16 v[244:247], a[236:239], a[160:163], v[244:247]\n"
"s_add_u32 m0, s46, 135168\n"
"v_mfma_f32_16x16x32_bf16 v[236:239], a[236:239], a[156:159], 0\n"
"s_waitcnt lgkmcnt(0)\n"
"v_mfma_f32_16x16x32_bf16 v[236:239], a[232:235], a[128:131], v[236:239]\n"
"buffer_load_dwordx4 v14, s[24:27], s74 offen lds\n"
"v_mfma_f32_16x16x32_bf16 v[228:231], a[232:235], a[124:127], 0\n"
"v_mfma_f32_16x16x32_bf16 v[228:231], a[236:239], a[152:155], v[228:231]\n"
"v_mfma_f32_16x16x32_bf16 v[220:223], a[236:239], a[148:151], 0\n"
"ds_read_b64_tr_b16 a[224:225], v11 offset:0\n"
"v_mfma_f32_16x16x32_bf16 v[220:223], a[232:235], a[120:123], v[220:223]\n"
"buffer_load_dwordx4 v15, s[24:27], s74 offen offset:1024 lds\n"
"v_mfma_f32_16x16x32_bf16 v[212:215], a[232:235], a[116:119], 0\n"
"ds_read_b64_tr_b16 a[226:227], v11 offset:1024\n"
"v_mfma_f32_16x16x32_bf16 v[212:215], a[236:239], a[144:147], v[212:215]\n"
"buffer_store_dwordx4 v[88:91], v1, s[20:23], s38 offen offset:3072 sc1\n"
"v_mfma_f32_16x16x32_bf16 v[204:207], a[236:239], a[140:143], 0\n"
"ds_read_b64_tr_b16 a[228:229], v11 offset:2048\n"
"v_mfma_f32_16x16x32_bf16 v[204:207], a[232:235], a[112:115], v[204:207]\n"
"buffer_load_dwordx4 v21, s[24:27], s74 offen offset:2048 lds\n"
"v_mfma_f32_16x16x32_bf16 v[200:203], a[240:243], a[168:171], v[200:203]\n"
"ds_read_b64_tr_b16 a[230:231], v11 offset:3072\n"
"v_mfma_f32_16x16x32_bf16 v[200:203], a[244:247], a[196:199], v[200:203]\n"
"v_mfma_f32_16x16x32_bf16 v[208:211], a[244:247], a[200:203], v[208:211]\n"
"buffer_load_dwordx4 v23, s[24:27], s74 offen offset:3072 lds\n"
"v_mfma_f32_16x16x32_bf16 v[208:211], a[240:243], a[172:175], v[208:211]\n"
"s_xor_b32 s61, s59, 1572864\n"
"buffer_load_dwordx2 v[16:17], v5, s[20:23], s61 offen offset:384 sc1\n"
"v_mfma_f32_16x16x32_bf16 v[216:219], a[240:243], a[176:179], v[216:219]\n"
"v_mfma_f32_16x16x32_bf16 v[216:219], a[244:247], a[204:207], v[216:219]\n"
"v_mfma_f32_16x16x32_bf16 v[224:227], a[244:247], a[208:211], v[224:227]\n"
"v_mfma_f32_16x16x32_bf16 v[224:227], a[240:243], a[180:183], v[224:227]\n"
"v_mfma_f32_16x16x32_bf16 v[232:235], a[240:243], a[184:187], v[232:235]\n"
"ds_read_b64_tr_b16 a[236:237], v12 offset:2048\n"
"v_mfma_f32_16x16x32_bf16 v[232:235], a[244:247], a[212:215], v[232:235]\n"
"ds_read_b64_tr_b16 a[238:239], v12 offset:3072\n"
"v_mfma_f32_16x16x32_bf16 v[240:243], a[244:247], a[216:219], v[240:243]\n"
"ds_read_b64_tr_b16 a[232:233], v12 offset:0\n"
"v_mfma_f32_16x16x32_bf16 v[240:243], a[240:243], a[188:191], v[240:243]\n"
"s_xor_b32 s75, s48, 66304\n"
"v_mfma_f32_16x16x32_bf16 v[248:251], a[240:243], a[192:195], v[248:251]\n"
"ds_read_b64_tr_b16 a[234:235], v12 offset:1024\n"
"v_mfma_f32_16x16x32_bf16 v[248:251], a[244:247], a[220:223], v[248:251]\n"
"v_mfma_f32_16x16x32_bf16 v[252:255], a[252:255], a[220:223], v[252:255]\n"
"v_mfma_f32_16x16x32_bf16 v[252:255], a[248:251], a[192:195], v[252:255]\n"
"v_mfma_f32_16x16x32_bf16 v[244:247], a[248:251], a[188:191], v[244:247]\n"
"v_mfma_f32_16x16x32_bf16 v[244:247], a[252:255], a[216:219], v[244:247]\n"
"s_add_u32 m0, s46, 0\n"
"v_mfma_f32_16x16x32_bf16 v[236:239], a[252:255], a[212:215], v[236:239]\n"
"s_waitcnt lgkmcnt(0)\n"
"v_mfma_f32_16x16x32_bf16 v[236:239], a[248:251], a[184:187], v[236:239]\n"
"buffer_load_dwordx4 v14, s[24:27], s75 offen lds\n"
"v_mfma_f32_16x16x32_bf16 v[228:231], a[248:251], a[180:183], v[228:231]\n"
"v_mfma_f32_16x16x32_bf16 v[228:231], a[252:255], a[208:211], v[228:231]\n"
"v_mfma_f32_16x16x32_bf16 v[220:223], a[252:255], a[204:207], v[220:223]\n"
"ds_read_b64_tr_b16 a[240:241], v11 offset:32768\n"
"v_mfma_f32_16x16x32_bf16 v[220:223], a[248:251], a[176:179], v[220:223]\n"
"buffer_load_dwordx4 v15, s[24:27], s75 offen offset:1024 lds\n"
"v_mfma_f32_16x16x32_bf16 v[212:215], a[248:251], a[172:175], v[212:215]\n"
"ds_read_b64_tr_b16 a[242:243], v11 offset:33792\n"
"v_mfma_f32_16x16x32_bf16 v[212:215], a[252:255], a[200:203], v[212:215]\n"
"ds_read_b128 a[112:115], v6 offset:32768\n"
"v_mfma_f32_16x16x32_bf16 v[204:207], a[252:255], a[196:199], v[204:207]\n"
"s_waitcnt vmcnt(27)\n"
"ds_read_b64_tr_b16 a[244:245], v11 offset:34816\n"
"v_mfma_f32_16x16x32_bf16 v[204:207], a[248:251], a[168:171], v[204:207]\n"
"buffer_load_dwordx4 v21, s[24:27], s75 offen offset:2048 lds\n"
"v_mfma_f32_16x16x32_bf16 v[144:147], a[224:227], a[0:3], v[144:147]\n"
"ds_read_b64_tr_b16 a[246:247], v11 offset:35840\n"
"v_mfma_f32_16x16x32_bf16 v[144:147], a[228:231], a[28:31], v[144:147]\n"
"ds_read_b128 a[140:143], v6 offset:34816\n"
"v_mfma_f32_16x16x32_bf16 v[152:155], a[228:231], a[32:35], v[152:155]\n"
"ds_read_b128 a[144:147], v6 offset:38912\n"
"v_mfma_f32_16x16x32_bf16 v[152:155], a[224:227], a[4:7], v[152:155]\n"
"buffer_load_dwordx4 v23, s[24:27], s75 offen offset:3072 lds\n"
"v_mfma_f32_16x16x32_bf16 v[160:163], a[224:227], a[8:11], v[160:163]\n"
"ds_read_b128 a[116:119], v6 offset:36864\n"
"v_mfma_f32_16x16x32_bf16 v[160:163], a[228:231], a[36:39], v[160:163]\n"
"ds_read_b128 a[120:123], v6 offset:40960\n"
"v_mfma_f32_16x16x32_bf16 v[168:171], a[228:231], a[40:43], v[168:171]\n"
"ds_read_b128 a[148:151], v6 offset:43008\n"
"v_mfma_f32_16x16x32_bf16 v[168:171], a[224:227], a[12:15], v[168:171]\n"
"v_mfma_f32_16x16x32_bf16 v[176:179], a[224:227], a[16:19], v[176:179]\n"
"ds_read_b64_tr_b16 a[252:253], v12 offset:34816\n"
"v_mfma_f32_16x16x32_bf16 v[176:179], a[228:231], a[44:47], v[176:179]\n"
"ds_read_b64_tr_b16 a[254:255], v12 offset:35840\n"
"v_mfma_f32_16x16x32_bf16 v[184:187], a[228:231], a[48:51], v[184:187]\n"
"ds_read_b64_tr_b16 a[248:249], v12 offset:32768\n"
"v_mfma_f32_16x16x32_bf16 v[184:187], a[224:227], a[20:23], v[184:187]\n"
"s_xor_b32 s74, s48, 99072\n"
"v_mfma_f32_16x16x32_bf16 v[192:195], a[224:227], a[24:27], v[192:195]\n"
"ds_read_b64_tr_b16 a[250:251], v12 offset:33792\n"
"v_mfma_f32_16x16x32_bf16 v[192:195], a[228:231], a[52:55], v[192:195]\n"
"ds_read_b128 a[152:155], v6 offset:47104\n"
"v_mfma_f32_16x16x32_bf16 v[196:199], a[236:239], a[52:55], v[196:199]\n"
"s_waitcnt vmcnt(4)\n"
"v_cmp_ne_u64 vcc, s[68:69], v[16:17]\n"
"s_cbranch_vccnz 43f\n"
"53:\n"
"v_mfma_f32_16x16x32_bf16 v[196:199], a[232:235], a[24:27], v[196:199]\n"
"s_xor_b32 s76, s55, 0\n"
"buffer_load_dwordx4 v[32:35], v1, s[20:23], s76 offen sc1\n"
"v_mfma_f32_16x16x32_bf16 v[188:191], a[232:235], a[20:23], v[188:191]\n"
"ds_read_b128 a[124:127], v6 offset:45056\n"
"v_mfma_f32_16x16x32_bf16 v[188:191], a[236:239], a[48:51], v[188:191]\n"
"s_add_u32 m0, s46, 32768\n"
"ds_read_b128 a[128:131], v6 offset:49152\n"
"v_mfma_f32_16x16x32_bf16 v[180:183], a[236:239], a[44:47], v[180:183]\n"
"s_waitcnt lgkmcnt(3)\n"
"ds_read_b128 a[156:159], v6 offset:51200\n"
"v_mfma_f32_16x16x32_bf16 v[180:183], a[232:235], a[16:19], v[180:183]\n"
"s_xor_b32 s76, s52, 0\n"
"buffer_load_dwordx4 v14, s[24:27], s74 offen lds\n"
"v_mfma_f32_16x16x32_bf16 v[172:175], a[232:235], a[12:15], v[172:175]\n"
"ds_read_b128 a[160:163], v6 offset:55296\n"
"v_mfma_f32_16x16x32_bf16 v[172:175], a[236:239], a[40:43], v[172:175]\n"
"ds_read_b128 a[132:135], v6 offset:53248\n"
"v_mfma_f32_16x16x32_bf16 v[164:167], a[236:239], a[36:39], v[164:167]\n"
"ds_read_b64_tr_b16 a[224:225], v11 offset:36864\n"
"v_mfma_f32_16x16x32_bf16 v[164:167], a[232:235], a[8:11], v[164:167]\n"
"buffer_load_dwordx4 v15, s[24:27], s74 offen offset:1024 lds\n"
"v_mfma_f32_16x16x32_bf16 v[156:159], a[232:235], a[4:7], v[156:159]\n"
"buffer_store_dwordx2 v[8:9], v19, s[20:23], s60 offen offset:512 sc1\n"
"v_mfma_f32_16x16x32_bf16 v[156:159], a[236:239], a[32:35], v[156:159]\n"
"ds_read_b64_tr_b16 a[226:227], v11 offset:37888\n"
"v_mfma_f32_16x16x32_bf16 v[148:151], a[236:239], a[28:31], v[148:151]\n"
"ds_read_b64_tr_b16 a[228:229], v11 offset:38912\n"
"v_mfma_f32_16x16x32_bf16 v[148:151], a[232:235], a[0:3], v[148:151]\n"
"buffer_load_dwordx4 v21, s[24:27], s74 offen offset:2048 lds\n"
"v_mfma_f32_16x16x32_bf16 v[144:147], a[240:243], a[56:59], v[144:147]\n"
"ds_read_b64_tr_b16 a[230:231], v11 offset:39936\n"
"v_mfma_f32_16x16x32_bf16 v[144:147], a[244:247], a[84:87], v[144:147]\n"
"ds_read_b128 a[136:139], v6 offset:57344\n"
"v_mfma_f32_16x16x32_bf16 v[152:155], a[244:247], a[88:91], v[152:155]\n"
"ds_read_b128 a[164:167], v6 offset:59392\n"
"v_mfma_f32_16x16x32_bf16 v[152:155], a[240:243], a[60:63], v[152:155]\n"
"buffer_load_dwordx4 v23, s[24:27], s74 offen offset:3072 lds\n"
"v_mfma_f32_16x16x32_bf16 v[160:163], a[240:243], a[64:67], v[160:163]\n"
"ds_read_b128 a[168:171], v6 offset:33792\n"
"v_mfma_f32_16x16x32_bf16 v[160:163], a[244:247], a[92:95], v[160:163]\n"
"ds_read_b128 a[196:199], v6 offset:35840\n"
"v_mfma_f32_16x16x32_bf16 v[168:171], a[244:247], a[96:99], v[168:171]\n"
"ds_read_b128 a[200:203], v6 offset:39936\n"
"v_mfma_f32_16x16x32_bf16 v[168:171], a[240:243], a[68:71], v[168:171]\n"
"buffer_load_dwordx4 v[36:39], v1, s[20:23], s76 offen offset:1024 sc1\n"
"v_mfma_f32_16x16x32_bf16 v[176:179], a[240:243], a[72:75], v[176:179]\n"
"ds_read_b64_tr_b16 a[236:237], v12 offset:38912\n"
"v_mfma_f32_16x16x32_bf16 v[176:179], a[244:247], a[100:103], v[176:179]\n"
"ds_read_b64_tr_b16 a[238:239], v12 offset:39936\n"
"v_mfma_f32_16x16x32_bf16 v[184:187], a[244:247], a[104:107], v[184:187]\n"
"ds_read_b64_tr_b16 a[232:233], v12 offset:36864\n"
"v_mfma_f32_16x16x32_bf16 v[184:187], a[240:243], a[76:79], v[184:187]\n"
"s_xor_b32 s75, s48, 768\n"
"buffer_load_dwordx4 v[40:43], v1, s[20:23], s76 offen offset:2048 sc1\n"
"v_mfma_f32_16x16x32_bf16 v[192:195], a[240:243], a[80:83], v[192:195]\n"
"ds_read_b64_tr_b16 a[234:235], v12 offset:37888\n"
"v_mfma_f32_16x16x32_bf16 v[192:195], a[244:247], a[108:111], v[192:195]\n"
"ds_read_b128 a[172:175], v6 offset:37888\n"
"v_mfma_f32_16x16x32_bf16 v[196:199], a[252:255], a[108:111], v[196:199]\n"
"ds_read_b128 a[176:179], v6 offset:41984\n"
"v_mfma_f32_16x16x32_bf16 v[196:199], a[248:251], a[80:83], v[196:199]\n"
"buffer_load_dwordx4 v[44:47], v1, s[20:23], s76 offen offset:3072 sc1\n"
"v_mfma_f32_16x16x32_bf16 v[188:191], a[248:251], a[76:79], v[188:191]\n"
"ds_read_b128 a[204:207], v6 offset:44032\n"
"v_mfma_f32_16x16x32_bf16 v[188:191], a[252:255], a[104:107], v[188:191]\n"
"s_add_u32 m0, s46, 36864\n"
"ds_read_b128 a[208:211], v6 offset:48128\n"
"v_mfma_f32_16x16x32_bf16 v[180:183], a[252:255], a[100:103], v[180:183]\n"
"s_waitcnt lgkmcnt(4)\n"
"ds_read_b128 a[180:183], v6 offset:46080\n"
"v_mfma_f32_16x16x32_bf16 v[180:183], a[248:251], a[72:75], v[180:183]\n"
"s_xor_b32 s76, s55, 4096\n"
"buffer_load_dwordx4 v14, s[24:27], s75 offen lds\n"
"v_mfma_f32_16x16x32_bf16 v[172:175], a[248:251], a[68:71], v[172:175]\n"
"ds_read_b128 a[184:187], v6 offset:50176\n"
"v_mfma_f32_16x16x32_bf16 v[172:175], a[252:255], a[96:99], v[172:175]\n"
"ds_read_b128 a[212:215], v6 offset:52224\n"
"v_mfma_f32_16x16x32_bf16 v[164:167], a[252:255], a[92:95], v[164:167]\n"
"ds_read_b64_tr_b16 a[240:241], v11 offset:4096\n"
"v_mfma_f32_16x16x32_bf16 v[164:167], a[248:251], a[64:67], v[164:167]\n"
"buffer_load_dwordx4 v15, s[24:27], s75 offen offset:1024 lds\n"
"v_mfma_f32_16x16x32_bf16 v[156:159], a[248:251], a[60:63], v[156:159]\n"
"ds_read_b64_tr_b16 a[242:243], v11 offset:5120\n"
"v_mfma_f32_16x16x32_bf16 v[156:159], a[252:255], a[88:91], v[156:159]\n"
"ds_read_b128 a[216:219], v6 offset:56320\n"
"v_mfma_f32_16x16x32_bf16 v[148:151], a[252:255], a[84:87], v[148:151]\n"
"ds_read_b64_tr_b16 a[244:245], v11 offset:6144\n"
"v_mfma_f32_16x16x32_bf16 v[148:151], a[248:251], a[56:59], v[148:151]\n"
"buffer_load_dwordx4 v21, s[24:27], s75 offen offset:2048 lds\n"
"v_mfma_f32_16x16x32_bf16 v[144:147], a[224:227], a[112:115], v[144:147]\n"
"ds_read_b64_tr_b16 a[246:247], v11 offset:7168\n"
"v_mfma_f32_16x16x32_bf16 v[144:147], a[228:231], a[140:143], v[144:147]\n"
"ds_read_b128 a[188:191], v6 offset:54272\n"
"v_mfma_f32_16x16x32_bf16 v[152:155], a[228:231], a[144:147], v[152:155]\n"
"ds_read_b128 a[192:195], v6 offset:58368\n"
"v_mfma_f32_16x16x32_bf16 v[152:155], a[224:227], a[116:119], v[152:155]\n"
"buffer_load_dwordx4 v23, s[24:27], s75 offen offset:3072 lds\n"
"v_mfma_f32_16x16x32_bf16 v[160:163], a[224:227], a[120:123], v[160:163]\n"
"ds_read_b128 a[220:223], v6 offset:60416\n"
"v_mfma_f32_16x16x32_bf16 v[160:163], a[228:231], a[148:151], v[160:163]\n"
"buffer_load_dwordx4 v[48:51], v10, s[20:23], s76 offen sc1\n"
"v_mfma_f32_16x16x32_bf16 v[168:171], a[228:231], a[152:155], v[168:171]\n"
"v_mfma_f32_16x16x32_bf16 v[168:171], a[224:227], a[124:127], v[168:171]\n"
"s_xor_b32 s76, s52, 4096\n"
"buffer_load_dwordx4 v[52:55], v10, s[20:23], s76 offen offset:1024 sc1\n"
"v_mfma_f32_16x16x32_bf16 v[176:179], a[224:227], a[128:131], v[176:179]\n"
"ds_read_b64_tr_b16 a[252:253], v12 offset:6144\n"
"v_mfma_f32_16x16x32_bf16 v[176:179], a[228:231], a[156:159], v[176:179]\n"
"ds_read_b64_tr_b16 a[254:255], v12 offset:7168\n"
"v_mfma_f32_16x16x32_bf16 v[184:187], a[228:231], a[160:163], v[184:187]\n"
"ds_read_b64_tr_b16 a[248:249], v12 offset:4096\n"
"v_mfma_f32_16x16x32_bf16 v[184:187], a[224:227], a[132:135], v[184:187]\n"
"s_xor_b32 s74, s48, 33536\n"
"buffer_load_dwordx4 v[56:59], v10, s[20:23], s76 offen offset:2048 sc1\n"
"v_mfma_f32_16x16x32_bf16 v[192:195], a[224:227], a[136:139], v[192:195]\n"
"ds_read_b64_tr_b16 a[250:251], v12 offset:5120\n"
"v_mfma_f32_16x16x32_bf16 v[192:195], a[228:231], a[164:167], v[192:195]\n"
"v_mfma_f32_16x16x32_bf16 v[196:199], a[236:239], a[164:167], v[196:199]\n"
"v_mfma_f32_16x16x32_bf16 v[196:199], a[232:235], a[136:139], v[196:199]\n"
"buffer_load_dwordx4 v[60:63], v10, s[20:23], s76 offen offset:3072 sc1\n"
"v_mfma_f32_16x16x32_bf16 v[188:191], a[232:235], a[132:135], v[188:191]\n"
"v_mfma_f32_16x16x32_bf16 v[188:191], a[236:239], a[160:163], v[188:191]\n"
"s_add_u32 m0, s46, 4096\n"
"v_mfma_f32_16x16x32_bf16 v[180:183], a[236:239], a[156:159], v[180:183]\n"
"s_waitcnt lgkmcnt(0)\n"
"v_mfma_f32_16x16x32_bf16 v[180:183], a[232:235], a[128:131], v[180:183]\n"
"buffer_load_dwordx4 v14, s[24:27], s74 offen lds\n"
"v_mfma_f32_16x16x32_bf16 v[172:175], a[232:235], a[124:127], v[172:175]\n"
"v_mfma_f32_16x16x32_bf16 v[172:175], a[236:239], a[152:155], v[172:175]\n"
"v_mfma_f32_16x16x32_bf16 v[164:167], a[236:239], a[148:151], v[164:167]\n"
"ds_read_b64_tr_b16 a[224:225], v2 offset:0\n"
"v_mfma_f32_16x16x32_bf16 v[164:167], a[232:235], a[120:123], v[164:167]\n"
"buffer_load_dwordx4 v15, s[24:27], s74 offen offset:1024 lds\n"
"v_mfma_f32_16x16x32_bf16 v[156:159], a[232:235], a[116:119], v[156:159]\n"
"ds_read_b64_tr_b16 a[226:227], v2 offset:1024\n"
"v_mfma_f32_16x16x32_bf16 v[156:159], a[236:239], a[144:147], v[156:159]\n"
"v_mfma_f32_16x16x32_bf16 v[148:151], a[236:239], a[140:143], v[148:151]\n"
"ds_read_b64_tr_b16 a[228:229], v2 offset:2048\n"
"v_mfma_f32_16x16x32_bf16 v[148:151], a[232:235], a[112:115], v[148:151]\n"
"buffer_load_dwordx4 v21, s[24:27], s74 offen offset:2048 lds\n"
"v_mfma_f32_16x16x32_bf16 v[144:147], a[240:243], a[168:171], v[144:147]\n"
"ds_read_b64_tr_b16 a[230:231], v2 offset:3072\n"
"v_mfma_f32_16x16x32_bf16 v[144:147], a[244:247], a[196:199], v[144:147]\n"
"s_xor_b32 s61, s59, 1048576\n"
"buffer_load_dwordx2 v[16:17], v5, s[20:23], s61 offen offset:512 sc1\n"
"v_mfma_f32_16x16x32_bf16 v[152:155], a[244:247], a[200:203], v[152:155]\n"
"buffer_load_dwordx4 v23, s[24:27], s74 offen offset:3072 lds\n"
"v_mfma_f32_16x16x32_bf16 v[152:155], a[240:243], a[172:175], v[152:155]\n"
"v_mfma_f32_16x16x32_bf16 v[160:163], a[240:243], a[176:179], v[160:163]\n"
"v_mfma_f32_16x16x32_bf16 v[160:163], a[244:247], a[204:207], v[160:163]\n"
"v_mfma_f32_16x16x32_bf16 v[168:171], a[244:247], a[208:211], v[168:171]\n"
"v_mfma_f32_16x16x32_bf16 v[168:171], a[240:243], a[180:183], v[168:171]\n"
"v_mfma_f32_16x16x32_bf16 v[176:179], a[240:243], a[184:187], v[176:179]\n"
"ds_read_b64_tr_b16 a[236:237], v3 offset:2048\n"
"v_mfma_f32_16x16x32_bf16 v[176:179], a[244:247], a[212:215], v[176:179]\n"
"ds_read_b64_tr_b16 a[238:239], v3 offset:3072\n"
"v_mfma_f32_16x16x32_bf16 v[184:187], a[244:247], a[216:219], v[184:187]\n"
"ds_read_b64_tr_b16 a[232:233], v3 offset:0\n"
"v_mfma_f32_16x16x32_bf16 v[184:187], a[240:243], a[188:191], v[184:187]\n"
"v_mfma_f32_16x16x32_bf16 v[192:195], a[240:243], a[192:195], v[192:195]\n"
"ds_read_b64_tr_b16 a[234:235], v3 offset:1024\n"
"v_mfma_f32_16x16x32_bf16 v[192:195], a[244:247], a[220:223], v[192:195]\n"
"v_mfma_f32_16x16x32_bf16 v[196:199], a[252:255], a[220:223], v[196:199]\n"
"v_mfma_f32_16x16x32_bf16 v[196:199], a[248:251], a[192:195], v[196:199]\n"
"s_xor_b32 s38, s52, 1310720\n"
"v_mfma_f32_16x16x32_bf16 v[188:191], a[248:251], a[188:191], v[188:191]\n"
"v_mfma_f32_16x16x32_bf16 v[188:191], a[252:255], a[216:219], v[188:191]\n"
"v_mfma_f32_16x16x32_bf16 v[180:183], a[252:255], a[212:215], v[180:183]\n"
"v_mfma_f32_16x16x32_bf16 v[180:183], a[248:251], a[184:187], v[180:183]\n"
"s_nop 0\n"
"v_cvt_pk_bf16_f32 v192, v192, v193\n"
"v_cvt_pk_bf16_f32 v193, v194, v195\n"
"v_mfma_f32_16x16x32_bf16 v[172:175], a[248:251], a[180:183], v[172:175]\n"
"v_cvt_pk_bf16_f32 v194, v196, v197\n"
"v_cvt_pk_bf16_f32 v195, v198, v199\n"
"v_mfma_f32_16x16x32_bf16 v[172:175], a[252:255], a[208:211], v[172:175]\n"
"buffer_store_dwordx4 v[192:195], v1, s[20:23], s38 offen offset:3072 sc1\n"
"v_mfma_f32_16x16x32_bf16 v[164:167], a[252:255], a[204:207], v[164:167]\n"
"ds_read_b64_tr_b16 a[240:241], v2 offset:4096\n"
"v_cvt_pk_bf16_f32 v184, v184, v185\n"
"v_mfma_f32_16x16x32_bf16 v[164:167], a[248:251], a[176:179], v[164:167]\n"
"v_cvt_pk_bf16_f32 v185, v186, v187\n"
"v_cvt_pk_bf16_f32 v186, v188, v189\n"
"v_mfma_f32_16x16x32_bf16 v[156:159], a[248:251], a[172:175], v[156:159]\n"
"ds_read_b64_tr_b16 a[242:243], v2 offset:5120\n"
"v_cvt_pk_bf16_f32 v187, v190, v191\n"
"v_mfma_f32_16x16x32_bf16 v[156:159], a[252:255], a[200:203], v[156:159]\n"
"buffer_store_dwordx4 v[184:187], v1, s[20:23], s38 offen offset:2048 sc1\n"
"v_mfma_f32_16x16x32_bf16 v[148:151], a[252:255], a[196:199], v[148:151]\n"
"s_waitcnt lgkmcnt(8)\n"
"ds_read_b64_tr_b16 a[244:245], v2 offset:6144\n"
"v_cvt_pk_bf16_f32 v176, v176, v177\n"
"v_mfma_f32_16x16x32_bf16 v[148:151], a[248:251], a[168:171], v[148:151]\n"
"v_cvt_pk_bf16_f32 v177, v178, v179\n"
"v_cvt_pk_bf16_f32 v178, v180, v181\n"
"v_mfma_f32_16x16x32_bf16 v[200:203], a[224:227], a[0:3], v[200:203]\n"
"s_waitcnt lgkmcnt(7)\n"
"ds_read_b64_tr_b16 a[246:247], v2 offset:7168\n"
"v_cvt_pk_bf16_f32 v179, v182, v183\n"
"v_mfma_f32_16x16x32_bf16 v[200:203], a[228:231], a[28:31], v[200:203]\n"
"buffer_store_dwordx4 v[176:179], v1, s[20:23], s38 offen offset:1024 sc1\n"
"v_mfma_f32_16x16x32_bf16 v[208:211], a[228:231], a[32:35], v[208:211]\n"
"v_cvt_pk_bf16_f32 v168, v168, v169\n"
"v_cvt_pk_bf16_f32 v169, v170, v171\n"
"v_mfma_f32_16x16x32_bf16 v[208:211], a[224:227], a[4:7], v[208:211]\n"
"s_xor_b32 s38, s53, 1310720\n"
"v_cvt_pk_bf16_f32 v170, v172, v173\n"
"v_cvt_pk_bf16_f32 v171, v174, v175\n"
"v_mfma_f32_16x16x32_bf16 v[216:219], a[224:227], a[8:11], v[216:219]\n"
"v_cvt_pk_bf16_f32 v160, v160, v161\n"
"v_cvt_pk_bf16_f32 v161, v162, v163\n"
"v_mfma_f32_16x16x32_bf16 v[216:219], a[228:231], a[36:39], v[216:219]\n"
"buffer_store_dwordx4 v[168:171], v1, s[20:23], s38 offen sc1\n"
"v_mfma_f32_16x16x32_bf16 v[224:227], a[228:231], a[40:43], v[224:227]\n"
"v_cvt_pk_bf16_f32 v162, v164, v165\n"
"v_cvt_pk_bf16_f32 v163, v166, v167\n"
"v_mfma_f32_16x16x32_bf16 v[224:227], a[224:227], a[12:15], v[224:227]\n"
"s_xor_b32 s38, s52, 1441792\n"
"v_cvt_pk_bf16_f32 v152, v152, v153\n"
"v_cvt_pk_bf16_f32 v153, v154, v155\n"
"v_mfma_f32_16x16x32_bf16 v[232:235], a[224:227], a[16:19], v[232:235]\n"
"ds_read_b64_tr_b16 a[252:253], v3 offset:6144\n"
"v_cvt_pk_bf16_f32 v154, v156, v157\n"
"v_mfma_f32_16x16x32_bf16 v[232:235], a[228:231], a[44:47], v[232:235]\n"
"ds_read_b64_tr_b16 a[254:255], v3 offset:7168\n"
"v_cvt_pk_bf16_f32 v155, v158, v159\n"
"v_mfma_f32_16x16x32_bf16 v[240:243], a[228:231], a[48:51], v[240:243]\n"
"ds_read_b64_tr_b16 a[248:249], v3 offset:4096\n"
"v_cvt_pk_bf16_f32 v144, v144, v145\n"
"v_mfma_f32_16x16x32_bf16 v[240:243], a[224:227], a[20:23], v[240:243]\n"
"buffer_store_dwordx4 v[160:163], v1, s[20:23], s38 offen offset:1024 sc1\n"
"v_mfma_f32_16x16x32_bf16 v[248:251], a[224:227], a[24:27], v[248:251]\n"
"s_waitcnt lgkmcnt(9)\n"
"ds_read_b64_tr_b16 a[250:251], v3 offset:5120\n"
"v_cvt_pk_bf16_f32 v145, v146, v147\n"
"v_mfma_f32_16x16x32_bf16 v[248:251], a[228:231], a[52:55], v[248:251]\n"
"v_cvt_pk_bf16_f32 v146, v148, v149\n"
"v_cvt_pk_bf16_f32 v147, v150, v151\n"
"v_mfma_f32_16x16x32_bf16 v[252:255], a[236:239], a[52:55], v[252:255]\n"
"s_waitcnt lgkmcnt(8)\n"
"v_mfma_f32_16x16x32_bf16 v[252:255], a[232:235], a[24:27], v[252:255]\n"
"buffer_store_dwordx4 v[152:155], v1, s[20:23], s38 offen offset:2048 sc1\n"
"v_mfma_f32_16x16x32_bf16 v[244:247], a[232:235], a[20:23], v[244:247]\n"
"v_mfma_f32_16x16x32_bf16 v[244:247], a[236:239], a[48:51], v[244:247]\n"
"v_mfma_f32_16x16x32_bf16 v[236:239], a[236:239], a[44:47], v[236:239]\n"
"v_mfma_f32_16x16x32_bf16 v[236:239], a[232:235], a[16:19], v[236:239]\n"
"buffer_store_dwordx4 v[144:147], v1, s[20:23], s38 offen offset:3072 sc1\n"
"v_mfma_f32_16x16x32_bf16 v[228:231], a[232:235], a[12:15], v[228:231]\n"
"v_mfma_f32_16x16x32_bf16 v[228:231], a[236:239], a[40:43], v[228:231]\n"
"v_mfma_f32_16x16x32_bf16 v[220:223], a[236:239], a[36:39], v[220:223]\n"
"s_waitcnt vmcnt(31)\n"
"ds_read_b64_tr_b16 a[224:225], v11 offset:0\n"
"v_mfma_f32_16x16x32_bf16 v[220:223], a[232:235], a[8:11], v[220:223]\n"
"v_mfma_f32_16x16x32_bf16 v[212:215], a[232:235], a[4:7], v[212:215]\n"
"ds_read_b64_tr_b16 a[226:227], v11 offset:1024\n"
"v_mfma_f32_16x16x32_bf16 v[212:215], a[236:239], a[32:35], v[212:215]\n"
"v_mfma_f32_16x16x32_bf16 v[204:207], a[236:239], a[28:31], v[204:207]\n"
"s_waitcnt vmcnt(29) lgkmcnt(8)\n"
"ds_read_b64_tr_b16 a[228:229], v11 offset:2048\n"
"v_mfma_f32_16x16x32_bf16 v[204:207], a[232:235], a[0:3], v[204:207]\n"
"v_mfma_f32_16x16x32_bf16 v[200:203], a[240:243], a[56:59], v[200:203]\n"
"s_waitcnt vmcnt(8) lgkmcnt(7)\n"
"v_cmp_ne_u64 vcc, s[68:69], v[16:17]\n"
"s_cbranch_vccnz 44f\n"
"54:\n"
"v_mfma_f32_16x16x32_bf16 v[200:203], a[244:247], a[84:87], v[200:203]\n"
"s_xor_b32 s76, s55, 65536\n"
"ds_read_b64_tr_b16 a[230:231], v11 offset:3072\n"
"v_mfma_f32_16x16x32_bf16 v[208:211], a[244:247], a[88:91], v[208:211]\n"
"ds_read_b64_tr_b16 a[232:233], v12 offset:0\n"
"v_mfma_f32_16x16x32_bf16 v[208:211], a[240:243], a[60:63], v[208:211]\n"
"buffer_load_dwordx4 v[64:67], v1, s[20:23], s76 offen sc1\n"
"v_mfma_f32_16x16x32_bf16 v[216:219], a[240:243], a[64:67], v[216:219]\n"
"ds_read_b64_tr_b16 a[234:235], v12 offset:1024\n"
"v_mfma_f32_16x16x32_bf16 v[216:219], a[244:247], a[92:95], v[216:219]\n"
"s_xor_b32 s76, s52, 65536\n"
"ds_read_b64_tr_b16 a[236:237], v12 offset:2048\n"
"v_mfma_f32_16x16x32_bf16 v[224:227], a[244:247], a[96:99], v[224:227]\n"
"ds_read_b64_tr_b16 a[238:239], v12 offset:3072\n"
"v_mfma_f32_16x16x32_bf16 v[224:227], a[240:243], a[68:71], v[224:227]\n"
"buffer_load_dwordx4 v[68:71], v1, s[20:23], s76 offen offset:1024 sc1\n"
"v_mfma_f32_16x16x32_bf16 v[232:235], a[240:243], a[72:75], v[232:235]\n"
"ds_read_b128 a[0:3], v6 offset:0\n"
"v_mfma_f32_16x16x32_bf16 v[232:235], a[244:247], a[100:103], v[232:235]\n"
"ds_read_b128 a[28:31], v6 offset:2048\n"
"v_mfma_f32_16x16x32_bf16 v[240:243], a[244:247], a[104:107], v[240:243]\n"
"ds_read_b128 a[32:35], v6 offset:6144\n"
"v_mfma_f32_16x16x32_bf16 v[240:243], a[240:243], a[76:79], v[240:243]\n"
"buffer_load_dwordx4 v[72:75], v1, s[20:23], s76 offen offset:2048 sc1\n"
"v_mfma_f32_16x16x32_bf16 v[248:251], a[240:243], a[80:83], v[248:251]\n"
"s_waitcnt lgkmcnt(13)\n"
"ds_read_b128 a[4:7], v6 offset:4096\n"
"v_mfma_f32_16x16x32_bf16 v[248:251], a[244:247], a[108:111], v[248:251]\n"
"ds_read_b128 a[8:11], v6 offset:8192\n"
"v_mfma_f32_16x16x32_bf16 v[252:255], a[252:255], a[108:111], v[252:255]\n"
"s_waitcnt lgkmcnt(13)\n"
"ds_read_b64_tr_b16 a[240:241], v11 offset:32768\n"
"v_mfma_f32_16x16x32_bf16 v[252:255], a[248:251], a[80:83], v[252:255]\n"
"buffer_load_dwordx4 v[76:79], v1, s[20:23], s76 offen offset:3072 sc1\n"
"v_mfma_f32_16x16x32_bf16 v[244:247], a[248:251], a[76:79], v[244:247]\n"
"ds_read_b64_tr_b16 a[242:243], v11 offset:33792\n"
"v_mfma_f32_16x16x32_bf16 v[244:247], a[252:255], a[104:107], v[244:247]\n"
"s_xor_b32 s76, s55, 69632\n"
"ds_read_b128 a[36:39], v6 offset:10240\n"
"v_mfma_f32_16x16x32_bf16 v[236:239], a[252:255], a[100:103], v[236:239]\n"
"ds_read_b64_tr_b16 a[244:245], v11 offset:34816\n"
"v_mfma_f32_16x16x32_bf16 v[236:239], a[248:251], a[72:75], v[236:239]\n"
"buffer_load_dwordx4 v[80:83], v10, s[20:23], s76 offen sc1\n"
"v_mfma_f32_16x16x32_bf16 v[228:231], a[248:251], a[68:71], v[228:231]\n"
"ds_read_b64_tr_b16 a[246:247], v11 offset:35840\n"
"v_mfma_f32_16x16x32_bf16 v[228:231], a[252:255], a[96:99], v[228:231]\n"
"s_xor_b32 s76, s52, 69632\n"
"ds_read_b128 a[40:43], v6 offset:14336\n"
"v_mfma_f32_16x16x32_bf16 v[220:223], a[252:255], a[92:95], v[220:223]\n"
"ds_read_b128 a[12:15], v6 offset:12288\n"
"v_mfma_f32_16x16x32_bf16 v[220:223], a[248:251], a[64:67], v[220:223]\n"
"buffer_load_dwordx4 v[84:87], v10, s[20:23], s76 offen offset:1024 sc1\n"
"v_mfma_f32_16x16x32_bf16 v[212:215], a[248:251], a[60:63], v[212:215]\n"
"v_mfma_f32_16x16x32_bf16 v[212:215], a[252:255], a[88:91], v[212:215]\n"
"v_mfma_f32_16x16x32_bf16 v[204:207], a[252:255], a[84:87], v[204:207]\n"
"s_waitcnt lgkmcnt(15)\n"
"v_mfma_f32_16x16x32_bf16 v[204:207], a[248:251], a[56:59], v[204:207]\n"
"buffer_load_dwordx4 v[88:91], v10, s[20:23], s76 offen offset:2048 sc1\n"
"v_mfma_f32_16x16x32_bf16 v[200:203], a[224:227], a[112:115], v[200:203]\n"
"s_waitcnt lgkmcnt(15)\n"
"ds_read_b64_tr_b16 a[248:249], v12 offset:32768\n"
"v_mfma_f32_16x16x32_bf16 v[200:203], a[228:231], a[140:143], v[200:203]\n"
"ds_read_b64_tr_b16 a[250:251], v12 offset:33792\n"
"v_mfma_f32_16x16x32_bf16 v[208:211], a[228:231], a[144:147], v[208:211]\n"
"ds_read_b64_tr_b16 a[252:253], v12 offset:34816\n"
"v_mfma_f32_16x16x32_bf16 v[208:211], a[224:227], a[116:119], v[208:211]\n"
"buffer_load_dwordx4 v[92:95], v10, s[20:23], s76 offen offset:3072 sc1\n"
"v_mfma_f32_16x16x32_bf16 v[216:219], a[224:227], a[120:123], v[216:219]\n"
"ds_read_b64_tr_b16 a[254:255], v12 offset:35840\n"
"v_mfma_f32_16x16x32_bf16 v[216:219], a[228:231], a[148:151], v[216:219]\n"
"ds_read_b128 a[56:59], v6 offset:1024\n"
"v_mfma_f32_16x16x32_bf16 v[224:227], a[228:231], a[152:155], v[224:227]\n"
"s_waitcnt lgkmcnt(15)\n"
"ds_read_b64_tr_b16 v[144:145], v11 offset:36864\n"
"v_mfma_f32_16x16x32_bf16 v[224:227], a[224:227], a[124:127], v[224:227]\n"
"v_mfma_f32_16x16x32_bf16 v[228:231], a[232:235], a[124:127], v[228:231]\n"
"s_waitcnt lgkmcnt(15)\n"
"ds_read_b64_tr_b16 v[146:147], v11 offset:37888\n"
"v_mfma_f32_16x16x32_bf16 v[228:231], a[236:239], a[152:155], v[228:231]\n"
"ds_read_b128 a[84:87], v6 offset:3072\n"
"v_mfma_f32_16x16x32_bf16 v[220:223], a[236:239], a[148:151], v[220:223]\n"
"ds_read_b64_tr_b16 v[148:149], v11 offset:38912\n"
"v_mfma_f32_16x16x32_bf16 v[220:223], a[232:235], a[120:123], v[220:223]\n"
"v_mfma_f32_16x16x32_bf16 v[212:215], a[232:235], a[116:119], v[212:215]\n"
"ds_read_b64_tr_b16 v[150:151], v11 offset:39936\n"
"v_mfma_f32_16x16x32_bf16 v[212:215], a[236:239], a[144:147], v[212:215]\n"
"ds_read_b64_tr_b16 v[152:153], v12 offset:36864\n"
"v_mfma_f32_16x16x32_bf16 v[204:207], a[236:239], a[140:143], v[204:207]\n"
"s_waitcnt lgkmcnt(15)\n"
"ds_read_b64_tr_b16 v[154:155], v12 offset:37888\n"
"v_mfma_f32_16x16x32_bf16 v[204:207], a[232:235], a[112:115], v[204:207]\n"
"v_mfma_f32_16x16x32_bf16 v[200:203], a[240:243], a[168:171], v[200:203]\n"
"s_waitcnt vmcnt(8) lgkmcnt(14)\n"
"buffer_store_dwordx2 v[8:9], v19, s[20:23], s60 offen offset:640 sc1\n"
"v_mfma_f32_16x16x32_bf16 v[200:203], a[244:247], a[196:199], v[200:203]\n"
"ds_read_b64_tr_b16 v[156:157], v12 offset:38912\n"
"v_mfma_f32_16x16x32_bf16 v[208:211], a[244:247], a[200:203], v[208:211]\n"
"ds_read_b64_tr_b16 v[158:159], v12 offset:39936\n"
"v_mfma_f32_16x16x32_bf16 v[208:211], a[240:243], a[172:175], v[208:211]\n"
"v_mfma_f32_16x16x32_bf16 v[216:219], a[240:243], a[176:179], v[216:219]\n"
"ds_read_b64_tr_b16 v[160:161], v11 offset:4096\n"
"v_mfma_f32_16x16x32_bf16 v[216:219], a[244:247], a[204:207], v[216:219]\n"
"ds_read_b64_tr_b16 v[162:163], v11 offset:5120\n"
"v_mfma_f32_16x16x32_bf16 v[224:227], a[244:247], a[208:211], v[224:227]\n"
"s_waitcnt lgkmcnt(14)\n"
"ds_read_b64_tr_b16 v[164:165], v11 offset:6144\n"
"v_mfma_f32_16x16x32_bf16 v[224:227], a[240:243], a[180:183], v[224:227]\n"
"v_mfma_f32_16x16x32_bf16 v[228:231], a[248:251], a[180:183], v[228:231]\n"
"s_waitcnt lgkmcnt(13)\n"
"ds_read_b64_tr_b16 v[166:167], v11 offset:7168\n"
"v_mfma_f32_16x16x32_bf16 v[228:231], a[252:255], a[208:211], v[228:231]\n"
"ds_read_b128 a[88:91], v6 offset:7168\n"
"v_mfma_f32_16x16x32_bf16 v[220:223], a[252:255], a[204:207], v[220:223]\n"
"ds_read_b128 a[60:63], v6 offset:5120\n"
"v_mfma_f32_16x16x32_bf16 v[220:223], a[248:251], a[176:179], v[220:223]\n"
"v_mfma_f32_16x16x32_bf16 v[212:215], a[248:251], a[172:175], v[212:215]\n"
"ds_read_b128 a[64:67], v6 offset:9216\n"
"v_mfma_f32_16x16x32_bf16 v[212:215], a[252:255], a[200:203], v[212:215]\n"
"ds_read_b128 a[92:95], v6 offset:11264\n"
"v_mfma_f32_16x16x32_bf16 v[204:207], a[252:255], a[196:199], v[204:207]\n"
"s_waitcnt lgkmcnt(15)\n"
"ds_read_b128 a[96:99], v6 offset:15360\n"
"v_mfma_f32_16x16x32_bf16 v[204:207], a[248:251], a[168:171], v[204:207]\n"
"v_mfma_f32_16x16x32_bf16 v[200:203], v[144:147], a[0:3], v[200:203]\n"
"s_waitcnt lgkmcnt(13)\n"
"ds_read_b128 a[68:71], v6 offset:13312\n"
"v_mfma_f32_16x16x32_bf16 v[200:203], v[148:151], a[28:31], v[200:203]\n"
"ds_read_b64_tr_b16 v[168:169], v12 offset:4096\n"
"v_mfma_f32_16x16x32_bf16 v[208:211], v[148:151], a[32:35], v[208:211]\n"
"ds_read_b64_tr_b16 v[170:171], v12 offset:5120\n"
"v_mfma_f32_16x16x32_bf16 v[208:211], v[144:147], a[4:7], v[208:211]\n"
"v_mfma_f32_16x16x32_bf16 v[216:219], v[144:147], a[8:11], v[216:219]\n"
"ds_read_b64_tr_b16 v[172:173], v12 offset:6144\n"
"v_mfma_f32_16x16x32_bf16 v[216:219], v[148:151], a[36:39], v[216:219]\n"
"ds_read_b64_tr_b16 v[174:175], v12 offset:7168\n"
"v_mfma_f32_16x16x32_bf16 v[224:227], v[148:151], a[40:43], v[224:227]\n"
"s_waitcnt lgkmcnt(15)\n"
"ds_read_b128 a[16:19], v6 offset:16384\n"
"v_mfma_f32_16x16x32_bf16 v[224:227], v[144:147], a[12:15], v[224:227]\n"
"v_mfma_f32_16x16x32_bf16 v[228:231], v[152:155], a[12:15], v[228:231]\n"
"s_waitcnt lgkmcnt(15)\n"
"ds_read_b128 a[44:47], v6 offset:18432\n"
"v_mfma_f32_16x16x32_bf16 v[228:231], v[156:159], a[40:43], v[228:231]\n"
"ds_read_b128 a[48:51], v6 offset:22528\n"
"v_mfma_f32_16x16x32_bf16 v[220:223], v[156:159], a[36:39], v[220:223]\n"
"ds_read_b128 a[20:23], v6 offset:20480\n"
"v_mfma_f32_16x16x32_bf16 v[220:223], v[152:155], a[8:11], v[220:223]\n"
"v_mfma_f32_16x16x32_bf16 v[212:215], v[152:155], a[4:7], v[212:215]\n"
"ds_read_b128 a[24:27], v6 offset:24576\n"
"v_mfma_f32_16x16x32_bf16 v[212:215], v[156:159], a[32:35], v[212:215]\n"
"ds_read_b128 a[52:55], v6 offset:26624\n"
"v_mfma_f32_16x16x32_bf16 v[204:207], v[156:159], a[28:31], v[204:207]\n"
"s_waitcnt lgkmcnt(15)\n"
"ds_read_b128 a[72:75], v6 offset:17408\n"
"v_mfma_f32_16x16x32_bf16 v[204:207], v[152:155], a[0:3], v[204:207]\n"
"v_mfma_f32_16x16x32_bf16 v[200:203], v[160:163], a[56:59], v[200:203]\n"
"s_waitcnt lgkmcnt(15)\n"
"ds_read_b128 a[100:103], v6 offset:19456\n"
"v_mfma_f32_16x16x32_bf16 v[200:203], v[164:167], a[84:87], v[200:203]\n"
"ds_read_b128 a[104:107], v6 offset:23552\n"
"v_mfma_f32_16x16x32_bf16 v[208:211], v[164:167], a[88:91], v[208:211]\n"
"s_waitcnt lgkmcnt(15)\n"
"ds_read_b128 a[76:79], v6 offset:21504\n"
"v_mfma_f32_16x16x32_bf16 v[208:211], v[160:163], a[60:63], v[208:211]\n"
"v_mfma_f32_16x16x32_bf16 v[216:219], v[160:163], a[64:67], v[216:219]\n"
"s_waitcnt lgkmcnt(15)\n"
"ds_read_b128 a[80:83], v6 offset:25600\n"
"v_mfma_f32_16x16x32_bf16 v[216:219], v[164:167], a[92:95], v[216:219]\n"
"ds_read_b128 a[108:111], v6 offset:27648\n"
"v_mfma_f32_16x16x32_bf16 v[224:227], v[164:167], a[96:99], v[224:227]\n"
"s_waitcnt lgkmcnt(14)\n"
"v_mfma_f32_16x16x32_bf16 v[224:227], v[160:163], a[68:71], v[224:227]\n"
"v_mfma_f32_16x16x32_bf16 v[228:231], v[168:171], a[68:71], v[228:231]\n"
"s_waitcnt lgkmcnt(12)\n"
"v_mfma_f32_16x16x32_bf16 v[228:231], v[172:175], a[96:99], v[228:231]\n"
"s_xor_b32 s38, s54, 1114112\n"
"v_mfma_f32_16x16x32_bf16 v[220:223], v[172:175], a[92:95], v[220:223]\n"
"v_mfma_f32_16x16x32_bf16 v[220:223], v[168:171], a[64:67], v[220:223]\n"
"v_mfma_f32_16x16x32_bf16 v[212:215], v[168:171], a[60:63], v[212:215]\n"
"v_mfma_f32_16x16x32_bf16 v[212:215], v[172:175], a[88:91], v[212:215]\n"
"v_cvt_pk_bf16_f32 v224, v224, v225\n"
"v_cvt_pk_bf16_f32 v225, v226, v227\n"
"v_mfma_f32_16x16x32_bf16 v[204:207], v[172:175], a[84:87], v[204:207]\n"
"v_cvt_pk_bf16_f32 v226, v228, v229\n"
"v_cvt_pk_bf16_f32 v227, v230, v231\n"
"v_mfma_f32_16x16x32_bf16 v[204:207], v[168:171], a[56:59], v[204:207]\n"
"buffer_store_dwordx4 v[224:227], v1, s[20:23], s38 offen sc1\n"
"v_mfma_f32_16x16x32_bf16 v[232:235], a[224:227], a[128:131], v[232:235]\n"
"v_cvt_pk_bf16_f32 v216, v216, v217\n"
"v_cvt_pk_bf16_f32 v217, v218, v219\n"
"v_mfma_f32_16x16x32_bf16 v[232:235], a[228:231], a[156:159], v[232:235]\n"
"s_xor_b32 s38, s52, 1245184\n"
"v_cvt_pk_bf16_f32 v218, v220, v221\n"
"v_cvt_pk_bf16_f32 v219, v222, v223\n"
"v_mfma_f32_16x16x32_bf16 v[240:243], a[228:231], a[160:163], v[240:243]\n"
"buffer_store_dwordx4 v[216:219], v1, s[20:23], s38 offen offset:1024 sc1\n"
"v_mfma_f32_16x16x32_bf16 v[240:243], a[224:227], a[132:135], v[240:243]\n"
"v_cvt_pk_bf16_f32 v208, v208, v209\n"
"v_cvt_pk_bf16_f32 v209, v210, v211\n"
"v_mfma_f32_16x16x32_bf16 v[248:251], a[224:227], a[136:139], v[248:251]\n"
"v_cvt_pk_bf16_f32 v210, v212, v213\n"
"v_cvt_pk_bf16_f32 v211, v214, v215\n"
"v_mfma_f32_16x16x32_bf16 v[248:251], a[228:231], a[164:167], v[248:251]\n"
"buffer_store_dwordx4 v[208:211], v1, s[20:23], s38 offen offset:2048 sc1\n"
"v_mfma_f32_16x16x32_bf16 v[252:255], a[236:239], a[164:167], v[252:255]\n"
"v_cvt_pk_bf16_f32 v200, v200, v201\n"
"v_cvt_pk_bf16_f32 v201, v202, v203\n"
"v_mfma_f32_16x16x32_bf16 v[252:255], a[232:235], a[136:139], v[252:255]\n"
"v_cvt_pk_bf16_f32 v202, v204, v205\n"
"v_cvt_pk_bf16_f32 v203, v206, v207\n"
"v_mfma_f32_16x16x32_bf16 v[244:247], a[232:235], a[132:135], v[244:247]\n"
"buffer_store_dwordx4 v[200:203], v1, s[20:23], s38 offen offset:3072 sc1\n"
"v_mfma_f32_16x16x32_bf16 v[244:247], a[236:239], a[160:163], v[244:247]\n"
"v_mfma_f32_16x16x32_bf16 v[236:239], a[236:239], a[156:159], v[236:239]\n"
"v_mfma_f32_16x16x32_bf16 v[236:239], a[232:235], a[128:131], v[236:239]\n"
"v_mfma_f32_16x16x32_bf16 v[232:235], a[240:243], a[184:187], v[232:235]\n"
"v_mfma_f32_16x16x32_bf16 v[232:235], a[244:247], a[212:215], v[232:235]\n"
"v_mfma_f32_16x16x32_bf16 v[240:243], a[244:247], a[216:219], v[240:243]\n"
"v_mfma_f32_16x16x32_bf16 v[240:243], a[240:243], a[188:191], v[240:243]\n"
"v_mfma_f32_16x16x32_bf16 v[248:251], a[240:243], a[192:195], v[248:251]\n"
"v_mfma_f32_16x16x32_bf16 v[248:251], a[244:247], a[220:223], v[248:251]\n"
"v_mfma_f32_16x16x32_bf16 v[252:255], a[252:255], a[220:223], v[252:255]\n"
"v_mfma_f32_16x16x32_bf16 v[252:255], a[248:251], a[192:195], v[252:255]\n"
"v_mfma_f32_16x16x32_bf16 v[244:247], a[248:251], a[188:191], v[244:247]\n"
"v_mfma_f32_16x16x32_bf16 v[244:247], a[252:255], a[216:219], v[244:247]\n"
"v_mfma_f32_16x16x32_bf16 v[236:239], a[252:255], a[212:215], v[236:239]\n"
"s_waitcnt lgkmcnt(11)\n"
"v_mfma_f32_16x16x32_bf16 v[236:239], a[248:251], a[184:187], v[236:239]\n"
"v_mfma_f32_16x16x32_bf16 v[232:235], v[144:147], a[16:19], v[232:235]\n"
"s_waitcnt lgkmcnt(9)\n"
"v_mfma_f32_16x16x32_bf16 v[232:235], v[148:151], a[44:47], v[232:235]\n"
"v_mfma_f32_16x16x32_bf16 v[240:243], v[148:151], a[48:51], v[240:243]\n"
"s_waitcnt lgkmcnt(7)\n"
"v_mfma_f32_16x16x32_bf16 v[240:243], v[144:147], a[20:23], v[240:243]\n"
"v_mfma_f32_16x16x32_bf16 v[248:251], v[144:147], a[24:27], v[248:251]\n"
"s_waitcnt lgkmcnt(6)\n"
"v_mfma_f32_16x16x32_bf16 v[248:251], v[148:151], a[52:55], v[248:251]\n"
"v_mfma_f32_16x16x32_bf16 v[252:255], v[156:159], a[52:55], v[252:255]\n"
"v_mfma_f32_16x16x32_bf16 v[252:255], v[152:155], a[24:27], v[252:255]\n"
"v_mfma_f32_16x16x32_bf16 v[244:247], v[152:155], a[20:23], v[244:247]\n"
"v_mfma_f32_16x16x32_bf16 v[244:247], v[156:159], a[48:51], v[244:247]\n"
"v_mfma_f32_16x16x32_bf16 v[236:239], v[156:159], a[44:47], v[236:239]\n"
"s_waitcnt lgkmcnt(5)\n"
"v_mfma_f32_16x16x32_bf16 v[236:239], v[152:155], a[16:19], v[236:239]\n"
"v_mfma_f32_16x16x32_bf16 v[232:235], v[160:163], a[72:75], v[232:235]\n"
"s_waitcnt lgkmcnt(3)\n"
"v_mfma_f32_16x16x32_bf16 v[232:235], v[164:167], a[100:103], v[232:235]\n"
"v_mfma_f32_16x16x32_bf16 v[240:243], v[164:167], a[104:107], v[240:243]\n"
"s_waitcnt lgkmcnt(1)\n"
"v_mfma_f32_16x16x32_bf16 v[240:243], v[160:163], a[76:79], v[240:243]\n"
"v_mfma_f32_16x16x32_bf16 v[248:251], v[160:163], a[80:83], v[248:251]\n"
"s_waitcnt lgkmcnt(0)\n"
"v_mfma_f32_16x16x32_bf16 v[248:251], v[164:167], a[108:111], v[248:251]\n"
"v_mfma_f32_16x16x32_bf16 v[252:255], v[172:175], a[108:111], v[252:255]\n"
"v_mfma_f32_16x16x32_bf16 v[252:255], v[168:171], a[80:83], v[252:255]\n"
"v_mfma_f32_16x16x32_bf16 v[244:247], v[168:171], a[76:79], v[244:247]\n"
"v_mfma_f32_16x16x32_bf16 v[244:247], v[172:175], a[104:107], v[244:247]\n"
"v_mfma_f32_16x16x32_bf16 v[236:239], v[172:175], a[100:103], v[236:239]\n"
"v_mfma_f32_16x16x32_bf16 v[236:239], v[168:171], a[72:75], v[236:239]\n"
"v_cvt_pk_bf16_f32 v232, v232, v233\n"
"v_cvt_pk_bf16_f32 v233, v234, v235\n"
"s_nop 5\n"
"v_cvt_pk_bf16_f32 v234, v236, v237\n"
"v_cvt_pk_bf16_f32 v235, v238, v239\n"
"v_cvt_pk_bf16_f32 v240, v240, v241\n"
"v_cvt_pk_bf16_f32 v241, v242, v243\n"
"v_cvt_pk_bf16_f32 v242, v244, v245\n"
"v_cvt_pk_bf16_f32 v243, v246, v247\n"
"v_cvt_pk_bf16_f32 v248, v248, v249\n"
"v_cvt_pk_bf16_f32 v249, v250, v251\n"
"v_cvt_pk_bf16_f32 v250, v252, v253\n"
"v_cvt_pk_bf16_f32 v251, v254, v255\n"
"v_mov_b32 %[tid], 0\n"
"v_mfma_f32_16x16x32_bf16 v[144:147], v[24:27], v[224:227], 0\n"
"v_mfma_f32_16x16x32_bf16 v[148:151], v[28:31], v[224:227], 0\n"
"v_mfma_f32_16x16x32_bf16 v[152:155], v[24:27], v[232:235], 0\n"
"v_mfma_f32_16x16x32_bf16 v[156:159], v[28:31], v[232:235], 0\n"
"v_mfma_f32_16x16x32_bf16 v[160:163], v[24:27], v[240:243], 0\n"
"v_mfma_f32_16x16x32_bf16 v[164:167], v[28:31], v[240:243], 0\n"
"v_mfma_f32_16x16x32_bf16 v[168:171], v[24:27], v[248:251], 0\n"
"v_mfma_f32_16x16x32_bf16 v[172:175], v[28:31], v[248:251], 0\n"
"s_xor_b32 s61, s59, 524288\n"
"buffer_load_dwordx2 v[16:17], v5, s[20:23], s61 offen offset:640 sc1\n"
"v_mfma_f32_16x16x32_bf16 v[144:147], v[24:27], v[32:35], v[144:147]\n"
"v_mfma_f32_16x16x32_bf16 v[148:151], v[28:31], v[32:35], v[148:151]\n"
"v_mfma_f32_16x16x32_bf16 v[152:155], v[24:27], v[36:39], v[152:155]\n"
"v_mfma_f32_16x16x32_bf16 v[156:159], v[28:31], v[36:39], v[156:159]\n"
"v_mfma_f32_16x16x32_bf16 v[160:163], v[24:27], v[40:43], v[160:163]\n"
"v_mfma_f32_16x16x32_bf16 v[164:167], v[28:31], v[40:43], v[164:167]\n"
"v_mfma_f32_16x16x32_bf16 v[168:171], v[24:27], v[44:47], v[168:171]\n"
"v_mfma_f32_16x16x32_bf16 v[172:175], v[28:31], v[44:47], v[172:175]\n"
"v_mfma_f32_16x16x32_bf16 v[144:147], v[24:27], v[48:51], v[144:147]\n"
"v_mfma_f32_16x16x32_bf16 v[148:151], v[28:31], v[48:51], v[148:151]\n"
"v_mfma_f32_16x16x32_bf16 v[152:155], v[24:27], v[52:55], v[152:155]\n"
"v_mfma_f32_16x16x32_bf16 v[156:159], v[28:31], v[52:55], v[156:159]\n"
"v_mfma_f32_16x16x32_bf16 v[160:163], v[24:27], v[56:59], v[160:163]\n"
"v_mfma_f32_16x16x32_bf16 v[164:167], v[28:31], v[56:59], v[164:167]\n"
"v_mfma_f32_16x16x32_bf16 v[168:171], v[24:27], v[60:63], v[168:171]\n"
"v_mfma_f32_16x16x32_bf16 v[172:175], v[28:31], v[60:63], v[172:175]\n"
"s_waitcnt vmcnt(1)\n"
"buffer_store_dwordx2 v[8:9], v19, s[20:23], s60 offen offset:768 sc1\n"
"v_mfma_f32_16x16x32_bf16 v[144:147], v[24:27], v[64:67], v[144:147]\n"
"v_mfma_f32_16x16x32_bf16 v[148:151], v[28:31], v[64:67], v[148:151]\n"
"v_mfma_f32_16x16x32_bf16 v[152:155], v[24:27], v[68:71], v[152:155]\n"
"v_mfma_f32_16x16x32_bf16 v[156:159], v[28:31], v[68:71], v[156:159]\n"
"v_mfma_f32_16x16x32_bf16 v[160:163], v[24:27], v[72:75], v[160:163]\n"
"v_mfma_f32_16x16x32_bf16 v[164:167], v[28:31], v[72:75], v[164:167]\n"
"v_mfma_f32_16x16x32_bf16 v[168:171], v[24:27], v[76:79], v[168:171]\n"
"v_mfma_f32_16x16x32_bf16 v[172:175], v[28:31], v[76:79], v[172:175]\n"
"v_mfma_f32_16x16x32_bf16 v[144:147], v[24:27], v[80:83], v[144:147]\n"
"v_mfma_f32_16x16x32_bf16 v[148:151], v[28:31], v[80:83], v[148:151]\n"
"v_mfma_f32_16x16x32_bf16 v[152:155], v[24:27], v[84:87], v[152:155]\n"
"v_mfma_f32_16x16x32_bf16 v[156:159], v[28:31], v[84:87], v[156:159]\n"
"v_mfma_f32_16x16x32_bf16 v[160:163], v[24:27], v[88:91], v[160:163]\n"
"v_mfma_f32_16x16x32_bf16 v[164:167], v[28:31], v[88:91], v[164:167]\n"
"v_mfma_f32_16x16x32_bf16 v[168:171], v[24:27], v[92:95], v[168:171]\n"
"v_mfma_f32_16x16x32_bf16 v[172:175], v[28:31], v[92:95], v[172:175]\n"
"s_waitcnt vmcnt(1)\n"
"v_cmp_ne_u64 vcc, s[68:69], v[16:17]\n"
"s_cbranch_vccnz 45f\n"
"55:\n"
"s_xor_b32 s76, s55, 1048576\n"
"buffer_load_dwordx4 v[96:99], v1, s[20:23], s76 offen sc1\n"
"s_xor_b32 s76, s52, 1048576\n"
"buffer_load_dwordx4 v[100:103], v1, s[20:23], s76 offen offset:1024 sc1\n"
"buffer_load_dwordx4 v[104:107], v1, s[20:23], s76 offen offset:2048 sc1\n"
"buffer_load_dwordx4 v[108:111], v1, s[20:23], s76 offen offset:3072 sc1\n"
"s_xor_b32 s76, s55, 1052672\n"
"buffer_load_dwordx4 v[112:115], v10, s[20:23], s76 offen sc1\n"
"s_xor_b32 s76, s52, 1052672\n"
"buffer_load_dwordx4 v[116:119], v10, s[20:23], s76 offen offset:1024 sc1\n"
"buffer_load_dwordx4 v[120:123], v10, s[20:23], s76 offen offset:2048 sc1\n"
"buffer_load_dwordx4 v[124:127], v10, s[20:23], s76 offen offset:3072 sc1\n"
"s_xor_b32 s61, s59, 0\n"
"buffer_load_dwordx2 v[16:17], v5, s[20:23], s61 offen offset:768 sc1\n"
"s_waitcnt vmcnt(8)\n"
"v_mfma_f32_16x16x32_bf16 v[144:147], v[24:27], v[96:99], v[144:147]\n"
"v_mfma_f32_16x16x32_bf16 v[148:151], v[28:31], v[96:99], v[148:151]\n"
"s_waitcnt vmcnt(7)\n"
"v_mfma_f32_16x16x32_bf16 v[152:155], v[24:27], v[100:103], v[152:155]\n"
"v_mfma_f32_16x16x32_bf16 v[156:159], v[28:31], v[100:103], v[156:159]\n"
"s_waitcnt vmcnt(6)\n"
"v_mfma_f32_16x16x32_bf16 v[160:163], v[24:27], v[104:107], v[160:163]\n"
"v_mfma_f32_16x16x32_bf16 v[164:167], v[28:31], v[104:107], v[164:167]\n"
"s_waitcnt vmcnt(5)\n"
"v_mfma_f32_16x16x32_bf16 v[168:171], v[24:27], v[108:111], v[168:171]\n"
"v_mfma_f32_16x16x32_bf16 v[172:175], v[28:31], v[108:111], v[172:175]\n"
"s_waitcnt vmcnt(4)\n"
"v_mfma_f32_16x16x32_bf16 v[144:147], v[24:27], v[112:115], v[144:147]\n"
"v_mfma_f32_16x16x32_bf16 v[148:151], v[28:31], v[112:115], v[148:151]\n"
"s_waitcnt vmcnt(3)\n"
"v_mfma_f32_16x16x32_bf16 v[152:155], v[24:27], v[116:119], v[152:155]\n"
"v_mfma_f32_16x16x32_bf16 v[156:159], v[28:31], v[116:119], v[156:159]\n"
"s_waitcnt vmcnt(2)\n"
"v_mfma_f32_16x16x32_bf16 v[160:163], v[24:27], v[120:123], v[160:163]\n"
"v_mfma_f32_16x16x32_bf16 v[164:167], v[28:31], v[120:123], v[164:167]\n"
"s_waitcnt vmcnt(1)\n"
"v_mfma_f32_16x16x32_bf16 v[168:171], v[24:27], v[124:127], v[168:171]\n"
"v_mfma_f32_16x16x32_bf16 v[172:175], v[28:31], v[124:127], v[172:175]\n"
"s_waitcnt vmcnt(0)\n"
"v_cmp_ne_u64 vcc, s[68:69], v[16:17]\n"
"s_cbranch_vccnz 46f\n"
"56:\n"
"s_xor_b32 s76, s55, 1118208\n"
"buffer_load_dwordx4 v[128:131], v10, s[20:23], s76 offen sc1\n"
"s_xor_b32 s76, s52, 1118208\n"
"buffer_load_dwordx4 v[132:135], v10, s[20:23], s76 offen offset:1024 sc1\n"
"buffer_load_dwordx4 v[136:139], v10, s[20:23], s76 offen offset:2048 sc1\n"
"buffer_load_dwordx4 v[140:143], v10, s[20:23], s76 offen offset:3072 sc1\n"
"buffer_store_dwordx2 v[8:9], v19, s[20:23], s62 offen sc1\n"
"s_waitcnt vmcnt(4)\n"
"v_mfma_f32_16x16x32_bf16 v[144:147], v[24:27], v[128:131], v[144:147]\n"
"v_mfma_f32_16x16x32_bf16 v[148:151], v[28:31], v[128:131], v[148:151]\n"
"s_waitcnt vmcnt(3)\n"
"v_mfma_f32_16x16x32_bf16 v[152:155], v[24:27], v[132:135], v[152:155]\n"
"v_mfma_f32_16x16x32_bf16 v[156:159], v[28:31], v[132:135], v[156:159]\n"
"s_waitcnt vmcnt(2)\n"
"v_mfma_f32_16x16x32_bf16 v[160:163], v[24:27], v[136:139], v[160:163]\n"
"v_mfma_f32_16x16x32_bf16 v[164:167], v[28:31], v[136:139], v[164:167]\n"
"s_waitcnt vmcnt(1)\n"
"v_mfma_f32_16x16x32_bf16 v[168:171], v[24:27], v[140:143], v[168:171]\n"
"v_mfma_f32_16x16x32_bf16 v[172:175], v[28:31], v[140:143], v[172:175]\n"
"v_fma_f32 %[tid], v151, 0, %[tid]\n"
"v_cvt_pk_bf16_f32 v144, v144, v145\n"
"v_cvt_pk_bf16_f32 v145, v146, v147\n"
"v_cvt_pk_bf16_f32 v146, v148, v149\n"
"v_cvt_pk_bf16_f32 v147, v150, v151\n"
"buffer_store_dwordx4 v[144:147], v13, s[28:31], s57 offen\n"
"v_fma_f32 %[tid], v159, 0, %[tid]\n"
"v_cvt_pk_bf16_f32 v152, v152, v153\n"
"v_cvt_pk_bf16_f32 v153, v154, v155\n"
"v_cvt_pk_bf16_f32 v154, v156, v157\n"
"v_cvt_pk_bf16_f32 v155, v158, v159\n"
"s_add_u32 s56, s56, s58\n"
"buffer_store_dwordx4 v[152:155], v13, s[28:31], s56 offen\n"
"v_fma_f32 %[tid], v167, 0, %[tid]\n"
"v_cvt_pk_bf16_f32 v160, v160, v161\n"
"v_cvt_pk_bf16_f32 v161, v162, v163\n"
"v_cvt_pk_bf16_f32 v162, v164, v165\n"
"v_cvt_pk_bf16_f32 v163, v166, v167\n"
"s_add_u32 s56, s56, s58\n"
"buffer_store_dwordx4 v[160:163], v13, s[28:31], s56 offen\n"
"v_fma_f32 %[tid], v175, 0, %[tid]\n"
"v_cvt_pk_bf16_f32 v168, v168, v169\n"
"v_cvt_pk_bf16_f32 v169, v170, v171\n"
"v_cvt_pk_bf16_f32 v170, v172, v173\n"
"v_cvt_pk_bf16_f32 v171, v174, v175\n"
"s_add_u32 s56, s56, s58\n"
"buffer_store_dwordx4 v[168:171], v13, s[28:31], s56 offen\n"
"s_lshr_b32 %[ow], s46, 13\n"
"s_branch 30f\n"
"61:\n"
"s_sleep 2\n"
"buffer_load_dwordx2 v[16:17], off, s[20:23], s61 offset:128 sc1\n"
"s_waitcnt vmcnt(0)\n"
"v_cmp_ne_u64 vcc, s[68:69], v[16:17]\n"
"s_cbranch_vccz 71b\n"
"s_sub_u32 s77, s77, 1\n"
"s_cbranch_scc0 61b\n"
"s_mov_b32 s77, 0\n"
"s_branch 71b\n"
"81:\n"
"s_sleep 2\n"
"buffer_load_dwordx2 v[16:17], off, s[20:23], s61 offset:128 sc1\n"
"s_waitcnt vmcnt(0)\n"
"v_cmp_ne_u64 vcc, s[68:69], v[16:17]\n"
"s_cbranch_vccz 91b\n"
"s_sub_u32 s77, s77, 1\n"
"s_cbranch_scc0 81b\n"
"s_mov_b32 s77, 0\n"
"s_branch 91b\n"
"42:\n"
"s_sleep 2\n"
"buffer_load_dwordx2 v[16:17], off, s[20:23], s61 offset:256 sc1\n"
"s_waitcnt vmcnt(0)\n"
"v_cmp_ne_u64 vcc, s[68:69], v[16:17]\n"
"s_cbranch_vccz 52b\n"
"s_sub_u32 s77, s77, 1\n"
"s_cbranch_scc0 42b\n"
"s_mov_b32 s77, 0\n"
"s_branch 52b\n"
"43:\n"
"s_sleep 2\n"
"buffer_load_dwordx2 v[16:17], v5, s[20:23], s61 offen offset:384 sc1\n"
"s_waitcnt vmcnt(0)\n"
"v_cmp_ne_u64 vcc, s[68:69], v[16:17]\n"
"s_cbranch_vccz 53b\n"
"s_sub_u32 s77, s77, 1\n"
"s_cbranch_scc0 43b\n"
"s_mov_b32 s77, 0\n"
"s_branch 53b\n"
"44:\n"
"s_sleep 2\n"
"buffer_load_dwordx2 v[16:17], v5, s[20:23], s61 offen offset:512 sc1\n"
"s_waitcnt vmcnt(0)\n"
"v_cmp_ne_u64 vcc, s[68:69], v[16:17]\n"
"s_cbranch_vccz 54b\n"
"s_sub_u32 s77, s77, 1\n"
"s_cbranch_scc0 44b\n"
"s_mov_b32 s77, 0\n"
"s_branch 54b\n"
"45:\n"
"s_sleep 2\n"
"buffer_load_dwordx2 v[16:17], v5, s[20:23], s61 offen offset:640 sc1\n"
"s_waitcnt vmcnt(0)\n"
"v_cmp_ne_u64 vcc, s[68:69], v[16:17]\n"
"s_cbranch_vccz 55b\n"
"s_sub_u32 s77, s77, 1\n"
"s_cbranch_scc0 45b\n"
"s_mov_b32 s77, 0\n"
"s_branch 55b\n"
"46:\n"
"s_sleep 2\n"
"buffer_load_dwordx2 v[16:17], v5, s[20:23], s61 offen offset:768 sc1\n"
"s_waitcnt vmcnt(0)\n"
"v_cmp_ne_u64 vcc, s[68:69], v[16:17]\n"
"s_cbranch_vccz 56b\n"
"s_sub_u32 s77, s77, 1\n"
"s_cbranch_scc0 46b\n"
"s_mov_b32 s77, 0\n"
"s_branch 56b\n"
"97:\n"
"s_sleep 2\n"
"buffer_load_dwordx2 v[16:17], v1, s[20:23], s67 offen sc1\n"
"s_waitcnt vmcnt(0)\n"
"v_cmp_eq_u64 vcc, s[68:69], v[16:17]\n"
"v_cmp_eq_u64 s[70:71], s[42:43], v[16:17]\n"
"s_nop 1\n"
"s_nor_b64 vcc, vcc, s[70:71]\n"
"s_cbranch_scc0 98b\n"
"s_sub_u32 s77, s77, 1\n"
"s_cbranch_scc0 97b\n"
"s_mov_b32 s77, 0\n"
"s_branch 98b\n"
"30:\n"
  : [tid]"+v"(tid),[ka]"+s"(ka),[bid]"+s"(bid),[ow]"=&s"(w)
  : [lds]"s"((unsigned)(size_t)smem)
  : "v1","v2","v3","v4","v5","v6","v7","v8","v9","v10","v11","v12","v13","v14","v15","v16","v17","v18","v19","v20","v21","v22","v23","v24","v25","v26","v27","v28","v29","v30","v31","v32","v33","v34","v35","v36","v37","v38","v39","v40","v41","v42","v43","v44","v45","v46","v47","v48","v49","v50","v51","v52","v53","v54","v55","v56","v57","v58","v59","v60","v61","v62","v63","v64","v65","v66","v67","v68","v69","v70","v71","v72","v73","v74","v75","v76","v77","v78","v79","v80","v81","v82","v83","v84","v85","v86","v87","v88","v89","v90","v91","v92","v93","v94","v95","v96","v97","v98","v99","v100","v101","v102","v103","v104","v105","v106","v107","v108","v109","v110","v111","v112","v113","v114","v115","v116","v117","v118","v119","v120","v121","v122","v123","v124","v125","v126","v127","v128","v129","v130","v131","v132","v133","v134","v135","v136","v137","v138","v139","v140","v141","v142","v143","v144","v145","v146","v147","v148","v149","v150","v151","v152","v153","v154","v155","v156","v157","v158","v159","v160","v161","v162","v163","v164","v165","v166","v167","v168","v169","v170","v171","v172","v173","v174","v175","v176","v177","v178","v179","v180","v181","v182","v183","v184","v185","v186","v187","v188","v189","v190","v191","v192","v193","v194","v195","v196","v197","v198","v199","v200","v201","v202","v203","v204","v205","v206","v207","v208","v209","v210","v211","v212","v213","v214","v215","v216","v217","v218","v219","v220","v221","v222","v223","v224","v225","v226","v227","v228","v229","v230","v231","v232","v233","v234","v235","v236","v237","v238","v239","v240","v241","v242","v243","v244","v245","v246","v247","v248","v249","v250","v251","v252","v253","v254","v255","a0","a1","a2","a3","a4","a5","a6","a7","a8","a9","a10","a11","a12","a13","a14","a15","a16","a17","a18","a19","a20","a21","a22","a23","a24","a25","a26","a27","a28","a29","a30","a31","a32","a33","a34","a35","a36","a37","a38","a39","a40","a41","a42","a43","a44","a45","a46","a47","a48","a49","a50","a51","a52","a53","a54","a55","a56","a57","a58","a59","a60","a61","a62","a63","a64","a65","a66","a67","a68","a69","a70","a71","a72","a73","a74","a75","a76","a77","a78","a79","a80","a81","a82","a83","a84","a85","a86","a87","a88","a89","a90","a91","a92","a93","a94","a95","a96","a97","a98","a99","a100","a101","a102","a103","a104","a105","a106","a107","a108","a109","a110","a111","a112","a113","a114","a115","a116","a117","a118","a119","a120","a121","a122","a123","a124","a125","a126","a127","a128","a129","a130","a131","a132","a133","a134","a135","a136","a137","a138","a139","a140","a141","a142","a143","a144","a145","a146","a147","a148","a149","a150","a151","a152","a153","a154","a155","a156","a157","a158","a159","a160","a161","a162","a163","a164","a165","a166","a167","a168","a169","a170","a171","a172","a173","a174","a175","a176","a177","a178","a179","a180","a181","a182","a183","a184","a185","a186","a187","a188","a189","a190","a191","a192","a193","a194","a195","a196","a197","a198","a199","a200","a201","a202","a203","a204","a205","a206","a207","a208","a209","a210","a211","a212","a213","a214","a215","a216","a217","a218","a219","a220","a221","a222","a223","a224","a225","a226","a227","a228","a229","a230","a231","a232","a233","a234","a235","a236","a237","a238","a239","a240","a241","a242","a243","a244","a245","a246","a247","a248","a249","a250","a251","a252","a253","a254","a255","s8","s9","s10","s11","s12","s13","s14","s15","s16","s17","s18","s19","s20","s21","s22","s23","s24","s25","s26","s27","s28","s29","s30","s31","s36","s37","s38","s39","s40","s41","s42","s43","s44","s45","s46","s47","s48","s49","s50","s51","s52","s53","s54","s55","s56","s57","s58","s59","s60","s61","s62","s63","s64","s65","s66","s67","s68","s69","s70","s71","s72","s73","s74","s75","s76","s77","s78","s79","s80","s81","s82","s83","s84","s85","s86","s87","s88","s89","s90","s91","s92","s93","s94","s95","s96","s97","s98","s99","vcc","scc","memory");

  asm volatile("s_waitcnt vmcnt(0)\n s_barrier":::"memory");
  if(__builtin_amdgcn_ballot_w64(tid!=0u&&tid!=0x80000000u)!=0ull){
    typedef const __attribute__((address_space(4))) unsigned long long* kp;
    kp a=(kp)ka;
    const unsigned gam=bid&7u, idx=bid>>3;
    const unsigned hp=((gam&3u)<<2)|(idx&3u), b=((gam>>2)<<3)|(idx>>2);
    const unsigned kap=hp&1u, hq=hp>>1, th=hq&1u, pown=hq>>1;
    if(!(th&&w==0u))
      exact_tile((const char*)a[0],(const char*)a[1],(const char*)a[2],(const char*)a[3],(const char*)a[4],(char*)a[5],(float*)(smem+w*40960),b,b*224+kap*112+16*(th?3+w:3-w),pown*128,__builtin_amdgcn_mbcnt_hi(~0u,__builtin_amdgcn_mbcnt_lo(~0u,0u)));
  }

}

extern "C" void kernel_launch(void* const* d_in, const int* in_sizes, int n_in, void* d_out, int out_size, void* d_ws, size_t ws_size, hipStream_t stream){
  gqa_fused<<<256,256,0,stream>>>((const char*)d_in[0],(const char*)d_in[1],(const char*)d_in[2],(const char*)d_in[3],(const char*)d_in[4],(char*)d_out,(char*)d_ws);
}
```
